```python
import torch

def _softmax(x, axis=-1):
    return torch.softmax(x, dim=axis)
def _gelu(x):
    return torch.nn.functional.gelu(x, approximate="tanh")
def _silu(x):
    return torch.nn.functional.silu(x)
def _sigmoid(x):
    return torch.sigmoid(x)
def _layernorm(x, axis=-1, eps=1e-5):
    m = x.mean(dim=axis, keepdim=True)
    v = x.var(dim=axis, keepdim=True, unbiased=False)
    return (x - m) / torch.sqrt(v + eps)
def _scatter_add(tgt, idx, src, axis=0):
    return tgt.clone().index_add_(axis, idx, src)
def _segment_sum(src, seg, n):
    out = torch.zeros((n,) + src.shape[1:], dtype=src.dtype)
    return out.index_add_(0, seg, src)
def _pad(x, pw):
    flat = tuple(v for pair in reversed(pw) for v in pair)
    return torch.nn.functional.pad(x, flat)

kernel_name = 'parallel_sum__sliding_block__glu_gated_bilinear__prompt'

def setup_inputs():

    _w = (lambda a: (lambda t: t.to(torch.bfloat16) if t.is_floating_point() else t)(torch.as_tensor(a)))
    def _A_setup():
        B, S, D = 1, 2048, 1024
        return {
            "x": _w((lambda *s: torch.randn(*s, dtype=torch.bfloat16) * 0.05)(B, S, D)),
            "wqkv": _w((lambda *s: torch.randn(*s, dtype=torch.bfloat16) * 0.05)(D, 3 * D)),
            "wo": _w((lambda *s: torch.randn(*s, dtype=torch.bfloat16) * 0.05)(D, D)),
        }
    _A_in = _A_setup()

    def _B_setup():
        B, S, D, F = 1, 2048, 1024, 3584
        return {
            "x": _w((lambda *s: torch.randn(*s, dtype=torch.bfloat16) * 0.05)(B, S, D)),
            "w_gate": _w((lambda *s: torch.randn(*s, dtype=torch.bfloat16) * 0.8)(D, F) * (D ** -0.5)),
            "w_up": _w((lambda *s: torch.randn(*s, dtype=torch.bfloat16) * 0.8)(D, F) * (D ** -0.5)),
            "w_down": _w((lambda *s: torch.randn(*s, dtype=torch.bfloat16) * 0.8)(F, D) * (F ** -0.5)),
        }
    _B_in = _B_setup()

    return {
        "A_x": _A_in["x"],
        "A_wqkv": _A_in["wqkv"],
        "A_wo": _A_in["wo"],
        "B_x": _B_in["x"],
        "B_w_gate": _B_in["w_gate"],
        "B_w_up": _B_in["w_up"],
        "B_w_down": _B_in["w_down"],
    }

def reference(A_x, A_wqkv, A_wo, B_x, B_w_gate, B_w_up, B_w_down):
    def _A_ref(x, wqkv, wo):
        B, S, D = x.shape
        H = 32; Dh = D // H; W = 256
        qkv = (lambda x, *d: x.permute(*d))((x @ wqkv).reshape(B, S, 3, H, Dh), 2, 0, 3, 1, 4)
        q, k, v = qkv[0], qkv[1], qkv[2]
        scores = torch.einsum("bhqd,bhkd->bhqk", q, k) * (Dh ** -0.5)
        idx = torch.arange(S)
        mask = (idx[:, None] - idx[None, :] >= 0) & (idx[:, None] - idx[None, :] < W)
        scores = torch.where(mask[None, None], scores, -1e9)
        p = _softmax(scores, axis=-1)
        o = torch.einsum("bhqk,bhkd->bhqd", p, v)
        return (lambda x, *d: x.permute(*d))(o, 0, 2, 1, 3).reshape(B, S, D) @ wo

    def _B_ref(x, w_gate, w_up, w_down):
        g = torch.einsum("bsd,df->bsf", x, w_gate)
        u = torch.einsum("bsd,df->bsf", x, w_up)
        h = g * u
        return torch.einsum("bsf,fd->bsd", h, w_down)

    x = A_x
    xn = _layernorm(x, axis=-1)
    return x + _A_ref(xn, A_wqkv, A_wo) + _B_ref(xn, B_w_gate, B_w_up, B_w_down)
```

```cpp
#include <hip/hip_runtime.h>
#include <hip/hip_bf16.h>
typedef unsigned short u16;
__device__ inline float bf2f(u16 v){ return __uint_as_float(((unsigned)v)<<16); }
__device__ inline u16 f2bf(float f){ unsigned u=__float_as_uint(f); u += 0x7fffu + ((u>>16)&1u); return (u16)(u>>16); }

__global__ void ln_k(const u16* x, u16* xn){
  int row = blockIdx.x; int t = threadIdx.x;
  __shared__ float red[256];
  float v[4]; float s=0;
  for(int i=0;i<4;i++){ v[i]=bf2f(x[row*1024+t*4+i]); s+=v[i]; }
  red[t]=s; __syncthreads();
  for(int o=128;o>0;o>>=1){ if(t<o) red[t]+=red[t+o]; __syncthreads(); }
  float m=red[0]/1024.f; __syncthreads();
  float q=0; for(int i=0;i<4;i++){ float d=v[i]-m; q+=d*d; }
  red[t]=q; __syncthreads();
  for(int o=128;o>0;o>>=1){ if(t<o) red[t]+=red[t+o]; __syncthreads(); }
  float r=1.f/sqrtf(red[0]/1024.f+1e-5f);
  for(int i=0;i<4;i++) xn[row*1024+t*4+i]=f2bf((v[i]-m)*r);
}
__global__ void gemm_k(const u16* A,int lda,const u16* B,int ldb,float* C,int ldc,int K,int accum){
  __shared__ float As[32][65], Bs[32][65];
  int tx=threadIdx.x&15, ty=threadIdx.x>>4;
  int r0=blockIdx.y*64, c0=blockIdx.x*64;
  float acc[4][4]={};
  for(int k0=0;k0<K;k0+=32){
    for(int i=threadIdx.x;i<64*32;i+=256){ int r=i>>5,k=i&31; As[k][r]=bf2f(A[(size_t)(r0+r)*lda+k0+k]); }
    for(int i=threadIdx.x;i<64*32;i+=256){ int k=i>>6,c=i&63; Bs[k][c]=bf2f(B[(size_t)(k0+k)*ldb+c0+c]); }
    __syncthreads();
    for(int k=0;k<32;k++){
      float a[4],b[4];
      for(int i=0;i<4;i++){ a[i]=As[k][ty*4+i]; b[i]=Bs[k][tx*4+i]; }
      for(int i=0;i<4;i++)for(int j=0;j<4;j++) acc[i][j]+=a[i]*b[j];
    }
    __syncthreads();
  }
  for(int i=0;i<4;i++)for(int j=0;j<4;j++){ size_t o=(size_t)(r0+ty*4+i)*ldc+c0+tx*4+j; C[o]=(accum?C[o]:0.f)+acc[i][j]; }
}
__global__ void cvt_k(const float* a,u16* o,int n){ int i=blockIdx.x*256+threadIdx.x; if(i<n) o[i]=f2bf(a[i]); }
__global__ void glu_k(const float* g,const float* u,u16* h,int n){ int i=blockIdx.x*256+threadIdx.x; if(i<n) h[i]=f2bf(bf2f(f2bf(g[i]))*bf2f(f2bf(u[i]))); }
__global__ void attn_k(const u16* qkv,u16* o){
  int q=blockIdx.x*64+threadIdx.x; int h=blockIdx.y;
  float qv[32]; for(int d=0;d<32;d++) qv[d]=bf2f(qkv[(size_t)q*3072+h*32+d]);
  float m=-1e30f,l=0,acc[32]; for(int d=0;d<32;d++) acc[d]=0;
  int k0=q-255; if(k0<0)k0=0;
  for(int k=k0;k<=q;k++){
    float s=0; for(int d=0;d<32;d++) s+=qv[d]*bf2f(qkv[(size_t)k*3072+1024+h*32+d]);
    s*=0.17677669529663687f;
    float mn=fmaxf(m,s); float f=__expf(m-mn), p=__expf(s-mn);
    l=l*f+p; for(int d=0;d<32;d++) acc[d]=acc[d]*f+p*bf2f(qkv[(size_t)k*3072+2048+h*32+d]);
    m=mn;
  }
  for(int d=0;d<32;d++) o[(size_t)q*1024+h*32+d]=f2bf(acc[d]/l);
}
__global__ void fin_k(const u16* x,const float* c,u16* out,int n){ int i=blockIdx.x*256+threadIdx.x; if(i<n) out[i]=f2bf(bf2f(x[i])+c[i]); }

extern "C" void kernel_launch(void* const* d_in, const int* in_sizes, int n_in, void* d_out, int out_size, void* d_ws, size_t ws_size, hipStream_t stream){
  const u16* x=(const u16*)d_in[0]; const u16* wqkv=(const u16*)d_in[1]; const u16* wo=(const u16*)d_in[2];
  const u16* wg=(const u16*)d_in[4]; const u16* wu=(const u16*)d_in[5]; const u16* wd=(const u16*)d_in[6];
  char* ws=(char*)d_ws;
  u16* xn=(u16*)ws; ws+=2048*1024*2;
  float* c0=(float*)ws; ws+=2048*3584*4;
  float* c1=(float*)ws; ws+=2048*3584*4;
  u16* qkv=(u16*)ws; ws+=2048*3072*2;
  u16* h=(u16*)ws; ws+=2048*3584*2;
  u16* o=(u16*)ws; ws+=2048*1024*2;
  ln_k<<<2048,256,0,stream>>>(x,xn);
  gemm_k<<<dim3(3072/64,32),256,0,stream>>>(xn,1024,wqkv,3072,c0,3072,1024,0);
  cvt_k<<<2048*3072/256,256,0,stream>>>(c0,qkv,2048*3072);
  attn_k<<<dim3(32,32),64,0,stream>>>(qkv,o);
  gemm_k<<<dim3(3584/64,32),256,0,stream>>>(xn,1024,wg,3584,c0,3584,1024,0);
  gemm_k<<<dim3(3584/64,32),256,0,stream>>>(xn,1024,wu,3584,c1,3584,1024,0);
  glu_k<<<2048*3584/256,256,0,stream>>>(c0,c1,h,2048*3584);
  gemm_k<<<dim3(1024/64,32),256,0,stream>>>(o,1024,wo,1024,c0,1024,1024,0);
  gemm_k<<<dim3(1024/64,32),256,0,stream>>>(h,3584,wd,1024,c0,1024,3584,1);
  fin_k<<<2048*1024/256,256,0,stream>>>(x,c0,(u16*)d_out,2048*1024);
}
```

```cpp
#include <hip/hip_runtime.h>
#include <hip/hip_bf16.h>
#define qA "v_mfma_f32_16x16x32_bf16 "
#define qB "v_mfma_f32_32x32x16_bf16 "
#define qC "ds_read_b64_tr_b16 "
#define qD "ds_read_b128 "
#define qE "buffer_load_dwordx4 "
#define qF "buffer_store_dwordx4 "
#define qG "v_accvgpr_read_b32 "
#define qH "v_pk_fma_f32 "
#define qI "v_pk_mul_f32 "
#define qJ "v_cvt_pk_bf16_f32 "
#define qK "v_permlane16_swap_b32 "
#define qL "v_exp_f32 "
#define qM "v_max3_f32 "
#define qN "s_add_u32 "
#define qO "s_waitcnt "
#define qP "v_cndmask_b32 "
#define qQ "ds_write_b128 "
#define qR " op_sel:[1,0,0] op_sel_hi:[1,1,1] neg_lo:[1,0,0] neg_hi:[1,0,0]\n"
#define qS " op_sel:[0,1] op_sel_hi:[1,1]\n"
#define qT " op_sel_hi:[1,0]\n"
#define qU " offen lds\n"
#define qV " offen sc1 lds\n"

#define G1_FIRST \
qE "v[48:51],v47,s[76:79],0 offen offset:0\n" qE "v[52:55],v47,s[76:79],0 offen offset:1024\n" qE "v[56:59],v47,s[76:79],0 offen offset:2048\n" qE "v[60:63],v47,s[76:79],0 offen offset:3072\n" \
"s_getpc_b64 s[92:93]\n" "G1PC_%=:\n" "s_mov_b32 s94,K2_CODE_END - G1PC_%=\n" "s_mov_b32 s95,0x20000\n" \
"s_max_i32 s94,s94,0\n" "s_min_u32 s94,s94,0x20000\n" "s_mov_b32 m0,s73\n" "s_nop 0\n" \
qE "v47,s[92:95],s74" qU "global_load_dword v100,v47,s[80:81] sc1\n" qN "m0,s61,0\n" "s_nop 0\n" \
qE "v40,s[40:43],0" qU qN "m0,s61,1024\n" "s_nop 0\n" qE "v40,s[40:43],s65" qU \
qN "m0,s61,2048\n" "s_nop 0\n" qE "v40,s[40:43],s66" qU qN "m0,s61,3072\n" \
"s_nop 0\n" qE "v40,s[40:43],s67" qU qN "m0,s61,4096\n" "s_nop 0\n" \
qE "v40,s[40:43],s68" qU qN "m0,s61,5120\n" "s_nop 0\n" qE "v40,s[40:43],s69" qU \
qN "m0,s61,6144\n" "s_nop 0\n" qE "v40,s[40:43],s70" qU qN "m0,s61,7168\n" \
"s_nop 0\n" qE "v40,s[40:43],s71" qU qN "s40,s40,128\n" "s_addc_u32 s41,s41,0\n"

#define G1_ALL \
qN "m0,s62,0\n" "s_nop 0\n" qE "v41,s[44:47],s56" qU qN "m0,s62,1984\n" \
"s_nop 0\n" qE "v41,s[44:47],s56 offen offset:64 lds\n" qN "m0,s62,3968\n" "s_nop 0\n" \
qE "v41,s[44:47],s56 offen offset:128 lds\n" qN "m0,s62,6144\n" "s_nop 0\n" qE "v42,s[48:51],s57" qU \
qN "m0,s62,8192\n" "s_nop 0\n" qE "v43,s[52:55],s58" qU qN "s56,s56,229376\n" \
qN "s57,s57,196608\n" qN "s58,s58,s60\n" "s_min_u32 s56,s56,7110656\n" "s_min_u32 s57,s57,6094848\n" \
"s_min_u32 s58,s58,s72\n" qN "m0,s62,20480\n" "s_nop 0\n" qE "v41,s[44:47],s56" qU \
qN "m0,s62,22464\n" "s_nop 0\n" qE "v41,s[44:47],s56 offen offset:64 lds\n" qN "m0,s62,24448\n" \
"s_nop 0\n" qE "v41,s[44:47],s56 offen offset:128 lds\n" qN "m0,s62,26624\n" "s_nop 0\n" \
qE "v42,s[48:51],s57" qU qN "m0,s62,28672\n" "s_nop 0\n" qE "v43,s[52:55],s58" qU \
qN "s56,s56,229376\n" qN "s57,s57,196608\n" qN "s58,s58,s60\n" "s_min_u32 s56,s56,7110656\n" \
"s_min_u32 s57,s57,6094848\n" "s_min_u32 s58,s58,s72\n" qN "m0,s61,32768\n" "s_nop 0\n" \
qE "v40,s[40:43],0" qU qN "m0,s61,33792\n" "s_nop 0\n" qE "v40,s[40:43],s65" qU \
qN "m0,s61,34816\n" "s_nop 0\n" qE "v40,s[40:43],s66" qU qN "m0,s61,35840\n" \
"s_nop 0\n" qE "v40,s[40:43],s67" qU qN "m0,s61,36864\n" "s_nop 0\n" \
qE "v40,s[40:43],s68" qU qN "m0,s61,37888\n" "s_nop 0\n" qE "v40,s[40:43],s69" qU \
qN "m0,s61,38912\n" "s_nop 0\n" qE "v40,s[40:43],s70" qU qN "m0,s61,39936\n" \
"s_nop 0\n" qE "v40,s[40:43],s71" qU qN "s40,s40,128\n" "s_addc_u32 s41,s41,0\n" \
qN "m0,s62,40960\n" "s_nop 0\n" qE "v41,s[44:47],s56" qU qN "m0,s62,42944\n" \
"s_nop 0\n" qE "v41,s[44:47],s56 offen offset:64 lds\n" qN "m0,s62,44928\n" "s_nop 0\n" \
qE "v41,s[44:47],s56 offen offset:128 lds\n" qN "m0,s62,47104\n" "s_nop 0\n" qE "v42,s[48:51],s57" qU \
qN "m0,s62,49152\n" "s_nop 0\n" qE "v43,s[52:55],s58" qU qN "s56,s56,229376\n" \
qN "s57,s57,196608\n" qN "s58,s58,s60\n" "s_min_u32 s56,s56,7110656\n" "s_min_u32 s57,s57,6094848\n" \
"s_min_u32 s58,s58,s72\n" qO "vmcnt(31)\n" "v_mov_b32 v98,0xffff0000\n" "v_lshlrev_b32 v64,16,v48\n" \
"v_and_b32 v65,v98,v48\n" "v_lshlrev_b32 v68,16,v50\n" "v_and_b32 v69,v98,v50\n" "v_lshlrev_b32 v72,16,v52\n" \
"v_and_b32 v73,v98,v52\n" "v_lshlrev_b32 v76,16,v54\n" "v_and_b32 v77,v98,v54\n" "v_lshlrev_b32 v80,16,v56\n" \
"v_and_b32 v81,v98,v56\n" "v_lshlrev_b32 v84,16,v58\n" "v_and_b32 v85,v98,v58\n" "v_lshlrev_b32 v88,16,v60\n" \
"v_and_b32 v89,v98,v60\n" "v_lshlrev_b32 v92,16,v62\n" "v_and_b32 v93,v98,v62\n" "v_lshlrev_b32 v66,16,v49\n" \
"v_lshlrev_b32 v70,16,v51\n" "v_lshlrev_b32 v74,16,v53\n" "v_lshlrev_b32 v78,16,v55\n" "v_add_f32 v104,v64,v65\n" \
"v_add_f32 v105,v68,v69\n" "v_add_f32 v106,v72,v73\n" "v_add_f32 v107,v76,v77\n" "v_lshlrev_b32 v82,16,v57\n" \
"v_lshlrev_b32 v86,16,v59\n" "v_lshlrev_b32 v90,16,v61\n" "v_lshlrev_b32 v94,16,v63\n" "v_add_f32 v110,v80,v81\n" \
"v_add_f32 v111,v84,v85\n" "v_add_f32 v112,v88,v89\n" "v_add_f32 v113,v92,v93\n" "v_and_b32 v67,v98,v49\n" \
"v_and_b32 v71,v98,v51\n" "v_and_b32 v75,v98,v53\n" "v_and_b32 v79,v98,v55\n" "v_add_f32 v104,v104,v66\n" \
"v_add_f32 v105,v105,v70\n" "v_add_f32 v106,v106,v74\n" "v_add_f32 v107,v107,v78\n" "v_and_b32 v83,v98,v57\n" \
"v_and_b32 v87,v98,v59\n" "v_and_b32 v91,v98,v61\n" "v_and_b32 v95,v98,v63\n" "v_add_f32 v110,v110,v82\n" \
"v_add_f32 v111,v111,v86\n" "v_add_f32 v112,v112,v90\n" "v_add_f32 v113,v113,v94\n" "v_add_f32 v104,v104,v67\n" \
"v_add_f32 v105,v105,v71\n" "v_add_f32 v106,v106,v75\n" "v_add_f32 v107,v107,v79\n" "v_add_f32 v110,v110,v83\n" \
"v_add_f32 v111,v111,v87\n" "v_add_f32 v112,v112,v91\n" "v_add_f32 v113,v113,v95\n" "v_add_f32 v104,v104,v105\n" \
"v_add_f32 v106,v106,v107\n" "v_add_f32 v110,v110,v111\n" "v_add_f32 v112,v112,v113\n" "v_add_f32 v104,v104,v106\n" \
"v_add_f32 v110,v110,v112\n" "v_readfirstlane_b32 s75,v100\n" "v_add_f32_dpp v104,v104,v104 quad_perm:[1,0,3,2] row_mask:0xf bank_mask:0xf bound_ctrl:1\n" "v_add_f32_dpp v110,v110,v110 quad_perm:[1,0,3,2] row_mask:0xf bank_mask:0xf bound_ctrl:1\n" \
"s_mov_b64 s[92:93],2\n" "v_mov_b32 v28,0x3f803f80\n" "v_add_f32_dpp v104,v104,v104 quad_perm:[2,3,0,1] row_mask:0xf bank_mask:0xf bound_ctrl:1\n" "v_add_f32_dpp v110,v110,v110 quad_perm:[2,3,0,1] row_mask:0xf bank_mask:0xf bound_ctrl:1\n" \
qN "s75,s75,1\n" "v_mov_b32 v102,s75\n" "v_add_f32_dpp v104,v104,v104 row_half_mirror row_mask:0xf bank_mask:0xf bound_ctrl:1\n" "v_add_f32_dpp v110,v110,v110 row_half_mirror row_mask:0xf bank_mask:0xf bound_ctrl:1\n" \
"v_mov_b32 v120,v102\n" "v_add_f32_dpp v104,v104,v104 row_mirror row_mask:0xf bank_mask:0xf bound_ctrl:1\n" "v_add_f32_dpp v110,v110,v110 row_mirror row_mask:0xf bank_mask:0xf bound_ctrl:1\n" "v_mov_b32 v105,v104\n" \
"v_mov_b32 v111,v110\n" "v_mov_b32 v122,v102\n" qK "v104,v105\n" qK "v110,v111\n" \
"v_add_f32 v104,v104,v105\n" "v_add_f32 v110,v110,v111\n" "v_mov_b32 v105,v104\n" "v_mov_b32 v111,v110\n" \
"v_mov_b32 v29,0x3f803f80\n" "v_permlane32_swap_b32 v104,v105\n" "v_permlane32_swap_b32 v110,v111\n" "v_add_f32 v104,v104,v105\n" \
"v_add_f32 v110,v110,v111\n" "v_mul_f32 v108,0x3a800000,v104\n" "v_mul_f32 v114,0x3a800000,v110\n" "v_sub_f32 v64,v64,v108\n" \
"v_sub_f32 v68,v68,v108\n" "v_sub_f32 v72,v72,v108\n" "v_sub_f32 v76,v76,v108\n" "v_sub_f32 v80,v80,v114\n" \
"v_sub_f32 v84,v84,v114\n" "v_sub_f32 v88,v88,v114\n" "v_sub_f32 v92,v92,v114\n" "v_sub_f32 v65,v65,v108\n" \
"v_sub_f32 v69,v69,v108\n" "v_sub_f32 v73,v73,v108\n" "v_sub_f32 v77,v77,v108\n" "v_mul_f32 v104,v64,v64\n" \
"v_mul_f32 v105,v68,v68\n" "v_mul_f32 v106,v72,v72\n" "v_mul_f32 v107,v76,v76\n" "v_sub_f32 v81,v81,v114\n" \
"v_sub_f32 v85,v85,v114\n" "v_sub_f32 v89,v89,v114\n" "v_sub_f32 v93,v93,v114\n" "v_mul_f32 v110,v80,v80\n" \
"v_mul_f32 v111,v84,v84\n" "v_mul_f32 v112,v88,v88\n" "v_mul_f32 v113,v92,v92\n" "v_sub_f32 v66,v66,v108\n" \
"v_sub_f32 v70,v70,v108\n" "v_sub_f32 v74,v74,v108\n" "v_sub_f32 v78,v78,v108\n" "v_fmac_f32 v104,v65,v65\n" \
"v_fmac_f32 v105,v69,v69\n" "v_fmac_f32 v106,v73,v73\n" "v_fmac_f32 v107,v77,v77\n" "v_sub_f32 v82,v82,v114\n" \
"v_sub_f32 v86,v86,v114\n" "v_sub_f32 v90,v90,v114\n" "v_sub_f32 v94,v94,v114\n" "v_fmac_f32 v110,v81,v81\n" \
"v_fmac_f32 v111,v85,v85\n" "v_fmac_f32 v112,v89,v89\n" "v_fmac_f32 v113,v93,v93\n" "v_sub_f32 v67,v67,v108\n" \
"v_sub_f32 v71,v71,v108\n" "v_sub_f32 v75,v75,v108\n" "v_sub_f32 v79,v79,v108\n" "v_fmac_f32 v104,v66,v66\n" \
"v_fmac_f32 v105,v70,v70\n" "v_fmac_f32 v106,v74,v74\n" "v_fmac_f32 v107,v78,v78\n" "v_sub_f32 v83,v83,v114\n" \
"v_sub_f32 v87,v87,v114\n" "v_sub_f32 v91,v91,v114\n" "v_sub_f32 v95,v95,v114\n" "v_fmac_f32 v110,v82,v82\n" \
"v_fmac_f32 v111,v86,v86\n" "v_fmac_f32 v112,v90,v90\n" "v_fmac_f32 v113,v94,v94\n" "v_fmac_f32 v104,v67,v67\n" \
"v_fmac_f32 v105,v71,v71\n" "v_fmac_f32 v106,v75,v75\n" "v_fmac_f32 v107,v79,v79\n" "v_fmac_f32 v110,v83,v83\n" \
"v_fmac_f32 v111,v87,v87\n" "v_fmac_f32 v112,v91,v91\n" "v_fmac_f32 v113,v95,v95\n" "v_add_f32 v104,v104,v105\n" \
"v_add_f32 v106,v106,v107\n" "v_add_f32 v110,v110,v111\n" "v_add_f32 v112,v112,v113\n" "v_add_f32 v104,v104,v106\n" \
"v_add_f32 v110,v110,v112\n" qP "v121,v108,v114,s[92:93]\n" "v_add_f32_dpp v104,v104,v104 quad_perm:[1,0,3,2] row_mask:0xf bank_mask:0xf bound_ctrl:1\n" "v_add_f32_dpp v110,v110,v110 quad_perm:[1,0,3,2] row_mask:0xf bank_mask:0xf bound_ctrl:1\n" \
"v_mov_b32 v30,0x3f803f80\n" "v_add_f32_dpp v104,v104,v104 quad_perm:[2,3,0,1] row_mask:0xf bank_mask:0xf bound_ctrl:1\n" "v_add_f32_dpp v110,v110,v110 quad_perm:[2,3,0,1] row_mask:0xf bank_mask:0xf bound_ctrl:1\n" "v_mov_b32 v31,0x3f803f80\n" \
"v_add_f32_dpp v104,v104,v104 row_half_mirror row_mask:0xf bank_mask:0xf bound_ctrl:1\n" "v_add_f32_dpp v110,v110,v110 row_half_mirror row_mask:0xf bank_mask:0xf bound_ctrl:1\n" "s_nop 0\n" "v_add_f32_dpp v104,v104,v104 row_mirror row_mask:0xf bank_mask:0xf bound_ctrl:1\n" \
"v_add_f32_dpp v110,v110,v110 row_mirror row_mask:0xf bank_mask:0xf bound_ctrl:1\n" "v_mov_b32 v105,v104\n" "v_mov_b32 v111,v110\n" "s_nop 0\n" \
qK "v104,v105\n" qK "v110,v111\n" "v_add_f32 v104,v104,v105\n" "v_add_f32 v110,v110,v111\n" \
"v_mov_b32 v105,v104\n" "v_mov_b32 v111,v110\n" "s_nop 0\n" "v_permlane32_swap_b32 v104,v105\n" \
"v_permlane32_swap_b32 v110,v111\n" "v_add_f32 v104,v104,v105\n" "v_add_f32 v110,v110,v111\n" "v_mul_f32 v104,0x3a800000,v104\n" \
"v_mul_f32 v110,0x3a800000,v110\n" "v_add_f32 v104,0x3727c5ac,v104\n" "v_add_f32 v110,0x3727c5ac,v110\n" "v_rsq_f32 v109,v104\n" \
"v_rsq_f32 v115,v110\n" "s_nop 0\n" qP "v123,v109,v115,s[92:93]\n" "s_mov_b64 exec,3\n" \
qF "v[120:123],v45,s[84:87],0 offen sc1\n" "s_mov_b64 exec,-1\n" qO "vmcnt(19)\n" "s_barrier\n" \
"s_and_b32 s63,s59,3\n" "s_cmp_lt_u32 s63,2\n" "s_cbranch_scc0 G1W23_%=\n" "s_cmp_eq_u32 s63,0\n" \
"s_cbranch_scc0 G1W1_%=\n" qC "v[48:49],v32 offset:0\n" qC "v[50:51],v32 offset:256\n" qC "v[52:53],v32 offset:10240\n" \
qC "v[54:55],v32 offset:10496\n" qC "v[56:57],v33 offset:0\n" qC "v[58:59],v33 offset:256\n" qC "v[60:61],v33 offset:10240\n" \
qC "v[62:63],v33 offset:10496\n" qC "v[64:65],v32 offset:2048\n" qC "v[66:67],v32 offset:2304\n" qC "v[68:69],v32 offset:12288\n" \
qC "v[70:71],v32 offset:12544\n" qC "v[72:73],v33 offset:2048\n" qC "v[74:75],v33 offset:2304\n" qC "v[76:77],v33 offset:12288\n" \
qC "v[78:79],v33 offset:12544\n" qC "v[80:81],v32 offset:16384\n" qC "v[82:83],v32 offset:16640\n" qC "v[84:85],v33 offset:16384\n" \
qC "v[86:87],v33 offset:16640\n" qD "v[128:131],v38 offset:0\n" qD "v[132:135],v38 offset:2048\n" qD "v[136:139],v38 offset:4096\n" \
qD "v[140:143],v38 offset:6144\n" qD "v[144:147],v38 offset:8192\n" qD "v[148:151],v38 offset:10240\n" qD "v[152:155],v38 offset:12288\n" \
qD "v[156:159],v38 offset:14336\n" "s_mov_b32 s63,7\n" qO "lgkmcnt(0)\n" qA "a[0:3],v[48:51],v[128:131],0\n" \
qO "vmcnt(14)\n" qA "a[4:7],v[52:55],v[128:131],0\n" "s_barrier\n" qA "a[8:11],v[56:59],v[128:131],0\n" \
qC "v[88:89],v32 offset:20480\n" qN "m0,s62,61440\n" qA "a[12:15],v[60:63],v[128:131],0\n" qE "v41,s[44:47],s56" qU \
qA "a[16:19],v[64:67],v[128:131],0\n" qC "v[90:91],v32 offset:20736\n" qA "a[20:23],v[68:71],v[128:131],0\n" qN "m0,s62,63424\n" \
qA "a[24:27],v[72:75],v[128:131],0\n" qC "v[92:93],v32 offset:30720\n" qE "v41,s[44:47],s56 offen offset:64 lds\n" qA "a[28:31],v[76:79],v[128:131],0\n" \
qA "a[32:35],v[80:83],v[128:131],0\n" qC "v[94:95],v32 offset:30976\n" qN "m0,s62,65408\n" qA "a[36:39],v[84:87],v[128:131],0\n" \
qE "v41,s[44:47],s56 offen offset:128 lds\n" qA "v[8:11],v[48:51],v[28:31],0\n" qC "v[96:97],v33 offset:20480\n" qA "a[40:43],v[48:51],v[132:135],0\n" \
qN "m0,s62,67584\n" qA "a[44:47],v[52:55],v[132:135],0\n" qC "v[98:99],v33 offset:20736\n" qE "v42,s[48:51],s57" qU \
qA "a[48:51],v[56:59],v[132:135],0\n" qA "a[52:55],v[60:63],v[132:135],0\n" qC "v[100:101],v33 offset:30720\n" qN "m0,s62,69632\n" \
qA "a[56:59],v[64:67],v[132:135],0\n" qE "v43,s[52:55],s58" qU qA "a[60:63],v[68:71],v[132:135],0\n" qC "v[102:103],v33 offset:30976\n" \
qN "s56,s56,229376\n" qN "s57,s57,196608\n" qN "s58,s58,s60\n" qA "a[64:67],v[72:75],v[132:135],0\n" \
"s_min_u32 s56,s56,7110656\n" "s_min_u32 s57,s57,6094848\n" "s_min_u32 s58,s58,s72\n" qN "m0,s62,0\n" \
qA "a[68:71],v[76:79],v[132:135],0\n" qC "v[104:105],v32 offset:22528\n" qE "v41,s[44:47],s56" qU qA "a[72:75],v[80:83],v[132:135],0\n" \
qA "a[76:79],v[84:87],v[132:135],0\n" qC "v[106:107],v32 offset:22784\n" qN "m0,s62,1984\n" qA "a[80:83],v[48:51],v[136:139],0\n" \
qE "v41,s[44:47],s56 offen offset:64 lds\n" qA "a[84:87],v[52:55],v[136:139],0\n" qC "v[108:109],v32 offset:32768\n" qA "a[88:91],v[56:59],v[136:139],0\n" \
qN "m0,s62,3968\n" qA "a[92:95],v[60:63],v[136:139],0\n" qC "v[110:111],v32 offset:33024\n" qE "v41,s[44:47],s56 offen offset:128 lds\n" \
qA "a[96:99],v[64:67],v[136:139],0\n" qA "a[100:103],v[68:71],v[136:139],0\n" qC "v[112:113],v33 offset:22528\n" qN "m0,s62,6144\n" \
qA "v[12:15],v[52:55],v[28:31],0\n" qE "v42,s[48:51],s57" qU qA "a[104:107],v[72:75],v[136:139],0\n" qC "v[114:115],v33 offset:22784\n" \
qA "a[108:111],v[76:79],v[136:139],0\n" qN "m0,s62,8192\n" qA "a[112:115],v[80:83],v[136:139],0\n" qC "v[116:117],v33 offset:32768\n" \
qE "v43,s[52:55],s58" qU qA "a[116:119],v[84:87],v[136:139],0\n" qA "a[120:123],v[48:51],v[140:143],0\n" qC "v[118:119],v33 offset:33024\n" \
qN "s56,s56,229376\n" qA "a[124:127],v[52:55],v[140:143],0\n" qN "s57,s57,196608\n" qA "a[128:131],v[56:59],v[140:143],0\n" \
qC "v[120:121],v32 offset:36864\n" qN "s58,s58,s60\n" qA "a[132:135],v[60:63],v[140:143],0\n" "s_min_u32 s56,s56,7110656\n" \
qA "a[136:139],v[64:67],v[140:143],0\n" qC "v[122:123],v32 offset:37120\n" "s_min_u32 s57,s57,6094848\n" qA "a[140:143],v[68:71],v[140:143],0\n" \
"s_min_u32 s58,s58,s72\n" qA "a[144:147],v[72:75],v[140:143],0\n" qC "v[124:125],v33 offset:36864\n" qA "a[148:151],v[76:79],v[140:143],0\n" \
qA "a[152:155],v[80:83],v[140:143],0\n" qC "v[126:127],v33 offset:37120\n" qA "a[156:159],v[84:87],v[140:143],0\n" qA "a[160:163],v[48:51],v[144:147],0\n" \
qD "v[160:163],v39 offset:0\n" qA "a[164:167],v[52:55],v[144:147],0\n" qA "v[16:19],v[56:59],v[28:31],0\n" qD "v[164:167],v39 offset:2048\n" \
qA "a[168:171],v[56:59],v[144:147],0\n" qA "a[172:175],v[60:63],v[144:147],0\n" qD "v[168:171],v39 offset:4096\n" qA "a[176:179],v[64:67],v[144:147],0\n" \
qA "a[180:183],v[68:71],v[144:147],0\n" qD "v[172:175],v39 offset:6144\n" qA "a[184:187],v[72:75],v[144:147],0\n" qA "a[188:191],v[76:79],v[144:147],0\n" \
qD "v[176:179],v39 offset:8192\n" qA "a[192:195],v[80:83],v[144:147],0\n" qA "a[196:199],v[84:87],v[144:147],0\n" qD "v[180:183],v39 offset:10240\n" \
qA "a[200:203],v[48:51],v[148:151],0\n" qA "a[204:207],v[52:55],v[148:151],0\n" qD "v[184:187],v39 offset:12288\n" qA "a[208:211],v[56:59],v[148:151],0\n" \
qA "a[212:215],v[60:63],v[148:151],0\n" qD "v[188:191],v39 offset:14336\n" qA "a[216:219],v[64:67],v[148:151],0\n" qA "a[220:223],v[68:71],v[148:151],0\n" \
qA "a[224:227],v[72:75],v[148:151],0\n" qA "a[228:231],v[76:79],v[148:151],0\n" qA "v[20:23],v[60:63],v[28:31],0\n" qA "a[232:235],v[80:83],v[148:151],0\n" \
qA "a[236:239],v[84:87],v[148:151],0\n" qA "a[240:243],v[48:51],v[152:155],0\n" qA "a[244:247],v[52:55],v[152:155],0\n" qA "a[248:251],v[56:59],v[152:155],0\n" \
qA "a[252:255],v[60:63],v[152:155],0\n" qA "v[192:195],v[64:67],v[152:155],0\n" qA "v[196:199],v[68:71],v[152:155],0\n" qA "v[200:203],v[72:75],v[152:155],0\n" \
qA "v[204:207],v[76:79],v[152:155],0\n" qA "v[208:211],v[80:83],v[152:155],0\n" qA "v[212:215],v[84:87],v[152:155],0\n" qA "v[216:219],v[48:51],v[156:159],0\n" \
qA "v[220:223],v[52:55],v[156:159],0\n" qA "v[224:227],v[56:59],v[156:159],0\n" qA "v[228:231],v[60:63],v[156:159],0\n" qA "v[24:27],v[64:67],v[28:31],0\n" \
qA "v[232:235],v[64:67],v[156:159],0\n" qA "v[236:239],v[68:71],v[156:159],0\n" qA "v[240:243],v[72:75],v[156:159],0\n" qA "v[244:247],v[76:79],v[156:159],0\n" \
qA "v[248:251],v[80:83],v[156:159],0\n" qA "v[252:255],v[84:87],v[156:159],0\n" "G1L0_%=:\n" qO "lgkmcnt(0)\n" \
qA "a[0:3],v[88:91],v[160:163],a[0:3]\n" qO "vmcnt(11)\n" qA "a[4:7],v[92:95],v[160:163],a[4:7]\n" "s_barrier\n" \
qA "a[8:11],v[96:99],v[160:163],a[8:11]\n" qC "v[48:49],v35 offset:0\n" qA "a[12:15],v[100:103],v[160:163],a[12:15]\n" qN "m0,s61,0\n" \
qA "a[16:19],v[104:107],v[160:163],a[16:19]\n" qC "v[50:51],v35 offset:256\n" qA "a[20:23],v[108:111],v[160:163],a[20:23]\n" qE "v40,s[40:43],0" qU \
qA "a[24:27],v[112:115],v[160:163],a[24:27]\n" qC "v[52:53],v35 offset:10240\n" qA "a[28:31],v[116:119],v[160:163],a[28:31]\n" qN "m0,s61,1024\n" \
qA "a[32:35],v[120:123],v[160:163],a[32:35]\n" qC "v[54:55],v35 offset:10496\n" qA "a[36:39],v[124:127],v[160:163],a[36:39]\n" qE "v40,s[40:43],s65" qU \
qA "v[8:11],v[88:91],v[28:31],v[8:11]\n" qC "v[56:57],v36 offset:0\n" qA "a[40:43],v[88:91],v[164:167],a[40:43]\n" qN "m0,s61,2048\n" \
qA "a[44:47],v[92:95],v[164:167],a[44:47]\n" qC "v[58:59],v36 offset:256\n" qA "a[48:51],v[96:99],v[164:167],a[48:51]\n" qE "v40,s[40:43],s66" qU \
qA "a[52:55],v[100:103],v[164:167],a[52:55]\n" qC "v[60:61],v36 offset:10240\n" qA "a[56:59],v[104:107],v[164:167],a[56:59]\n" qN "m0,s61,3072\n" \
qA "a[60:63],v[108:111],v[164:167],a[60:63]\n" qC "v[62:63],v36 offset:10496\n" qA "a[64:67],v[112:115],v[164:167],a[64:67]\n" qE "v40,s[40:43],s67" qU \
qA "a[68:71],v[116:119],v[164:167],a[68:71]\n" qC "v[64:65],v35 offset:2048\n" qA "a[72:75],v[120:123],v[164:167],a[72:75]\n" qN "m0,s61,4096\n" \
qA "a[76:79],v[124:127],v[164:167],a[76:79]\n" qC "v[66:67],v35 offset:2304\n" qA "a[80:83],v[88:91],v[168:171],a[80:83]\n" qE "v40,s[40:43],s68" qU \
qA "a[84:87],v[92:95],v[168:171],a[84:87]\n" qC "v[68:69],v35 offset:12288\n" qA "a[88:91],v[96:99],v[168:171],a[88:91]\n" qN "m0,s61,5120\n" \
qA "a[92:95],v[100:103],v[168:171],a[92:95]\n" qC "v[70:71],v35 offset:12544\n" qA "a[96:99],v[104:107],v[168:171],a[96:99]\n" qE "v40,s[40:43],s69" qU \
qA "a[100:103],v[108:111],v[168:171],a[100:103]\n" qC "v[72:73],v36 offset:2048\n" qA "v[12:15],v[92:95],v[28:31],v[12:15]\n" qN "m0,s61,6144\n" \
qA "a[104:107],v[112:115],v[168:171],a[104:107]\n" qC "v[74:75],v36 offset:2304\n" qA "a[108:111],v[116:119],v[168:171],a[108:111]\n" qE "v40,s[40:43],s70" qU \
qA "a[112:115],v[120:123],v[168:171],a[112:115]\n" qC "v[76:77],v36 offset:12288\n" qA "a[116:119],v[124:127],v[168:171],a[116:119]\n" qN "m0,s61,7168\n" \
qA "a[120:123],v[88:91],v[172:175],a[120:123]\n" qC "v[78:79],v36 offset:12544\n" qA "a[124:127],v[92:95],v[172:175],a[124:127]\n" qE "v40,s[40:43],s71" qU \
qA "a[128:131],v[96:99],v[172:175],a[128:131]\n" qC "v[80:81],v35 offset:16384\n" qA "a[132:135],v[100:103],v[172:175],a[132:135]\n" qN "m0,s62,20480\n" \
qA "a[136:139],v[104:107],v[172:175],a[136:139]\n" qC "v[82:83],v35 offset:16640\n" qA "a[140:143],v[108:111],v[172:175],a[140:143]\n" qE "v41,s[44:47],s56" qU \
qA "a[144:147],v[112:115],v[172:175],a[144:147]\n" qC "v[84:85],v36 offset:16384\n" qA "a[148:151],v[116:119],v[172:175],a[148:151]\n" qN "m0,s62,22464\n" \
qA "a[152:155],v[120:123],v[172:175],a[152:155]\n" qC "v[86:87],v36 offset:16640\n" qA "a[156:159],v[124:127],v[172:175],a[156:159]\n" qE "v41,s[44:47],s56 offen offset:64 lds\n" \
qA "a[160:163],v[88:91],v[176:179],a[160:163]\n" qD "v[128:131],v38 offset:32768\n" qA "a[164:167],v[92:95],v[176:179],a[164:167]\n" qN "m0,s62,24448\n" \
qA "v[16:19],v[96:99],v[28:31],v[16:19]\n" qD "v[132:135],v38 offset:34816\n" qA "a[168:171],v[96:99],v[176:179],a[168:171]\n" qE "v41,s[44:47],s56 offen offset:128 lds\n" \
qA "a[172:175],v[100:103],v[176:179],a[172:175]\n" qD "v[136:139],v38 offset:36864\n" qA "a[176:179],v[104:107],v[176:179],a[176:179]\n" qN "m0,s62,26624\n" \
qA "a[180:183],v[108:111],v[176:179],a[180:183]\n" qD "v[140:143],v38 offset:38912\n" qA "a[184:187],v[112:115],v[176:179],a[184:187]\n" qE "v42,s[48:51],s57" qU \
qA "a[188:191],v[116:119],v[176:179],a[188:191]\n" qD "v[144:147],v38 offset:40960\n" qA "a[192:195],v[120:123],v[176:179],a[192:195]\n" qN "m0,s62,28672\n" \
qA "a[196:199],v[124:127],v[176:179],a[196:199]\n" qD "v[148:151],v38 offset:43008\n" qA "a[200:203],v[88:91],v[180:183],a[200:203]\n" qE "v43,s[52:55],s58" qU \
qA "a[204:207],v[92:95],v[180:183],a[204:207]\n" qD "v[152:155],v38 offset:45056\n" qA "a[208:211],v[96:99],v[180:183],a[208:211]\n" qA "a[212:215],v[100:103],v[180:183],a[212:215]\n" \
qD "v[156:159],v38 offset:47104\n" qA "a[216:219],v[104:107],v[180:183],a[216:219]\n" qA "a[220:223],v[108:111],v[180:183],a[220:223]\n" qA "a[224:227],v[112:115],v[180:183],a[224:227]\n" \
qA "a[228:231],v[116:119],v[180:183],a[228:231]\n" qN "s40,s40,128\n" qA "v[20:23],v[100:103],v[28:31],v[20:23]\n" "s_addc_u32 s41,s41,0\n" \
qA "a[232:235],v[120:123],v[180:183],a[232:235]\n" qN "s56,s56,229376\n" qA "a[236:239],v[124:127],v[180:183],a[236:239]\n" qN "s57,s57,196608\n" \
qA "a[240:243],v[88:91],v[184:187],a[240:243]\n" qN "s58,s58,s60\n" qA "a[244:247],v[92:95],v[184:187],a[244:247]\n" "s_min_u32 s56,s56,7110656\n" \
qA "a[248:251],v[96:99],v[184:187],a[248:251]\n" "s_min_u32 s57,s57,6094848\n" qA "a[252:255],v[100:103],v[184:187],a[252:255]\n" "s_min_u32 s58,s58,s72\n" \
qA "v[192:195],v[104:107],v[184:187],v[192:195]\n" qA "v[196:199],v[108:111],v[184:187],v[196:199]\n" qA "v[200:203],v[112:115],v[184:187],v[200:203]\n" qA "v[204:207],v[116:119],v[184:187],v[204:207]\n" \
qA "v[208:211],v[120:123],v[184:187],v[208:211]\n" qA "v[212:215],v[124:127],v[184:187],v[212:215]\n" qA "v[216:219],v[88:91],v[188:191],v[216:219]\n" qA "v[220:223],v[92:95],v[188:191],v[220:223]\n" \
qA "v[224:227],v[96:99],v[188:191],v[224:227]\n" qA "v[228:231],v[100:103],v[188:191],v[228:231]\n" qA "v[24:27],v[104:107],v[28:31],v[24:27]\n" qA "v[232:235],v[104:107],v[188:191],v[232:235]\n" \
qA "v[236:239],v[108:111],v[188:191],v[236:239]\n" qA "v[240:243],v[112:115],v[188:191],v[240:243]\n" qA "v[244:247],v[116:119],v[188:191],v[244:247]\n" qA "v[248:251],v[120:123],v[188:191],v[248:251]\n" \
qA "v[252:255],v[124:127],v[188:191],v[252:255]\n" qO "lgkmcnt(0)\n" qA "a[0:3],v[48:51],v[128:131],a[0:3]\n" qO "vmcnt(18)\n" \
qA "a[4:7],v[52:55],v[128:131],a[4:7]\n" "s_barrier\n" qA "a[8:11],v[56:59],v[128:131],a[8:11]\n" qC "v[88:89],v35 offset:20480\n" \
qA "a[12:15],v[60:63],v[128:131],a[12:15]\n" qN "m0,s62,40960\n" qA "a[16:19],v[64:67],v[128:131],a[16:19]\n" qC "v[90:91],v35 offset:20736\n" \
qA "a[20:23],v[68:71],v[128:131],a[20:23]\n" qE "v41,s[44:47],s56" qU qA "a[24:27],v[72:75],v[128:131],a[24:27]\n" qC "v[92:93],v35 offset:30720\n" \
qA "a[28:31],v[76:79],v[128:131],a[28:31]\n" qN "m0,s62,42944\n" qA "a[32:35],v[80:83],v[128:131],a[32:35]\n" qC "v[94:95],v35 offset:30976\n" \
qA "a[36:39],v[84:87],v[128:131],a[36:39]\n" qE "v41,s[44:47],s56 offen offset:64 lds\n" qA "v[8:11],v[48:51],v[28:31],v[8:11]\n" qC "v[96:97],v36 offset:20480\n" \
qA "a[40:43],v[48:51],v[132:135],a[40:43]\n" qN "m0,s62,44928\n" qA "a[44:47],v[52:55],v[132:135],a[44:47]\n" qC "v[98:99],v36 offset:20736\n" \
qA "a[48:51],v[56:59],v[132:135],a[48:51]\n" qE "v41,s[44:47],s56 offen offset:128 lds\n" qA "a[52:55],v[60:63],v[132:135],a[52:55]\n" qC "v[100:101],v36 offset:30720\n" \
qA "a[56:59],v[64:67],v[132:135],a[56:59]\n" qN "m0,s62,47104\n" qA "a[60:63],v[68:71],v[132:135],a[60:63]\n" qC "v[102:103],v36 offset:30976\n" \
qA "a[64:67],v[72:75],v[132:135],a[64:67]\n" qE "v42,s[48:51],s57" qU qA "a[68:71],v[76:79],v[132:135],a[68:71]\n" qC "v[104:105],v35 offset:22528\n" \
qA "a[72:75],v[80:83],v[132:135],a[72:75]\n" qN "m0,s62,49152\n" qA "a[76:79],v[84:87],v[132:135],a[76:79]\n" qC "v[106:107],v35 offset:22784\n" \
qA "a[80:83],v[48:51],v[136:139],a[80:83]\n" qE "v43,s[52:55],s58" qU qA "a[84:87],v[52:55],v[136:139],a[84:87]\n" qC "v[108:109],v35 offset:32768\n" \
qA "a[88:91],v[56:59],v[136:139],a[88:91]\n" qA "a[92:95],v[60:63],v[136:139],a[92:95]\n" qC "v[110:111],v35 offset:33024\n" qA "a[96:99],v[64:67],v[136:139],a[96:99]\n" \
qA "a[100:103],v[68:71],v[136:139],a[100:103]\n" qC "v[112:113],v36 offset:22528\n" qA "v[12:15],v[52:55],v[28:31],v[12:15]\n" qA "a[104:107],v[72:75],v[136:139],a[104:107]\n" \
qC "v[114:115],v36 offset:22784\n" qA "a[108:111],v[76:79],v[136:139],a[108:111]\n" qA "a[112:115],v[80:83],v[136:139],a[112:115]\n" qC "v[116:117],v36 offset:32768\n" \
qA "a[116:119],v[84:87],v[136:139],a[116:119]\n" qA "a[120:123],v[48:51],v[140:143],a[120:123]\n" qC "v[118:119],v36 offset:33024\n" qA "a[124:127],v[52:55],v[140:143],a[124:127]\n" \
qA "a[128:131],v[56:59],v[140:143],a[128:131]\n" qC "v[120:121],v35 offset:36864\n" qA "a[132:135],v[60:63],v[140:143],a[132:135]\n" qA "a[136:139],v[64:67],v[140:143],a[136:139]\n" \
qC "v[122:123],v35 offset:37120\n" qA "a[140:143],v[68:71],v[140:143],a[140:143]\n" qA "a[144:147],v[72:75],v[140:143],a[144:147]\n" qC "v[124:125],v36 offset:36864\n" \
qA "a[148:151],v[76:79],v[140:143],a[148:151]\n" qA "a[152:155],v[80:83],v[140:143],a[152:155]\n" qC "v[126:127],v36 offset:37120\n" qA "a[156:159],v[84:87],v[140:143],a[156:159]\n" \
qA "a[160:163],v[48:51],v[144:147],a[160:163]\n" qD "v[160:163],v39 offset:32768\n" qA "a[164:167],v[52:55],v[144:147],a[164:167]\n" qA "v[16:19],v[56:59],v[28:31],v[16:19]\n" \
qD "v[164:167],v39 offset:34816\n" qA "a[168:171],v[56:59],v[144:147],a[168:171]\n" qA "a[172:175],v[60:63],v[144:147],a[172:175]\n" qD "v[168:171],v39 offset:36864\n" \
qA "a[176:179],v[64:67],v[144:147],a[176:179]\n" qA "a[180:183],v[68:71],v[144:147],a[180:183]\n" qD "v[172:175],v39 offset:38912\n" qA "a[184:187],v[72:75],v[144:147],a[184:187]\n" \
qA "a[188:191],v[76:79],v[144:147],a[188:191]\n" qD "v[176:179],v39 offset:40960\n" qA "a[192:195],v[80:83],v[144:147],a[192:195]\n" qA "a[196:199],v[84:87],v[144:147],a[196:199]\n" \
qD "v[180:183],v39 offset:43008\n" qA "a[200:203],v[48:51],v[148:151],a[200:203]\n" qA "a[204:207],v[52:55],v[148:151],a[204:207]\n" qD "v[184:187],v39 offset:45056\n" \
qA "a[208:211],v[56:59],v[148:151],a[208:211]\n" qA "a[212:215],v[60:63],v[148:151],a[212:215]\n" qD "v[188:191],v39 offset:47104\n" qA "a[216:219],v[64:67],v[148:151],a[216:219]\n" \
qA "a[220:223],v[68:71],v[148:151],a[220:223]\n" qA "a[224:227],v[72:75],v[148:151],a[224:227]\n" qA "a[228:231],v[76:79],v[148:151],a[228:231]\n" qN "s56,s56,229376\n" \
qA "v[20:23],v[60:63],v[28:31],v[20:23]\n" qN "s57,s57,196608\n" qA "a[232:235],v[80:83],v[148:151],a[232:235]\n" qN "s58,s58,s60\n" \
qA "a[236:239],v[84:87],v[148:151],a[236:239]\n" "s_min_u32 s56,s56,7110656\n" qA "a[240:243],v[48:51],v[152:155],a[240:243]\n" "s_min_u32 s57,s57,6094848\n" \
qA "a[244:247],v[52:55],v[152:155],a[244:247]\n" "s_min_u32 s58,s58,s72\n" qA "a[248:251],v[56:59],v[152:155],a[248:251]\n" qA "a[252:255],v[60:63],v[152:155],a[252:255]\n" \
qA "v[192:195],v[64:67],v[152:155],v[192:195]\n" qA "v[196:199],v[68:71],v[152:155],v[196:199]\n" qA "v[200:203],v[72:75],v[152:155],v[200:203]\n" qA "v[204:207],v[76:79],v[152:155],v[204:207]\n" \
qA "v[208:211],v[80:83],v[152:155],v[208:211]\n" qA "v[212:215],v[84:87],v[152:155],v[212:215]\n" qA "v[216:219],v[48:51],v[156:159],v[216:219]\n" qA "v[220:223],v[52:55],v[156:159],v[220:223]\n" \
qA "v[224:227],v[56:59],v[156:159],v[224:227]\n" qA "v[228:231],v[60:63],v[156:159],v[228:231]\n" qA "v[24:27],v[64:67],v[28:31],v[24:27]\n" qA "v[232:235],v[64:67],v[156:159],v[232:235]\n" \
qA "v[236:239],v[68:71],v[156:159],v[236:239]\n" qA "v[240:243],v[72:75],v[156:159],v[240:243]\n" qA "v[244:247],v[76:79],v[156:159],v[244:247]\n" qA "v[248:251],v[80:83],v[156:159],v[248:251]\n" \
qA "v[252:255],v[84:87],v[156:159],v[252:255]\n" qO "lgkmcnt(0)\n" qA "a[0:3],v[88:91],v[160:163],a[0:3]\n" qO "vmcnt(10)\n" \
qA "a[4:7],v[92:95],v[160:163],a[4:7]\n" "s_barrier\n" qA "a[8:11],v[96:99],v[160:163],a[8:11]\n" qC "v[48:49],v32 offset:0\n" \
qA "a[12:15],v[100:103],v[160:163],a[12:15]\n" qN "m0,s61,32768\n" qA "a[16:19],v[104:107],v[160:163],a[16:19]\n" qC "v[50:51],v32 offset:256\n" \
qA "a[20:23],v[108:111],v[160:163],a[20:23]\n" qE "v40,s[40:43],0" qU qA "a[24:27],v[112:115],v[160:163],a[24:27]\n" qC "v[52:53],v32 offset:10240\n" \
qA "a[28:31],v[116:119],v[160:163],a[28:31]\n" qN "m0,s61,33792\n" qA "a[32:35],v[120:123],v[160:163],a[32:35]\n" qC "v[54:55],v32 offset:10496\n" \
qA "a[36:39],v[124:127],v[160:163],a[36:39]\n" qE "v40,s[40:43],s65" qU qA "v[8:11],v[88:91],v[28:31],v[8:11]\n" qC "v[56:57],v33 offset:0\n" \
qA "a[40:43],v[88:91],v[164:167],a[40:43]\n" qN "m0,s61,34816\n" qA "a[44:47],v[92:95],v[164:167],a[44:47]\n" qC "v[58:59],v33 offset:256\n" \
qA "a[48:51],v[96:99],v[164:167],a[48:51]\n" qE "v40,s[40:43],s66" qU qA "a[52:55],v[100:103],v[164:167],a[52:55]\n" qC "v[60:61],v33 offset:10240\n" \
qA "a[56:59],v[104:107],v[164:167],a[56:59]\n" qN "m0,s61,35840\n" qA "a[60:63],v[108:111],v[164:167],a[60:63]\n" qC "v[62:63],v33 offset:10496\n" \
qA "a[64:67],v[112:115],v[164:167],a[64:67]\n" qE "v40,s[40:43],s67" qU qA "a[68:71],v[116:119],v[164:167],a[68:71]\n" qC "v[64:65],v32 offset:2048\n" \
qA "a[72:75],v[120:123],v[164:167],a[72:75]\n" qN "m0,s61,36864\n" qA "a[76:79],v[124:127],v[164:167],a[76:79]\n" qC "v[66:67],v32 offset:2304\n" \
qA "a[80:83],v[88:91],v[168:171],a[80:83]\n" qE "v40,s[40:43],s68" qU qA "a[84:87],v[92:95],v[168:171],a[84:87]\n" qC "v[68:69],v32 offset:12288\n" \
qA "a[88:91],v[96:99],v[168:171],a[88:91]\n" qN "m0,s61,37888\n" qA "a[92:95],v[100:103],v[168:171],a[92:95]\n" qC "v[70:71],v32 offset:12544\n" \
qA "a[96:99],v[104:107],v[168:171],a[96:99]\n" qE "v40,s[40:43],s69" qU qA "a[100:103],v[108:111],v[168:171],a[100:103]\n" qC "v[72:73],v33 offset:2048\n" \
qA "v[12:15],v[92:95],v[28:31],v[12:15]\n" qN "m0,s61,38912\n" qA "a[104:107],v[112:115],v[168:171],a[104:107]\n" qC "v[74:75],v33 offset:2304\n" \
qA "a[108:111],v[116:119],v[168:171],a[108:111]\n" qE "v40,s[40:43],s70" qU qA "a[112:115],v[120:123],v[168:171],a[112:115]\n" qC "v[76:77],v33 offset:12288\n" \
qA "a[116:119],v[124:127],v[168:171],a[116:119]\n" qN "m0,s61,39936\n" qA "a[120:123],v[88:91],v[172:175],a[120:123]\n" qC "v[78:79],v33 offset:12544\n" \
qA "a[124:127],v[92:95],v[172:175],a[124:127]\n" qE "v40,s[40:43],s71" qU qA "a[128:131],v[96:99],v[172:175],a[128:131]\n" qC "v[80:81],v32 offset:16384\n" \
qA "a[132:135],v[100:103],v[172:175],a[132:135]\n" qN "m0,s62,61440\n" qA "a[136:139],v[104:107],v[172:175],a[136:139]\n" qC "v[82:83],v32 offset:16640\n" \
qA "a[140:143],v[108:111],v[172:175],a[140:143]\n" qE "v41,s[44:47],s56" qU qA "a[144:147],v[112:115],v[172:175],a[144:147]\n" qC "v[84:85],v33 offset:16384\n" \
qA "a[148:151],v[116:119],v[172:175],a[148:151]\n" qN "m0,s62,63424\n" qA "a[152:155],v[120:123],v[172:175],a[152:155]\n" qC "v[86:87],v33 offset:16640\n" \
qA "a[156:159],v[124:127],v[172:175],a[156:159]\n" qE "v41,s[44:47],s56 offen offset:64 lds\n" qA "a[160:163],v[88:91],v[176:179],a[160:163]\n" qD "v[128:131],v38 offset:0\n" \
qA "a[164:167],v[92:95],v[176:179],a[164:167]\n" qN "m0,s62,65408\n" qA "v[16:19],v[96:99],v[28:31],v[16:19]\n" qD "v[132:135],v38 offset:2048\n" \
qA "a[168:171],v[96:99],v[176:179],a[168:171]\n" qE "v41,s[44:47],s56 offen offset:128 lds\n" qA "a[172:175],v[100:103],v[176:179],a[172:175]\n" qD "v[136:139],v38 offset:4096\n" \
qA "a[176:179],v[104:107],v[176:179],a[176:179]\n" qN "m0,s62,67584\n" qA "a[180:183],v[108:111],v[176:179],a[180:183]\n" qD "v[140:143],v38 offset:6144\n" \
qA "a[184:187],v[112:115],v[176:179],a[184:187]\n" qE "v42,s[48:51],s57" qU qA "a[188:191],v[116:119],v[176:179],a[188:191]\n" qD "v[144:147],v38 offset:8192\n" \
qA "a[192:195],v[120:123],v[176:179],a[192:195]\n" qN "m0,s62,69632\n" qA "a[196:199],v[124:127],v[176:179],a[196:199]\n" qD "v[148:151],v38 offset:10240\n" \
qA "a[200:203],v[88:91],v[180:183],a[200:203]\n" qE "v43,s[52:55],s58" qU qA "a[204:207],v[92:95],v[180:183],a[204:207]\n" qD "v[152:155],v38 offset:12288\n" \
qA "a[208:211],v[96:99],v[180:183],a[208:211]\n" qA "a[212:215],v[100:103],v[180:183],a[212:215]\n" qD "v[156:159],v38 offset:14336\n" qA "a[216:219],v[104:107],v[180:183],a[216:219]\n" \
qA "a[220:223],v[108:111],v[180:183],a[220:223]\n" qA "a[224:227],v[112:115],v[180:183],a[224:227]\n" qA "a[228:231],v[116:119],v[180:183],a[228:231]\n" qN "s40,s40,128\n" \
qA "v[20:23],v[100:103],v[28:31],v[20:23]\n" "s_addc_u32 s41,s41,0\n" qA "a[232:235],v[120:123],v[180:183],a[232:235]\n" qN "s56,s56,229376\n" \
qA "a[236:239],v[124:127],v[180:183],a[236:239]\n" qN "s57,s57,196608\n" qA "a[240:243],v[88:91],v[184:187],a[240:243]\n" qN "s58,s58,s60\n" \
qA "a[244:247],v[92:95],v[184:187],a[244:247]\n" "s_min_u32 s56,s56,7110656\n" qA "a[248:251],v[96:99],v[184:187],a[248:251]\n" "s_min_u32 s57,s57,6094848\n" \
qA "a[252:255],v[100:103],v[184:187],a[252:255]\n" "s_min_u32 s58,s58,s72\n" qA "v[192:195],v[104:107],v[184:187],v[192:195]\n" qA "v[196:199],v[108:111],v[184:187],v[196:199]\n" \
qA "v[200:203],v[112:115],v[184:187],v[200:203]\n" qA "v[204:207],v[116:119],v[184:187],v[204:207]\n" qA "v[208:211],v[120:123],v[184:187],v[208:211]\n" qA "v[212:215],v[124:127],v[184:187],v[212:215]\n" \
qA "v[216:219],v[88:91],v[188:191],v[216:219]\n" qA "v[220:223],v[92:95],v[188:191],v[220:223]\n" qA "v[224:227],v[96:99],v[188:191],v[224:227]\n" qA "v[228:231],v[100:103],v[188:191],v[228:231]\n" \
qA "v[24:27],v[104:107],v[28:31],v[24:27]\n" qA "v[232:235],v[104:107],v[188:191],v[232:235]\n" qA "v[236:239],v[108:111],v[188:191],v[236:239]\n" qA "v[240:243],v[112:115],v[188:191],v[240:243]\n" \
qA "v[244:247],v[116:119],v[188:191],v[244:247]\n" qA "v[248:251],v[120:123],v[188:191],v[248:251]\n" qA "v[252:255],v[124:127],v[188:191],v[252:255]\n" qO "lgkmcnt(0)\n" \
qA "a[0:3],v[48:51],v[128:131],a[0:3]\n" qO "vmcnt(18)\n" qA "a[4:7],v[52:55],v[128:131],a[4:7]\n" "s_barrier\n" \
qA "a[8:11],v[56:59],v[128:131],a[8:11]\n" qC "v[88:89],v32 offset:20480\n" qA "a[12:15],v[60:63],v[128:131],a[12:15]\n" qN "m0,s62,0\n" \
qA "a[16:19],v[64:67],v[128:131],a[16:19]\n" qC "v[90:91],v32 offset:20736\n" qA "a[20:23],v[68:71],v[128:131],a[20:23]\n" qE "v41,s[44:47],s56" qU \
qA "a[24:27],v[72:75],v[128:131],a[24:27]\n" qC "v[92:93],v32 offset:30720\n" qA "a[28:31],v[76:79],v[128:131],a[28:31]\n" qN "m0,s62,1984\n" \
qA "a[32:35],v[80:83],v[128:131],a[32:35]\n" qC "v[94:95],v32 offset:30976\n" qA "a[36:39],v[84:87],v[128:131],a[36:39]\n" qE "v41,s[44:47],s56 offen offset:64 lds\n" \
qA "v[8:11],v[48:51],v[28:31],v[8:11]\n" qC "v[96:97],v33 offset:20480\n" qA "a[40:43],v[48:51],v[132:135],a[40:43]\n" qN "m0,s62,3968\n" \
qA "a[44:47],v[52:55],v[132:135],a[44:47]\n" qC "v[98:99],v33 offset:20736\n" qA "a[48:51],v[56:59],v[132:135],a[48:51]\n" qE "v41,s[44:47],s56 offen offset:128 lds\n" \
qA "a[52:55],v[60:63],v[132:135],a[52:55]\n" qC "v[100:101],v33 offset:30720\n" qA "a[56:59],v[64:67],v[132:135],a[56:59]\n" qN "m0,s62,6144\n" \
qA "a[60:63],v[68:71],v[132:135],a[60:63]\n" qC "v[102:103],v33 offset:30976\n" qA "a[64:67],v[72:75],v[132:135],a[64:67]\n" qE "v42,s[48:51],s57" qU \
qA "a[68:71],v[76:79],v[132:135],a[68:71]\n" qC "v[104:105],v32 offset:22528\n" qA "a[72:75],v[80:83],v[132:135],a[72:75]\n" qN "m0,s62,8192\n" \
qA "a[76:79],v[84:87],v[132:135],a[76:79]\n" qC "v[106:107],v32 offset:22784\n" qA "a[80:83],v[48:51],v[136:139],a[80:83]\n" qE "v43,s[52:55],s58" qU \
qA "a[84:87],v[52:55],v[136:139],a[84:87]\n" qC "v[108:109],v32 offset:32768\n" qA "a[88:91],v[56:59],v[136:139],a[88:91]\n" qA "a[92:95],v[60:63],v[136:139],a[92:95]\n" \
qC "v[110:111],v32 offset:33024\n" qA "a[96:99],v[64:67],v[136:139],a[96:99]\n" qA "a[100:103],v[68:71],v[136:139],a[100:103]\n" qC "v[112:113],v33 offset:22528\n" \
qA "v[12:15],v[52:55],v[28:31],v[12:15]\n" qA "a[104:107],v[72:75],v[136:139],a[104:107]\n" qC "v[114:115],v33 offset:22784\n" qA "a[108:111],v[76:79],v[136:139],a[108:111]\n" \
qA "a[112:115],v[80:83],v[136:139],a[112:115]\n" qC "v[116:117],v33 offset:32768\n" qA "a[116:119],v[84:87],v[136:139],a[116:119]\n" qA "a[120:123],v[48:51],v[140:143],a[120:123]\n" \
qC "v[118:119],v33 offset:33024\n" qA "a[124:127],v[52:55],v[140:143],a[124:127]\n" qA "a[128:131],v[56:59],v[140:143],a[128:131]\n" qC "v[120:121],v32 offset:36864\n" \
qA "a[132:135],v[60:63],v[140:143],a[132:135]\n" qA "a[136:139],v[64:67],v[140:143],a[136:139]\n" qC "v[122:123],v32 offset:37120\n" qA "a[140:143],v[68:71],v[140:143],a[140:143]\n" \
qA "a[144:147],v[72:75],v[140:143],a[144:147]\n" qC "v[124:125],v33 offset:36864\n" qA "a[148:151],v[76:79],v[140:143],a[148:151]\n" qA "a[152:155],v[80:83],v[140:143],a[152:155]\n" \
qC "v[126:127],v33 offset:37120\n" qA "a[156:159],v[84:87],v[140:143],a[156:159]\n" qA "a[160:163],v[48:51],v[144:147],a[160:163]\n" qD "v[160:163],v39 offset:0\n" \
qA "a[164:167],v[52:55],v[144:147],a[164:167]\n" qA "v[16:19],v[56:59],v[28:31],v[16:19]\n" qD "v[164:167],v39 offset:2048\n" qA "a[168:171],v[56:59],v[144:147],a[168:171]\n" \
qA "a[172:175],v[60:63],v[144:147],a[172:175]\n" qD "v[168:171],v39 offset:4096\n" qA "a[176:179],v[64:67],v[144:147],a[176:179]\n" qA "a[180:183],v[68:71],v[144:147],a[180:183]\n" \
qD "v[172:175],v39 offset:6144\n" qA "a[184:187],v[72:75],v[144:147],a[184:187]\n" qA "a[188:191],v[76:79],v[144:147],a[188:191]\n" qD "v[176:179],v39 offset:8192\n" \
qA "a[192:195],v[80:83],v[144:147],a[192:195]\n" qA "a[196:199],v[84:87],v[144:147],a[196:199]\n" qD "v[180:183],v39 offset:10240\n" qA "a[200:203],v[48:51],v[148:151],a[200:203]\n" \
qA "a[204:207],v[52:55],v[148:151],a[204:207]\n" qD "v[184:187],v39 offset:12288\n" qA "a[208:211],v[56:59],v[148:151],a[208:211]\n" qA "a[212:215],v[60:63],v[148:151],a[212:215]\n" \
qD "v[188:191],v39 offset:14336\n" qA "a[216:219],v[64:67],v[148:151],a[216:219]\n" qA "a[220:223],v[68:71],v[148:151],a[220:223]\n" qA "a[224:227],v[72:75],v[148:151],a[224:227]\n" \
qA "a[228:231],v[76:79],v[148:151],a[228:231]\n" qN "s56,s56,229376\n" qA "v[20:23],v[60:63],v[28:31],v[20:23]\n" qN "s57,s57,196608\n" \
qA "a[232:235],v[80:83],v[148:151],a[232:235]\n" qN "s58,s58,s60\n" qA "a[236:239],v[84:87],v[148:151],a[236:239]\n" "s_min_u32 s56,s56,7110656\n" \
qA "a[240:243],v[48:51],v[152:155],a[240:243]\n" "s_min_u32 s57,s57,6094848\n" qA "a[244:247],v[52:55],v[152:155],a[244:247]\n" "s_min_u32 s58,s58,s72\n" \
qA "a[248:251],v[56:59],v[152:155],a[248:251]\n" qA "a[252:255],v[60:63],v[152:155],a[252:255]\n" qA "v[192:195],v[64:67],v[152:155],v[192:195]\n" qA "v[196:199],v[68:71],v[152:155],v[196:199]\n" \
qA "v[200:203],v[72:75],v[152:155],v[200:203]\n" "s_mov_b32 m0,s64\n" qA "v[204:207],v[76:79],v[152:155],v[204:207]\n" qA "v[208:211],v[80:83],v[152:155],v[208:211]\n" \
qE "v46,s[84:87],0" qV qA "v[212:215],v[84:87],v[152:155],v[212:215]\n" qA "v[216:219],v[48:51],v[156:159],v[216:219]\n" qA "v[220:223],v[52:55],v[156:159],v[220:223]\n" \
qA "v[224:227],v[56:59],v[156:159],v[224:227]\n" qA "v[228:231],v[60:63],v[156:159],v[228:231]\n" qA "v[24:27],v[64:67],v[28:31],v[24:27]\n" qA "v[232:235],v[64:67],v[156:159],v[232:235]\n" \
qA "v[236:239],v[68:71],v[156:159],v[236:239]\n" qA "v[240:243],v[72:75],v[156:159],v[240:243]\n" qA "v[244:247],v[76:79],v[156:159],v[244:247]\n" qA "v[248:251],v[80:83],v[156:159],v[248:251]\n" \
"s_sub_u32 s63,s63,1\n" "s_cmp_lg_u32 s63,0\n" qA "v[252:255],v[84:87],v[156:159],v[252:255]\n" "s_cbranch_scc1 G1L0_%=\n" \
"G1TAIL0_%=:\n" qO "lgkmcnt(0)\n" qA "a[0:3],v[88:91],v[160:163],a[0:3]\n" qO "vmcnt(11)\n" \
qA "a[4:7],v[92:95],v[160:163],a[4:7]\n" "s_barrier\n" qA "a[8:11],v[96:99],v[160:163],a[8:11]\n" qC "v[48:49],v35 offset:0\n" \
qA "a[12:15],v[100:103],v[160:163],a[12:15]\n" "v_mov_b32 v2,-1\n" qA "a[16:19],v[104:107],v[160:163],a[16:19]\n" qC "v[50:51],v35 offset:256\n" \
"v_mov_b32 v3,-1\n" qA "a[20:23],v[108:111],v[160:163],a[20:23]\n" "v_mov_b32 v4,-1\n" qA "a[24:27],v[112:115],v[160:163],a[24:27]\n" \
qC "v[52:53],v35 offset:10240\n" "v_mov_b32 v5,-1\n" qA "a[28:31],v[116:119],v[160:163],a[28:31]\n" qA "a[32:35],v[120:123],v[160:163],a[32:35]\n" \
qC "v[54:55],v35 offset:10496\n" qA "a[36:39],v[124:127],v[160:163],a[36:39]\n" qF "v[2:5],v47,s[84:87],s90 offen offset:0 sc1\n" qA "v[8:11],v[88:91],v[28:31],v[8:11]\n" \
qC "v[56:57],v36 offset:0\n" qA "a[40:43],v[88:91],v[164:167],a[40:43]\n" qA "a[44:47],v[92:95],v[164:167],a[44:47]\n" qC "v[58:59],v36 offset:256\n" \
qA "a[48:51],v[96:99],v[164:167],a[48:51]\n" qA "a[52:55],v[100:103],v[164:167],a[52:55]\n" qC "v[60:61],v36 offset:10240\n" qA "a[56:59],v[104:107],v[164:167],a[56:59]\n" \
qA "a[60:63],v[108:111],v[164:167],a[60:63]\n" qC "v[62:63],v36 offset:10496\n" qA "a[64:67],v[112:115],v[164:167],a[64:67]\n" qA "a[68:71],v[116:119],v[164:167],a[68:71]\n" \
qC "v[64:65],v35 offset:2048\n" qA "a[72:75],v[120:123],v[164:167],a[72:75]\n" qA "a[76:79],v[124:127],v[164:167],a[76:79]\n" qC "v[66:67],v35 offset:2304\n" \
qA "a[80:83],v[88:91],v[168:171],a[80:83]\n" qF "v[2:5],v47,s[84:87],s90 offen offset:1024 sc1\n" qA "a[84:87],v[92:95],v[168:171],a[84:87]\n" qC "v[68:69],v35 offset:12288\n" \
qA "a[88:91],v[96:99],v[168:171],a[88:91]\n" qA "a[92:95],v[100:103],v[168:171],a[92:95]\n" qC "v[70:71],v35 offset:12544\n" qA "a[96:99],v[104:107],v[168:171],a[96:99]\n" \
qA "a[100:103],v[108:111],v[168:171],a[100:103]\n" qC "v[72:73],v36 offset:2048\n" qA "v[12:15],v[92:95],v[28:31],v[12:15]\n" qA "a[104:107],v[112:115],v[168:171],a[104:107]\n" \
qC "v[74:75],v36 offset:2304\n" qA "a[108:111],v[116:119],v[168:171],a[108:111]\n" qA "a[112:115],v[120:123],v[168:171],a[112:115]\n" qC "v[76:77],v36 offset:12288\n" \
qA "a[116:119],v[124:127],v[168:171],a[116:119]\n" qA "a[120:123],v[88:91],v[172:175],a[120:123]\n" qC "v[78:79],v36 offset:12544\n" qA "a[124:127],v[92:95],v[172:175],a[124:127]\n" \
qF "v[2:5],v47,s[84:87],s90 offen offset:2048 sc1\n" qA "a[128:131],v[96:99],v[172:175],a[128:131]\n" qC "v[80:81],v35 offset:16384\n" qA "a[132:135],v[100:103],v[172:175],a[132:135]\n" \
qA "a[136:139],v[104:107],v[172:175],a[136:139]\n" qC "v[82:83],v35 offset:16640\n" qA "a[140:143],v[108:111],v[172:175],a[140:143]\n" qA "a[144:147],v[112:115],v[172:175],a[144:147]\n" \
qC "v[84:85],v36 offset:16384\n" qA "a[148:151],v[116:119],v[172:175],a[148:151]\n" qA "a[152:155],v[120:123],v[172:175],a[152:155]\n" qC "v[86:87],v36 offset:16640\n" \
qA "a[156:159],v[124:127],v[172:175],a[156:159]\n" qA "a[160:163],v[88:91],v[176:179],a[160:163]\n" qD "v[128:131],v38 offset:32768\n" qA "a[164:167],v[92:95],v[176:179],a[164:167]\n" \
qA "v[16:19],v[96:99],v[28:31],v[16:19]\n" qD "v[132:135],v38 offset:34816\n" qA "a[168:171],v[96:99],v[176:179],a[168:171]\n" qF "v[2:5],v47,s[84:87],s90 offen offset:3072 sc1\n" \
qA "a[172:175],v[100:103],v[176:179],a[172:175]\n" qD "v[136:139],v38 offset:36864\n" qA "a[176:179],v[104:107],v[176:179],a[176:179]\n" qA "a[180:183],v[108:111],v[176:179],a[180:183]\n" \
qD "v[140:143],v38 offset:38912\n" qA "a[184:187],v[112:115],v[176:179],a[184:187]\n" qA "a[188:191],v[116:119],v[176:179],a[188:191]\n" qD "v[144:147],v38 offset:40960\n" \
qA "a[192:195],v[120:123],v[176:179],a[192:195]\n" qA "a[196:199],v[124:127],v[176:179],a[196:199]\n" qD "v[148:151],v38 offset:43008\n" qA "a[200:203],v[88:91],v[180:183],a[200:203]\n" \
qA "a[204:207],v[92:95],v[180:183],a[204:207]\n" qD "v[152:155],v38 offset:45056\n" qA "a[208:211],v[96:99],v[180:183],a[208:211]\n" qA "a[212:215],v[100:103],v[180:183],a[212:215]\n" \
qD "v[156:159],v38 offset:47104\n" qA "a[216:219],v[104:107],v[180:183],a[216:219]\n" qA "a[220:223],v[108:111],v[180:183],a[220:223]\n" "s_mov_b32 m0,s73\n" \
qA "a[224:227],v[112:115],v[180:183],a[224:227]\n" qA "a[228:231],v[116:119],v[180:183],a[228:231]\n" "global_load_lds_dwordx4 v47,s[100:101] offset:0\n" qA "v[20:23],v[100:103],v[28:31],v[20:23]\n" \
qA "a[232:235],v[120:123],v[180:183],a[232:235]\n" "global_load_lds_dwordx4 v47,s[100:101] offset:1024\n" qA "a[236:239],v[124:127],v[180:183],a[236:239]\n" qA "a[240:243],v[88:91],v[184:187],a[240:243]\n" \
"global_load_lds_dwordx4 v47,s[100:101] offset:2048\n" qA "a[244:247],v[92:95],v[184:187],a[244:247]\n" qA "a[248:251],v[96:99],v[184:187],a[248:251]\n" "global_load_lds_dwordx4 v47,s[100:101] offset:3072\n" \
qA "a[252:255],v[100:103],v[184:187],a[252:255]\n" qA "v[192:195],v[104:107],v[184:187],v[192:195]\n" "global_load_lds_dwordx4 v44,s[100:101] offset:0\n" qA "v[196:199],v[108:111],v[184:187],v[196:199]\n" \
qA "v[200:203],v[112:115],v[184:187],v[200:203]\n" "global_load_lds_dwordx4 v44,s[100:101] offset:1024\n" qA "v[204:207],v[116:119],v[184:187],v[204:207]\n" qA "v[208:211],v[120:123],v[184:187],v[208:211]\n" \
"global_load_lds_dwordx4 v44,s[100:101] offset:2048\n" qA "v[212:215],v[124:127],v[184:187],v[212:215]\n" qA "v[216:219],v[88:91],v[188:191],v[216:219]\n" "global_load_lds_dwordx4 v47,s[82:83] offset:0\n" \
qA "v[220:223],v[92:95],v[188:191],v[220:223]\n" qA "v[224:227],v[96:99],v[188:191],v[224:227]\n" "global_load_lds_dwordx4 v47,s[82:83] offset:1024\n" qA "v[228:231],v[100:103],v[188:191],v[228:231]\n" \
qA "v[24:27],v[104:107],v[28:31],v[24:27]\n" qA "v[232:235],v[104:107],v[188:191],v[232:235]\n" qA "v[236:239],v[108:111],v[188:191],v[236:239]\n" qA "v[240:243],v[112:115],v[188:191],v[240:243]\n" \
qA "v[244:247],v[116:119],v[188:191],v[244:247]\n" qA "v[248:251],v[120:123],v[188:191],v[248:251]\n" qA "v[252:255],v[124:127],v[188:191],v[252:255]\n" qO "lgkmcnt(0)\n" \
qA "a[0:3],v[48:51],v[128:131],a[0:3]\n" qO "vmcnt(13)\n" qA "a[4:7],v[52:55],v[128:131],a[4:7]\n" "s_barrier\n" \
qA "a[8:11],v[56:59],v[128:131],a[8:11]\n" qC "v[88:89],v35 offset:20480\n" qA "a[12:15],v[60:63],v[128:131],a[12:15]\n" qA "a[16:19],v[64:67],v[128:131],a[16:19]\n" \
qC "v[90:91],v35 offset:20736\n" qA "a[20:23],v[68:71],v[128:131],a[20:23]\n" qA "a[24:27],v[72:75],v[128:131],a[24:27]\n" qC "v[92:93],v35 offset:30720\n" \
qA "a[28:31],v[76:79],v[128:131],a[28:31]\n" qA "a[32:35],v[80:83],v[128:131],a[32:35]\n" qC "v[94:95],v35 offset:30976\n" qA "a[36:39],v[84:87],v[128:131],a[36:39]\n" \
qA "v[8:11],v[48:51],v[28:31],v[8:11]\n" qC "v[96:97],v36 offset:20480\n" qA "a[40:43],v[48:51],v[132:135],a[40:43]\n" qA "a[44:47],v[52:55],v[132:135],a[44:47]\n" \
qC "v[98:99],v36 offset:20736\n" qA "a[48:51],v[56:59],v[132:135],a[48:51]\n" qA "a[52:55],v[60:63],v[132:135],a[52:55]\n" qC "v[100:101],v36 offset:30720\n" \
qA "a[56:59],v[64:67],v[132:135],a[56:59]\n" qA "a[60:63],v[68:71],v[132:135],a[60:63]\n" qC "v[102:103],v36 offset:30976\n" qA "a[64:67],v[72:75],v[132:135],a[64:67]\n" \
qA "a[68:71],v[76:79],v[132:135],a[68:71]\n" qC "v[104:105],v35 offset:22528\n" qA "a[72:75],v[80:83],v[132:135],a[72:75]\n" qA "a[76:79],v[84:87],v[132:135],a[76:79]\n" \
qC "v[106:107],v35 offset:22784\n" qA "a[80:83],v[48:51],v[136:139],a[80:83]\n" qA "a[84:87],v[52:55],v[136:139],a[84:87]\n" qC "v[108:109],v35 offset:32768\n" \
qA "a[88:91],v[56:59],v[136:139],a[88:91]\n" qA "a[92:95],v[60:63],v[136:139],a[92:95]\n" qC "v[110:111],v35 offset:33024\n" qA "a[96:99],v[64:67],v[136:139],a[96:99]\n" \
qA "a[100:103],v[68:71],v[136:139],a[100:103]\n" qC "v[112:113],v36 offset:22528\n" qA "v[12:15],v[52:55],v[28:31],v[12:15]\n" qA "a[104:107],v[72:75],v[136:139],a[104:107]\n" \
qC "v[114:115],v36 offset:22784\n" qA "a[108:111],v[76:79],v[136:139],a[108:111]\n" qA "a[112:115],v[80:83],v[136:139],a[112:115]\n" qC "v[116:117],v36 offset:32768\n" \
qA "a[116:119],v[84:87],v[136:139],a[116:119]\n" qA "a[120:123],v[48:51],v[140:143],a[120:123]\n" qC "v[118:119],v36 offset:33024\n" qA "a[124:127],v[52:55],v[140:143],a[124:127]\n" \
qA "a[128:131],v[56:59],v[140:143],a[128:131]\n" qC "v[120:121],v35 offset:36864\n" qA "a[132:135],v[60:63],v[140:143],a[132:135]\n" qA "a[136:139],v[64:67],v[140:143],a[136:139]\n" \
qC "v[122:123],v35 offset:37120\n" qA "a[140:143],v[68:71],v[140:143],a[140:143]\n" qA "a[144:147],v[72:75],v[140:143],a[144:147]\n" qC "v[124:125],v36 offset:36864\n" \
qA "a[148:151],v[76:79],v[140:143],a[148:151]\n" qA "a[152:155],v[80:83],v[140:143],a[152:155]\n" qC "v[126:127],v36 offset:37120\n" qA "a[156:159],v[84:87],v[140:143],a[156:159]\n" \
qA "a[160:163],v[48:51],v[144:147],a[160:163]\n" qD "v[160:163],v39 offset:32768\n" qA "a[164:167],v[52:55],v[144:147],a[164:167]\n" qA "v[16:19],v[56:59],v[28:31],v[16:19]\n" \
qD "v[164:167],v39 offset:34816\n" qA "a[168:171],v[56:59],v[144:147],a[168:171]\n" qA "a[172:175],v[60:63],v[144:147],a[172:175]\n" qD "v[168:171],v39 offset:36864\n" \
qA "a[176:179],v[64:67],v[144:147],a[176:179]\n" qA "a[180:183],v[68:71],v[144:147],a[180:183]\n" qD "v[172:175],v39 offset:38912\n" qA "a[184:187],v[72:75],v[144:147],a[184:187]\n" \
qA "a[188:191],v[76:79],v[144:147],a[188:191]\n" qD "v[176:179],v39 offset:40960\n" qA "a[192:195],v[80:83],v[144:147],a[192:195]\n" qA "a[196:199],v[84:87],v[144:147],a[196:199]\n" \
qD "v[180:183],v39 offset:43008\n" qA "a[200:203],v[48:51],v[148:151],a[200:203]\n" qA "a[204:207],v[52:55],v[148:151],a[204:207]\n" qD "v[184:187],v39 offset:45056\n" \
qA "a[208:211],v[56:59],v[148:151],a[208:211]\n" qA "a[212:215],v[60:63],v[148:151],a[212:215]\n" qD "v[188:191],v39 offset:47104\n" qA "a[216:219],v[64:67],v[148:151],a[216:219]\n" \
qA "a[220:223],v[68:71],v[148:151],a[220:223]\n" qA "a[224:227],v[72:75],v[148:151],a[224:227]\n" qA "a[228:231],v[76:79],v[148:151],a[228:231]\n" qA "v[20:23],v[60:63],v[28:31],v[20:23]\n" \
qA "a[232:235],v[80:83],v[148:151],a[232:235]\n" qA "a[236:239],v[84:87],v[148:151],a[236:239]\n" qA "a[240:243],v[48:51],v[152:155],a[240:243]\n" qA "a[244:247],v[52:55],v[152:155],a[244:247]\n" \
qA "a[248:251],v[56:59],v[152:155],a[248:251]\n" qA "a[252:255],v[60:63],v[152:155],a[252:255]\n" qA "v[192:195],v[64:67],v[152:155],v[192:195]\n" qA "v[196:199],v[68:71],v[152:155],v[196:199]\n" \
qA "v[200:203],v[72:75],v[152:155],v[200:203]\n" qA "v[204:207],v[76:79],v[152:155],v[204:207]\n" qA "v[208:211],v[80:83],v[152:155],v[208:211]\n" qA "v[212:215],v[84:87],v[152:155],v[212:215]\n" \
qA "v[216:219],v[48:51],v[156:159],v[216:219]\n" qA "v[220:223],v[52:55],v[156:159],v[220:223]\n" qA "v[224:227],v[56:59],v[156:159],v[224:227]\n" qA "v[228:231],v[60:63],v[156:159],v[228:231]\n" \
qA "v[24:27],v[64:67],v[28:31],v[24:27]\n" qA "v[232:235],v[64:67],v[156:159],v[232:235]\n" qA "v[236:239],v[68:71],v[156:159],v[236:239]\n" qA "v[240:243],v[72:75],v[156:159],v[240:243]\n" \
qA "v[244:247],v[76:79],v[156:159],v[244:247]\n" qA "v[248:251],v[80:83],v[156:159],v[248:251]\n" qA "v[252:255],v[84:87],v[156:159],v[252:255]\n" qO "lgkmcnt(0)\n" \
qA "a[0:3],v[88:91],v[160:163],a[0:3]\n" qA "a[4:7],v[92:95],v[160:163],a[4:7]\n" qA "a[8:11],v[96:99],v[160:163],a[8:11]\n" qD "v[48:51],v7 offset:0\n" \
qA "a[12:15],v[100:103],v[160:163],a[12:15]\n" qD "v[52:55],v7 offset:256\n" qA "a[16:19],v[104:107],v[160:163],a[16:19]\n" qD "v[56:59],v7 offset:512\n" \
qA "a[20:23],v[108:111],v[160:163],a[20:23]\n" qD "v[60:63],v7 offset:768\n" qA "a[24:27],v[112:115],v[160:163],a[24:27]\n" qD "v[64:67],v7 offset:1024\n" \
qA "a[28:31],v[116:119],v[160:163],a[28:31]\n" qD "v[68:71],v7 offset:1280\n" qA "a[32:35],v[120:123],v[160:163],a[32:35]\n" qD "v[72:75],v7 offset:1536\n" \
qA "a[36:39],v[124:127],v[160:163],a[36:39]\n" qD "v[76:79],v7 offset:1792\n" qA "v[8:11],v[88:91],v[28:31],v[8:11]\n" qA "a[40:43],v[88:91],v[164:167],a[40:43]\n" \
qA "a[44:47],v[92:95],v[164:167],a[44:47]\n" qA "a[48:51],v[96:99],v[164:167],a[48:51]\n" qA "a[52:55],v[100:103],v[164:167],a[52:55]\n" qA "a[56:59],v[104:107],v[164:167],a[56:59]\n" \
qA "a[60:63],v[108:111],v[164:167],a[60:63]\n" qA "a[64:67],v[112:115],v[164:167],a[64:67]\n" qA "a[68:71],v[116:119],v[164:167],a[68:71]\n" qA "a[72:75],v[120:123],v[164:167],a[72:75]\n" \
qA "a[76:79],v[124:127],v[164:167],a[76:79]\n" qA "a[80:83],v[88:91],v[168:171],a[80:83]\n" qA "a[84:87],v[92:95],v[168:171],a[84:87]\n" qA "a[88:91],v[96:99],v[168:171],a[88:91]\n" \
qA "a[92:95],v[100:103],v[168:171],a[92:95]\n" qA "a[96:99],v[104:107],v[168:171],a[96:99]\n" qA "a[100:103],v[108:111],v[168:171],a[100:103]\n" qA "v[12:15],v[92:95],v[28:31],v[12:15]\n" \
qA "a[104:107],v[112:115],v[168:171],a[104:107]\n" qA "a[108:111],v[116:119],v[168:171],a[108:111]\n" qA "a[112:115],v[120:123],v[168:171],a[112:115]\n" qO "lgkmcnt(0)\n" \
qA "a[116:119],v[124:127],v[168:171],a[116:119]\n" "v_min3_u32 v84,v48,v50,v52\n" qA "a[120:123],v[88:91],v[172:175],a[120:123]\n" "v_max3_u32 v85,v48,v50,v52\n" \
qA "a[124:127],v[92:95],v[172:175],a[124:127]\n" "v_min3_u32 v84,v84,v54,v56\n" qA "a[128:131],v[96:99],v[172:175],a[128:131]\n" "v_max3_u32 v85,v85,v54,v56\n" \
qA "a[132:135],v[100:103],v[172:175],a[132:135]\n" "v_min3_u32 v84,v84,v58,v60\n" qA "a[136:139],v[104:107],v[172:175],a[136:139]\n" "v_max3_u32 v85,v85,v58,v60\n" \
qA "a[140:143],v[108:111],v[172:175],a[140:143]\n" "v_min3_u32 v84,v84,v62,v64\n" qA "a[144:147],v[112:115],v[172:175],a[144:147]\n" "v_max3_u32 v85,v85,v62,v64\n" \
qA "a[148:151],v[116:119],v[172:175],a[148:151]\n" "v_min3_u32 v84,v84,v66,v68\n" qA "a[152:155],v[120:123],v[172:175],a[152:155]\n" "v_max3_u32 v85,v85,v66,v68\n" \
qA "a[156:159],v[124:127],v[172:175],a[156:159]\n" "v_min3_u32 v84,v84,v70,v72\n" qA "a[160:163],v[88:91],v[176:179],a[160:163]\n" "v_max3_u32 v85,v85,v70,v72\n" \
qA "a[164:167],v[92:95],v[176:179],a[164:167]\n" "v_min3_u32 v84,v84,v74,v76\n" qA "v[16:19],v[96:99],v[28:31],v[16:19]\n" "v_max3_u32 v85,v85,v74,v76\n" \
qA "a[168:171],v[96:99],v[176:179],a[168:171]\n" "v_min_u32 v84,v84,v78\n" qA "a[172:175],v[100:103],v[176:179],a[172:175]\n" "v_max_u32 v85,v85,v78\n" \
qA "a[176:179],v[104:107],v[176:179],a[176:179]\n" qA "a[180:183],v[108:111],v[176:179],a[180:183]\n" qA "a[184:187],v[112:115],v[176:179],a[184:187]\n" "v_cmp_ne_u32 s[92:93],s75,v84\n" \
qA "a[188:191],v[116:119],v[176:179],a[188:191]\n" "v_cmp_ne_u32 vcc,s75,v85\n" qA "a[192:195],v[120:123],v[176:179],a[192:195]\n" qA "a[196:199],v[124:127],v[176:179],a[196:199]\n" \
qA "a[200:203],v[88:91],v[180:183],a[200:203]\n" qA "a[204:207],v[92:95],v[180:183],a[204:207]\n" "s_or_b64 s[92:93],s[92:93],vcc\n" qA "a[208:211],v[96:99],v[180:183],a[208:211]\n" \
qA "a[212:215],v[100:103],v[180:183],a[212:215]\n" "v_and_b32 v86,0xf0,v47\n" qA "a[216:219],v[104:107],v[180:183],a[216:219]\n" "v_lshlrev_b32 v86,2,v86\n" \
qA "a[220:223],v[108:111],v[180:183],a[220:223]\n" "v_bfe_u32 v87,v47,8,1\n" qA "a[224:227],v[112:115],v[180:183],a[224:227]\n" "v_bfe_u32 v83,v47,9,1\n" \
qA "a[228:231],v[116:119],v[180:183],a[228:231]\n" "v_lshl_or_b32 v87,v87,1,v83\n" qA "v[20:23],v[100:103],v[28:31],v[20:23]\n" "v_bfe_u32 v83,v47,6,2\n" \
qA "a[232:235],v[120:123],v[180:183],a[232:235]\n" "v_xor_b32 v83,v83,v87\n" qA "a[236:239],v[124:127],v[180:183],a[236:239]\n" "v_lshl_add_u32 v83,v83,4,v86\n" \
qA "a[240:243],v[88:91],v[184:187],a[240:243]\n" "v_add_u32 v3,0,v83\n" qA "a[244:247],v[92:95],v[184:187],a[244:247]\n" "v_lshl_add_u32 v87,v87,4,v86\n" \
qA "a[248:251],v[96:99],v[184:187],a[248:251]\n" "v_add_u32 v1,32768,v83\n" qA "a[252:255],v[100:103],v[184:187],a[252:255]\n" "v_add_u32 v2,0,v87\n" \
qA "v[192:195],v[104:107],v[184:187],v[192:195]\n" "v_lshrrev_b32 v83,8,v47\n" qA "v[196:199],v[108:111],v[184:187],v[196:199]\n" "v_lshlrev_b32 v83,4,v83\n" \
qA "v[200:203],v[112:115],v[184:187],v[200:203]\n" "v_add_u32 v5,147456,v83\n" qA "v[204:207],v[116:119],v[184:187],v[204:207]\n" "v_add_u32 v4,0,v5\n" \
qA "v[208:211],v[120:123],v[184:187],v[208:211]\n" qN "s56,s76,4096\n" qA "v[212:215],v[124:127],v[184:187],v[212:215]\n" qA "v[216:219],v[88:91],v[188:191],v[216:219]\n" \
qA "v[220:223],v[92:95],v[188:191],v[220:223]\n" qA "v[224:227],v[96:99],v[188:191],v[224:227]\n" qA "v[228:231],v[100:103],v[188:191],v[228:231]\n" qA "v[24:27],v[104:107],v[28:31],v[24:27]\n" \
qA "v[232:235],v[104:107],v[188:191],v[232:235]\n" qA "v[236:239],v[108:111],v[188:191],v[236:239]\n" qA "v[240:243],v[112:115],v[188:191],v[240:243]\n" qA "v[244:247],v[116:119],v[188:191],v[244:247]\n" \
qA "v[248:251],v[120:123],v[188:191],v[248:251]\n" qA "v[252:255],v[124:127],v[188:191],v[252:255]\n" "s_cmp_eq_u64 s[92:93],0\n" "s_cbranch_scc1 G1OK0_%=\n" \
"s_mov_b32 s63,0x100000\n" "v_subrev_u32 v82,s64,v7\n" "v_add_u32 v82,0,v82\n" "G1RE0_%=:\n" \
"s_sleep 1\n" qE "v[48:51],v82,s[84:87],0 offen offset:0 sc1\n" qE "v[52:55],v82,s[84:87],0 offen offset:256 sc1\n" qE "v[56:59],v82,s[84:87],0 offen offset:512 sc1\n" \
qE "v[60:63],v82,s[84:87],0 offen offset:768 sc1\n" qE "v[64:67],v82,s[84:87],0 offen offset:1024 sc1\n" qE "v[68:71],v82,s[84:87],0 offen offset:1280 sc1\n" qE "v[72:75],v82,s[84:87],0 offen offset:1536 sc1\n" \
qE "v[76:79],v82,s[84:87],0 offen offset:1792 sc1\n" qO "vmcnt(0)\n" "v_min3_u32 v84,v48,v50,v52\n" "v_max3_u32 v85,v48,v50,v52\n" \
"v_min3_u32 v84,v84,v54,v56\n" "v_max3_u32 v85,v85,v54,v56\n" "v_min3_u32 v84,v84,v58,v60\n" "v_max3_u32 v85,v85,v58,v60\n" \
"v_min3_u32 v84,v84,v62,v64\n" "v_max3_u32 v85,v85,v62,v64\n" "v_min3_u32 v84,v84,v66,v68\n" "v_max3_u32 v85,v85,v66,v68\n" \
"v_min3_u32 v84,v84,v70,v72\n" "v_max3_u32 v85,v85,v70,v72\n" "v_min3_u32 v84,v84,v74,v76\n" "v_max3_u32 v85,v85,v74,v76\n" \
"v_min_u32 v84,v84,v78\n" "v_max_u32 v85,v85,v78\n" "v_cmp_ne_u32 s[92:93],s75,v84\n" "v_cmp_ne_u32 vcc,s75,v85\n" \
"s_nop 3\n" "s_or_b64 s[92:93],s[92:93],vcc\n" "s_sub_u32 s63,s63,1\n" "s_cmp_eq_u32 s63,0\n" \
"s_cbranch_scc1 G1OK0_%=\n" "s_cmp_eq_u64 s[92:93],0\n" "s_cbranch_scc0 G1RE0_%=\n" "G1OK0_%=:\n" \
"s_nop 7\n" "s_mov_b32 exec_lo,0x00010001\n" "s_mov_b32 exec_hi,0x00010001\n" qQ "v4,v[8:11] offset:0\n" \
qQ "v4,v[12:15] offset:64\n" qQ "v4,v[16:19] offset:128\n" qQ "v4,v[20:23] offset:192\n" qQ "v4,v[24:27] offset:256\n" \
"s_mov_b64 exec,-1\n" qO "lgkmcnt(0)\n" "s_barrier\n" qD "v[8:11],v5 offset:0\n" \
qD "v[12:15],v5 offset:64\n" qD "v[16:19],v5 offset:128\n" qD "v[20:23],v5 offset:192\n" qD "v[24:27],v5 offset:256\n" \
qD "v[28:31],v5 offset:320\n" qD "v[32:35],v5 offset:384\n" qD "v[36:39],v5 offset:448\n" qD "v[40:43],v5 offset:512\n" \
qD "v[44:47],v5 offset:576\n" qO "lgkmcnt(0)\n" qG "v80,a32\n" qG "v81,a33\n" \
qG "v82,a34\n" qG "v83,a35\n" qG "v84,a36\n" qG "v85,a37\n" \
qG "v86,a38\n" qG "v87,a39\n" qG "v88,a72\n" qG "v89,a73\n" \
qG "v90,a74\n" qG "v91,a75\n" qG "v92,a76\n" qG "v93,a77\n" \
qG "v94,a78\n" qG "v95,a79\n" qG "v96,a112\n" qG "v97,a113\n" \
qG "v98,a114\n" qG "v99,a115\n" qG "v100,a116\n" qG "v101,a117\n" \
qG "v102,a118\n" qG "v103,a119\n" qG "v104,a152\n" qG "v105,a153\n" \
qG "v106,a154\n" qG "v107,a155\n" qG "v108,a156\n" qG "v109,a157\n" \
qG "v110,a158\n" qG "v111,a159\n" qH "v[80:81],v[48:49],v[40:41],v[80:81]" qR qH "v[82:83],v[48:49],v[42:43],v[82:83]" qR \
qH "v[84:85],v[48:49],v[44:45],v[84:85]" qR qH "v[86:87],v[48:49],v[46:47],v[86:87]" qR qH "v[88:89],v[52:53],v[40:41],v[88:89]" qR qH "v[90:91],v[52:53],v[42:43],v[90:91]" qR \
qH "v[92:93],v[52:53],v[44:45],v[92:93]" qR qH "v[94:95],v[52:53],v[46:47],v[94:95]" qR qH "v[96:97],v[56:57],v[40:41],v[96:97]" qR qH "v[98:99],v[56:57],v[42:43],v[98:99]" qR \
qH "v[100:101],v[56:57],v[44:45],v[100:101]" qR qH "v[102:103],v[56:57],v[46:47],v[102:103]" qR qH "v[104:105],v[60:61],v[40:41],v[104:105]" qR qH "v[106:107],v[60:61],v[42:43],v[106:107]" qR \
qH "v[108:109],v[60:61],v[44:45],v[108:109]" qR qH "v[110:111],v[60:61],v[46:47],v[110:111]" qR qI "v[80:81],v[80:81],v[50:51]" qS qI "v[82:83],v[82:83],v[50:51]" qS \
qI "v[84:85],v[84:85],v[50:51]" qS qI "v[86:87],v[86:87],v[50:51]" qS qI "v[88:89],v[88:89],v[54:55]" qS qI "v[90:91],v[90:91],v[54:55]" qS \
qI "v[92:93],v[92:93],v[54:55]" qS qI "v[94:95],v[94:95],v[54:55]" qS qI "v[96:97],v[96:97],v[58:59]" qS qI "v[98:99],v[98:99],v[58:59]" qS \
qI "v[100:101],v[100:101],v[58:59]" qS qI "v[102:103],v[102:103],v[58:59]" qS qI "v[104:105],v[104:105],v[62:63]" qS qI "v[106:107],v[106:107],v[62:63]" qS \
qI "v[108:109],v[108:109],v[62:63]" qS qI "v[110:111],v[110:111],v[62:63]" qS qJ "v112,v80,v81\n" qJ "v113,v82,v83\n" \
qJ "v114,v84,v85\n" qJ "v115,v86,v87\n" qJ "v116,v88,v89\n" qJ "v117,v90,v91\n" \
qJ "v118,v92,v93\n" qJ "v119,v94,v95\n" qJ "v120,v96,v97\n" qJ "v121,v98,v99\n" \
qJ "v122,v100,v101\n" qJ "v123,v102,v103\n" qJ "v124,v104,v105\n" qJ "v125,v106,v107\n" \
qJ "v126,v108,v109\n" qJ "v127,v110,v111\n" qK "v112,v114\n" qK "v113,v115\n" \
qK "v116,v118\n" qK "v117,v119\n" qK "v120,v122\n" qK "v121,v123\n" \
qK "v124,v126\n" qK "v125,v127\n" qF "v[112:115],v2,s[84:87],s76 offen offset:0 sc1\n" qQ "v1,v[112:115] offset:0\n" \
qG "v80,a192\n" qG "v81,a193\n" qG "v82,a194\n" qG "v83,a195\n" \
qQ "v1,v[116:119] offset:1024\n" qG "v84,a196\n" qF "v[116:119],v2,s[84:87],s76 offen offset:1024 sc1\n" qG "v85,a197\n" \
qG "v86,a198\n" qG "v87,a199\n" qG "v88,a232\n" qG "v89,a233\n" \
qG "v90,a234\n" qG "v91,a235\n" qG "v92,a236\n" qF "v[120:123],v2,s[84:87],s76 offen offset:2048 sc1\n" \
qG "v93,a237\n" qG "v94,a238\n" qG "v95,a239\n" qQ "v1,v[120:123] offset:2048\n" \
qH "v[80:81],v[64:65],v[40:41],v[80:81]" qR qH "v[82:83],v[64:65],v[42:43],v[82:83]" qR qH "v[84:85],v[64:65],v[44:45],v[84:85]" qR qH "v[86:87],v[64:65],v[46:47],v[86:87]" qR \
qF "v[124:127],v2,s[84:87],s76 offen offset:3072 sc1\n" qH "v[88:89],v[68:69],v[40:41],v[88:89]" qR qH "v[90:91],v[68:69],v[42:43],v[90:91]" qR qH "v[92:93],v[68:69],v[44:45],v[92:93]" qR \
qH "v[94:95],v[68:69],v[46:47],v[94:95]" qR qH "v[96:97],v[72:73],v[40:41],v[208:209]" qR qH "v[98:99],v[72:73],v[42:43],v[210:211]" qR qH "v[100:101],v[72:73],v[44:45],v[212:213]" qR \
qH "v[102:103],v[72:73],v[46:47],v[214:215]" qR qH "v[104:105],v[76:77],v[40:41],v[248:249]" qR qH "v[106:107],v[76:77],v[42:43],v[250:251]" qR qH "v[108:109],v[76:77],v[44:45],v[252:253]" qR \
qH "v[110:111],v[76:77],v[46:47],v[254:255]" qR qQ "v1,v[124:127] offset:3072\n" qI "v[80:81],v[80:81],v[66:67]" qS qI "v[82:83],v[82:83],v[66:67]" qS \
qI "v[84:85],v[84:85],v[66:67]" qS qI "v[86:87],v[86:87],v[66:67]" qS qI "v[88:89],v[88:89],v[70:71]" qS qI "v[90:91],v[90:91],v[70:71]" qS \
qI "v[92:93],v[92:93],v[70:71]" qS qI "v[94:95],v[94:95],v[70:71]" qS qI "v[96:97],v[96:97],v[74:75]" qS qI "v[98:99],v[98:99],v[74:75]" qS \
qI "v[100:101],v[100:101],v[74:75]" qS qI "v[102:103],v[102:103],v[74:75]" qS qI "v[104:105],v[104:105],v[78:79]" qS qI "v[106:107],v[106:107],v[78:79]" qS \
qI "v[108:109],v[108:109],v[78:79]" qS qI "v[110:111],v[110:111],v[78:79]" qS qJ "v112,v80,v81\n" qJ "v113,v82,v83\n" \
qJ "v114,v84,v85\n" qJ "v115,v86,v87\n" qJ "v116,v88,v89\n" qJ "v117,v90,v91\n" \
qJ "v118,v92,v93\n" qJ "v119,v94,v95\n" qJ "v120,v96,v97\n" qJ "v121,v98,v99\n" \
qJ "v122,v100,v101\n" qJ "v123,v102,v103\n" qJ "v124,v104,v105\n" qJ "v125,v106,v107\n" \
qJ "v126,v108,v109\n" qJ "v127,v110,v111\n" qK "v112,v114\n" qK "v113,v115\n" \
qK "v116,v118\n" qK "v117,v119\n" qK "v120,v122\n" qK "v121,v123\n" \
qK "v124,v126\n" qK "v125,v127\n" qG "v80,a0\n" qG "v81,a1\n" \
qG "v82,a2\n" qG "v83,a3\n" qG "v88,a8\n" qG "v89,a9\n" \
qG "v90,a10\n" qG "v91,a11\n" qG "v96,a16\n" qG "v97,a17\n" \
qG "v98,a18\n" qG "v99,a19\n" qG "v104,a24\n" qG "v105,a25\n" \
qG "v106,a26\n" qG "v107,a27\n" qH "v[80:81],v[48:49],v[8:9],v[80:81]" qR qH "v[82:83],v[48:49],v[10:11],v[82:83]" qR \
qH "v[88:89],v[48:49],v[16:17],v[88:89]" qR qH "v[90:91],v[48:49],v[18:19],v[90:91]" qR qH "v[96:97],v[48:49],v[24:25],v[96:97]" qR qH "v[98:99],v[48:49],v[26:27],v[98:99]" qR \
qH "v[104:105],v[48:49],v[32:33],v[104:105]" qR qH "v[106:107],v[48:49],v[34:35],v[106:107]" qR "v_mul_f32 v6,v51,v51\n" qG "v84,a4\n" \
qG "v85,a5\n" qG "v86,a6\n" qG "v87,a7\n" qG "v92,a12\n" \
qG "v93,a13\n" qG "v94,a14\n" qG "v95,a15\n" qG "v100,a20\n" \
qG "v101,a21\n" qG "v102,a22\n" qG "v103,a23\n" qG "v108,a28\n" \
qG "v109,a29\n" qG "v110,a30\n" qG "v111,a31\n" qF "v[112:115],v2,s[84:87],s56 offen offset:0 sc1\n" \
qH "v[84:85],v[48:49],v[12:13],v[84:85]" qR qH "v[86:87],v[48:49],v[14:15],v[86:87]" qR qI "v[80:81],v[80:81],v[6:7]" qT qI "v[82:83],v[82:83],v[6:7]" qT \
qH "v[92:93],v[48:49],v[20:21],v[92:93]" qR qH "v[94:95],v[48:49],v[22:23],v[94:95]" qR qI "v[88:89],v[88:89],v[6:7]" qT qI "v[90:91],v[90:91],v[6:7]" qT \
qF "v[116:119],v2,s[84:87],s56 offen offset:1024 sc1\n" qH "v[100:101],v[48:49],v[28:29],v[100:101]" qR qH "v[102:103],v[48:49],v[30:31],v[102:103]" qR qI "v[96:97],v[96:97],v[6:7]" qT \
qI "v[98:99],v[98:99],v[6:7]" qT qH "v[108:109],v[48:49],v[36:37],v[108:109]" qR qH "v[110:111],v[48:49],v[38:39],v[110:111]" qR qI "v[104:105],v[104:105],v[6:7]" qT \
qI "v[106:107],v[106:107],v[6:7]" qT qF "v[120:123],v2,s[84:87],s56 offen offset:2048 sc1\n" qI "v[80:81],v[80:81],v[84:85]\n" qI "v[82:83],v[82:83],v[86:87]\n" \
qI "v[88:89],v[88:89],v[92:93]\n" qI "v[90:91],v[90:91],v[94:95]\n" qI "v[96:97],v[96:97],v[100:101]\n" qI "v[98:99],v[98:99],v[102:103]\n" \
qI "v[104:105],v[104:105],v[108:109]\n" qI "v[106:107],v[106:107],v[110:111]\n" qF "v[124:127],v2,s[84:87],s56 offen offset:3072 sc1\n" qJ "v128,v80,v81\n" \
qJ "v129,v82,v83\n" qJ "v130,v88,v89\n" qJ "v131,v90,v91\n" qJ "v132,v96,v97\n" \
qJ "v133,v98,v99\n" qJ "v134,v104,v105\n" qJ "v135,v106,v107\n" qG "v80,a40\n" \
qG "v81,a41\n" qG "v82,a42\n" qG "v83,a43\n" qG "v88,a48\n" \
qG "v89,a49\n" qG "v90,a50\n" qG "v91,a51\n" qG "v96,a56\n" \
qG "v97,a57\n" qG "v98,a58\n" qG "v99,a59\n" qG "v104,a64\n" \
qG "v105,a65\n" qG "v106,a66\n" qG "v107,a67\n" qH "v[80:81],v[52:53],v[8:9],v[80:81]" qR \
qH "v[82:83],v[52:53],v[10:11],v[82:83]" qR qH "v[88:89],v[52:53],v[16:17],v[88:89]" qR qH "v[90:91],v[52:53],v[18:19],v[90:91]" qR qH "v[96:97],v[52:53],v[24:25],v[96:97]" qR \
qH "v[98:99],v[52:53],v[26:27],v[98:99]" qR qH "v[104:105],v[52:53],v[32:33],v[104:105]" qR qH "v[106:107],v[52:53],v[34:35],v[106:107]" qR "v_mul_f32 v7,v55,v55\n" \
qG "v84,a44\n" qG "v85,a45\n" qG "v86,a46\n" qG "v87,a47\n" \
qG "v92,a52\n" qG "v93,a53\n" qG "v94,a54\n" qG "v95,a55\n" \
qG "v100,a60\n" qG "v101,a61\n" qG "v102,a62\n" qG "v103,a63\n" \
qG "v108,a68\n" qG "v109,a69\n" qG "v110,a70\n" qG "v111,a71\n" \
qH "v[84:85],v[52:53],v[12:13],v[84:85]" qR qH "v[86:87],v[52:53],v[14:15],v[86:87]" qR qI "v[80:81],v[80:81],v[6:7]" qS qI "v[82:83],v[82:83],v[6:7]" qS \
qH "v[92:93],v[52:53],v[20:21],v[92:93]" qR qH "v[94:95],v[52:53],v[22:23],v[94:95]" qR qI "v[88:89],v[88:89],v[6:7]" qS qI "v[90:91],v[90:91],v[6:7]" qS \
qH "v[100:101],v[52:53],v[28:29],v[100:101]" qR qH "v[102:103],v[52:53],v[30:31],v[102:103]" qR qI "v[96:97],v[96:97],v[6:7]" qS qI "v[98:99],v[98:99],v[6:7]" qS \
qH "v[108:109],v[52:53],v[36:37],v[108:109]" qR qH "v[110:111],v[52:53],v[38:39],v[110:111]" qR qI "v[104:105],v[104:105],v[6:7]" qS qI "v[106:107],v[106:107],v[6:7]" qS \
qI "v[80:81],v[80:81],v[84:85]\n" qI "v[82:83],v[82:83],v[86:87]\n" qI "v[88:89],v[88:89],v[92:93]\n" qI "v[90:91],v[90:91],v[94:95]\n" \
qI "v[96:97],v[96:97],v[100:101]\n" qI "v[98:99],v[98:99],v[102:103]\n" qI "v[104:105],v[104:105],v[108:109]\n" qI "v[106:107],v[106:107],v[110:111]\n" \
qJ "v136,v80,v81\n" qJ "v137,v82,v83\n" qJ "v138,v88,v89\n" qJ "v139,v90,v91\n" \
qJ "v140,v96,v97\n" qJ "v141,v98,v99\n" qJ "v142,v104,v105\n" qJ "v143,v106,v107\n" \
qG "v80,a80\n" qG "v81,a81\n" qG "v82,a82\n" qG "v83,a83\n" \
qG "v88,a88\n" qG "v89,a89\n" qG "v90,a90\n" qG "v91,a91\n" \
qG "v96,a96\n" qG "v97,a97\n" qG "v98,a98\n" qG "v99,a99\n" \
qG "v104,a104\n" qG "v105,a105\n" qG "v106,a106\n" qG "v107,a107\n" \
qH "v[80:81],v[56:57],v[8:9],v[80:81]" qR qH "v[82:83],v[56:57],v[10:11],v[82:83]" qR qH "v[88:89],v[56:57],v[16:17],v[88:89]" qR qH "v[90:91],v[56:57],v[18:19],v[90:91]" qR \
qH "v[96:97],v[56:57],v[24:25],v[96:97]" qR qH "v[98:99],v[56:57],v[26:27],v[98:99]" qR qH "v[104:105],v[56:57],v[32:33],v[104:105]" qR qH "v[106:107],v[56:57],v[34:35],v[106:107]" qR \
"v_mul_f32 v6,v59,v59\n" qG "v84,a84\n" qG "v85,a85\n" qG "v86,a86\n" \
qG "v87,a87\n" qG "v92,a92\n" qG "v93,a93\n" qG "v94,a94\n" \
qG "v95,a95\n" qG "v100,a100\n" qG "v101,a101\n" qG "v102,a102\n" \
qG "v103,a103\n" qG "v108,a108\n" qG "v109,a109\n" qG "v110,a110\n" \
qG "v111,a111\n" qH "v[84:85],v[56:57],v[12:13],v[84:85]" qR qH "v[86:87],v[56:57],v[14:15],v[86:87]" qR qI "v[80:81],v[80:81],v[6:7]" qT \
qI "v[82:83],v[82:83],v[6:7]" qT qH "v[92:93],v[56:57],v[20:21],v[92:93]" qR qH "v[94:95],v[56:57],v[22:23],v[94:95]" qR qI "v[88:89],v[88:89],v[6:7]" qT \
qI "v[90:91],v[90:91],v[6:7]" qT qH "v[100:101],v[56:57],v[28:29],v[100:101]" qR qH "v[102:103],v[56:57],v[30:31],v[102:103]" qR qI "v[96:97],v[96:97],v[6:7]" qT \
qI "v[98:99],v[98:99],v[6:7]" qT qH "v[108:109],v[56:57],v[36:37],v[108:109]" qR qH "v[110:111],v[56:57],v[38:39],v[110:111]" qR qI "v[104:105],v[104:105],v[6:7]" qT \
qI "v[106:107],v[106:107],v[6:7]" qT qQ "v1,v[112:115] offset:4096\n" qI "v[80:81],v[80:81],v[84:85]\n" qI "v[82:83],v[82:83],v[86:87]\n" \
qI "v[88:89],v[88:89],v[92:93]\n" qI "v[90:91],v[90:91],v[94:95]\n" qI "v[96:97],v[96:97],v[100:101]\n" qI "v[98:99],v[98:99],v[102:103]\n" \
qI "v[104:105],v[104:105],v[108:109]\n" qI "v[106:107],v[106:107],v[110:111]\n" qQ "v1,v[116:119] offset:5120\n" qJ "v144,v80,v81\n" \
qJ "v145,v82,v83\n" qJ "v146,v88,v89\n" qJ "v147,v90,v91\n" qJ "v148,v96,v97\n" \
qJ "v149,v98,v99\n" qJ "v150,v104,v105\n" qJ "v151,v106,v107\n" qQ "v1,v[120:123] offset:6144\n" \
qK "v128,v130\n" qK "v129,v131\n" qQ "v1,v[124:127] offset:7168\n" qK "v132,v134\n" \
qK "v133,v135\n" qK "v136,v138\n" qK "v137,v139\n" qK "v140,v142\n" \
qK "v141,v143\n" qK "v144,v146\n" qK "v145,v147\n" qK "v148,v150\n" \
qK "v149,v151\n" "v_mbcnt_lo_u32_b32 v82,-1,0\n" "v_mbcnt_hi_u32_b32 v82,-1,v82\n" "v_lshlrev_b32 v82,4,v82\n" \
"v_and_b32 v1,48,v82\n" "v_lshrrev_b32 v1,1,v1\n" "v_mov_b32 v3,s75\n" "v_xor_b32 v2,0x9e3779b9,v3\n" \
qO "vmcnt(0) lgkmcnt(0)\n" "s_mov_b64 exec,1\n" "buffer_store_dwordx2 v[2:3],v1,s[84:87],s77 offen sc1\n" "s_mov_b64 exec,-1\n" \
qG "v80,a120\n" qG "v81,a121\n" qG "v82,a122\n" qG "v83,a123\n" \
qG "v88,a128\n" qG "v89,a129\n" qG "v90,a130\n" qG "v91,a131\n" \
qG "v96,a136\n" qG "v97,a137\n" qG "v98,a138\n" qG "v99,a139\n" \
qG "v104,a144\n" qG "v105,a145\n" qG "v106,a146\n" qG "v107,a147\n" \
qH "v[80:81],v[60:61],v[8:9],v[80:81]" qR qH "v[82:83],v[60:61],v[10:11],v[82:83]" qR qH "v[88:89],v[60:61],v[16:17],v[88:89]" qR qH "v[90:91],v[60:61],v[18:19],v[90:91]" qR \
qH "v[96:97],v[60:61],v[24:25],v[96:97]" qR qH "v[98:99],v[60:61],v[26:27],v[98:99]" qR qH "v[104:105],v[60:61],v[32:33],v[104:105]" qR qH "v[106:107],v[60:61],v[34:35],v[106:107]" qR \
"v_mul_f32 v7,v63,v63\n" qG "v84,a124\n" qG "v85,a125\n" qG "v86,a126\n" \
qG "v87,a127\n" qG "v92,a132\n" qG "v93,a133\n" qG "v94,a134\n" \
qG "v95,a135\n" qG "v100,a140\n" qG "v101,a141\n" qG "v102,a142\n" \
qG "v103,a143\n" qG "v108,a148\n" qG "v109,a149\n" qG "v110,a150\n" \
qG "v111,a151\n" qH "v[84:85],v[60:61],v[12:13],v[84:85]" qR qH "v[86:87],v[60:61],v[14:15],v[86:87]" qR qI "v[80:81],v[80:81],v[6:7]" qS \
qI "v[82:83],v[82:83],v[6:7]" qS qH "v[92:93],v[60:61],v[20:21],v[92:93]" qR qH "v[94:95],v[60:61],v[22:23],v[94:95]" qR qI "v[88:89],v[88:89],v[6:7]" qS \
qI "v[90:91],v[90:91],v[6:7]" qS qH "v[100:101],v[60:61],v[28:29],v[100:101]" qR qH "v[102:103],v[60:61],v[30:31],v[102:103]" qR qI "v[96:97],v[96:97],v[6:7]" qS \
qI "v[98:99],v[98:99],v[6:7]" qS qH "v[108:109],v[60:61],v[36:37],v[108:109]" qR qH "v[110:111],v[60:61],v[38:39],v[110:111]" qR qI "v[104:105],v[104:105],v[6:7]" qS \
qI "v[106:107],v[106:107],v[6:7]" qS qI "v[80:81],v[80:81],v[84:85]\n" qI "v[82:83],v[82:83],v[86:87]\n" qI "v[88:89],v[88:89],v[92:93]\n" \
qI "v[90:91],v[90:91],v[94:95]\n" qI "v[96:97],v[96:97],v[100:101]\n" qI "v[98:99],v[98:99],v[102:103]\n" qI "v[104:105],v[104:105],v[108:109]\n" \
qI "v[106:107],v[106:107],v[110:111]\n" qJ "v152,v80,v81\n" qJ "v153,v82,v83\n" qJ "v154,v88,v89\n" \
qJ "v155,v90,v91\n" qJ "v156,v96,v97\n" qJ "v157,v98,v99\n" qJ "v158,v104,v105\n" \
qJ "v159,v106,v107\n" qG "v80,a160\n" qG "v81,a161\n" qG "v82,a162\n" \
qG "v83,a163\n" qG "v88,a168\n" qG "v89,a169\n" qG "v90,a170\n" \
qG "v91,a171\n" qG "v96,a176\n" qG "v97,a177\n" qG "v98,a178\n" \
qG "v99,a179\n" qG "v104,a184\n" qG "v105,a185\n" qG "v106,a186\n" \
qG "v107,a187\n" qH "v[80:81],v[64:65],v[8:9],v[80:81]" qR qH "v[82:83],v[64:65],v[10:11],v[82:83]" qR qH "v[88:89],v[64:65],v[16:17],v[88:89]" qR \
qH "v[90:91],v[64:65],v[18:19],v[90:91]" qR qH "v[96:97],v[64:65],v[24:25],v[96:97]" qR qH "v[98:99],v[64:65],v[26:27],v[98:99]" qR qH "v[104:105],v[64:65],v[32:33],v[104:105]" qR \
qH "v[106:107],v[64:65],v[34:35],v[106:107]" qR "v_mul_f32 v6,v67,v67\n" qG "v84,a164\n" qG "v85,a165\n" \
qG "v86,a166\n" qG "v87,a167\n" qG "v92,a172\n" qG "v93,a173\n" \
qG "v94,a174\n" qG "v95,a175\n" qG "v100,a180\n" qG "v101,a181\n" \
qG "v102,a182\n" qG "v103,a183\n" qG "v108,a188\n" qG "v109,a189\n" \
qG "v110,a190\n" qG "v111,a191\n" qH "v[84:85],v[64:65],v[12:13],v[84:85]" qR qH "v[86:87],v[64:65],v[14:15],v[86:87]" qR \
qI "v[80:81],v[80:81],v[6:7]" qT qI "v[82:83],v[82:83],v[6:7]" qT qH "v[92:93],v[64:65],v[20:21],v[92:93]" qR qH "v[94:95],v[64:65],v[22:23],v[94:95]" qR \
qI "v[88:89],v[88:89],v[6:7]" qT qI "v[90:91],v[90:91],v[6:7]" qT qH "v[100:101],v[64:65],v[28:29],v[100:101]" qR qH "v[102:103],v[64:65],v[30:31],v[102:103]" qR \
qI "v[96:97],v[96:97],v[6:7]" qT qI "v[98:99],v[98:99],v[6:7]" qT qH "v[108:109],v[64:65],v[36:37],v[108:109]" qR qH "v[110:111],v[64:65],v[38:39],v[110:111]" qR \
qI "v[104:105],v[104:105],v[6:7]" qT qI "v[106:107],v[106:107],v[6:7]" qT qI "v[80:81],v[80:81],v[84:85]\n" qI "v[82:83],v[82:83],v[86:87]\n" \
qI "v[88:89],v[88:89],v[92:93]\n" qI "v[90:91],v[90:91],v[94:95]\n" qI "v[96:97],v[96:97],v[100:101]\n" qI "v[98:99],v[98:99],v[102:103]\n" \
qI "v[104:105],v[104:105],v[108:109]\n" qI "v[106:107],v[106:107],v[110:111]\n" qJ "v160,v80,v81\n" qJ "v161,v82,v83\n" \
qJ "v162,v88,v89\n" qJ "v163,v90,v91\n" qJ "v164,v96,v97\n" qJ "v165,v98,v99\n" \
qJ "v166,v104,v105\n" qJ "v167,v106,v107\n" qG "v80,a200\n" qG "v81,a201\n" \
qG "v82,a202\n" qG "v83,a203\n" qG "v88,a208\n" qG "v89,a209\n" \
qG "v90,a210\n" qG "v91,a211\n" qG "v96,a216\n" qG "v97,a217\n" \
qG "v98,a218\n" qG "v99,a219\n" qG "v104,a224\n" qG "v105,a225\n" \
qG "v106,a226\n" qG "v107,a227\n" qH "v[80:81],v[68:69],v[8:9],v[80:81]" qR qH "v[82:83],v[68:69],v[10:11],v[82:83]" qR \
qH "v[88:89],v[68:69],v[16:17],v[88:89]" qR qH "v[90:91],v[68:69],v[18:19],v[90:91]" qR qH "v[96:97],v[68:69],v[24:25],v[96:97]" qR qH "v[98:99],v[68:69],v[26:27],v[98:99]" qR \
qH "v[104:105],v[68:69],v[32:33],v[104:105]" qR qH "v[106:107],v[68:69],v[34:35],v[106:107]" qR "v_mul_f32 v7,v71,v71\n" qG "v84,a204\n" \
qG "v85,a205\n" qG "v86,a206\n" qG "v87,a207\n" qG "v92,a212\n" \
qG "v93,a213\n" qG "v94,a214\n" qG "v95,a215\n" qG "v100,a220\n" \
qG "v101,a221\n" qG "v102,a222\n" qG "v103,a223\n" qG "v108,a228\n" \
qG "v109,a229\n" qG "v110,a230\n" qG "v111,a231\n" qH "v[84:85],v[68:69],v[12:13],v[84:85]" qR \
qH "v[86:87],v[68:69],v[14:15],v[86:87]" qR qI "v[80:81],v[80:81],v[6:7]" qS qI "v[82:83],v[82:83],v[6:7]" qS qH "v[92:93],v[68:69],v[20:21],v[92:93]" qR \
qH "v[94:95],v[68:69],v[22:23],v[94:95]" qR qI "v[88:89],v[88:89],v[6:7]" qS qI "v[90:91],v[90:91],v[6:7]" qS qH "v[100:101],v[68:69],v[28:29],v[100:101]" qR \
qH "v[102:103],v[68:69],v[30:31],v[102:103]" qR qI "v[96:97],v[96:97],v[6:7]" qS qI "v[98:99],v[98:99],v[6:7]" qS qH "v[108:109],v[68:69],v[36:37],v[108:109]" qR \
qH "v[110:111],v[68:69],v[38:39],v[110:111]" qR qI "v[104:105],v[104:105],v[6:7]" qS qI "v[106:107],v[106:107],v[6:7]" qS qI "v[80:81],v[80:81],v[84:85]\n" \
qI "v[82:83],v[82:83],v[86:87]\n" qI "v[88:89],v[88:89],v[92:93]\n" qI "v[90:91],v[90:91],v[94:95]\n" qI "v[96:97],v[96:97],v[100:101]\n" \
qI "v[98:99],v[98:99],v[102:103]\n" qI "v[104:105],v[104:105],v[108:109]\n" qI "v[106:107],v[106:107],v[110:111]\n" qJ "v168,v80,v81\n" \
qJ "v169,v82,v83\n" qJ "v170,v88,v89\n" qJ "v171,v90,v91\n" qJ "v172,v96,v97\n" \
qJ "v173,v98,v99\n" qJ "v174,v104,v105\n" qJ "v175,v106,v107\n" qK "v152,v154\n" \
qK "v153,v155\n" qK "v156,v158\n" qK "v157,v159\n" qK "v160,v162\n" \
qK "v161,v163\n" qK "v164,v166\n" qK "v165,v167\n" qK "v168,v170\n" \
qK "v169,v171\n" qK "v172,v174\n" qK "v173,v175\n" "s_bitcmp1_b32 s59,9\n" \
"s_cbranch_scc1 EPP0_%=\n" "s_xor_b32 s94,s75,0x9e3779b9\n" "s_mov_b32 s95,s75\n" "s_mov_b32 s58,0x400000\n" \
"EPQ0_%=:\n" "buffer_load_dwordx2 v[2:3],v1,s[84:87],s78 offen sc1\n" qO "vmcnt(0)\n" "v_cmp_ne_u64 vcc,s[94:95],v[2:3]\n" \
"s_nop 3\n" "s_cmp_eq_u64 vcc,0\n" "s_cbranch_scc1 EPP0_%=\n" "s_sleep 1\n" \
"s_sub_u32 s58,s58,1\n" "s_cmp_eq_u32 s58,0\n" "s_cbranch_scc0 EPQ0_%=\n" "EPP0_%=:\n" \
"v_mbcnt_lo_u32_b32 v82,-1,0\n" "v_mbcnt_hi_u32_b32 v82,-1,v82\n" "v_lshlrev_b32 v82,4,v82\n" "v_mov_b32 v47,v82\n" \
"v_add_u32 v1,0,v82\n" "v_lshrrev_b32 v2,6,v82\n" "v_lshlrev_b32 v2,6,v2\n" "v_bfe_u32 v3,v82,4,2\n" \
"v_lshrrev_b32 v83,8,v82\n" "v_xor_b32 v83,v83,v3\n" "v_lshl_add_u32 v3,v3,4,v2\n" "v_lshl_add_u32 v2,v83,4,v2\n" \
"v_add_u32 v2,16384,v2\n" "v_add_u32 v3,49152,v3\n" "s_bitcmp1_b32 s59,9\n" "s_cbranch_scc1 EPZ0_%=\n" \
"s_sub_u32 s57,s76,1048576\n" qE "a[0:3],v1,s[84:87],s57 offen sc1\n" qN "s57,s57,4096\n" qE "a[4:7],v1,s[84:87],s57 offen sc1\n" \
qN "s57,s57,4096\n" qE "a[8:11],v1,s[84:87],s57 offen sc1\n" qN "s57,s57,4096\n" qE "a[12:15],v1,s[84:87],s57 offen sc1\n" \
qN "s57,s57,4096\n" qE "a[16:19],v1,s[84:87],s57 offen sc1\n" qN "s57,s57,4096\n" qE "a[20:23],v1,s[84:87],s57 offen sc1\n" \
qN "s57,s57,4096\n" qE "a[24:27],v1,s[84:87],s57 offen sc1\n" qN "s57,s57,4096\n" qE "a[28:31],v1,s[84:87],s57 offen sc1\n" \
"s_branch EPL0_%=\n" "EPZ0_%=:\n" "v_accvgpr_write_b32 a0,0\n" "v_accvgpr_write_b32 a1,0\n" \
"v_accvgpr_write_b32 a2,0\n" "v_accvgpr_write_b32 a3,0\n" "v_accvgpr_write_b32 a4,0\n" "v_accvgpr_write_b32 a5,0\n" \
"v_accvgpr_write_b32 a6,0\n" "v_accvgpr_write_b32 a7,0\n" "v_accvgpr_write_b32 a8,0\n" "v_accvgpr_write_b32 a9,0\n" \
"v_accvgpr_write_b32 a10,0\n" "v_accvgpr_write_b32 a11,0\n" "v_accvgpr_write_b32 a12,0\n" "v_accvgpr_write_b32 a13,0\n" \
"v_accvgpr_write_b32 a14,0\n" "v_accvgpr_write_b32 a15,0\n" "v_accvgpr_write_b32 a16,0\n" "v_accvgpr_write_b32 a17,0\n" \
"v_accvgpr_write_b32 a18,0\n" "v_accvgpr_write_b32 a19,0\n" "v_accvgpr_write_b32 a20,0\n" "v_accvgpr_write_b32 a21,0\n" \
"v_accvgpr_write_b32 a22,0\n" "v_accvgpr_write_b32 a23,0\n" "v_accvgpr_write_b32 a24,0\n" "v_accvgpr_write_b32 a25,0\n" \
"v_accvgpr_write_b32 a26,0\n" "v_accvgpr_write_b32 a27,0\n" "v_accvgpr_write_b32 a28,0\n" "v_accvgpr_write_b32 a29,0\n" \
"v_accvgpr_write_b32 a30,0\n" "v_accvgpr_write_b32 a31,0\n" "EPL0_%=:\n" qG "v80,a240\n" \
qG "v81,a241\n" qG "v82,a242\n" qG "v83,a243\n" qG "v88,a248\n" \
qG "v89,a249\n" qG "v90,a250\n" qG "v91,a251\n" qH "v[80:81],v[72:73],v[8:9],v[80:81]" qR \
qH "v[82:83],v[72:73],v[10:11],v[82:83]" qR qH "v[88:89],v[72:73],v[16:17],v[88:89]" qR qH "v[90:91],v[72:73],v[18:19],v[90:91]" qR qH "v[96:97],v[72:73],v[24:25],v[192:193]" qR \
qH "v[98:99],v[72:73],v[26:27],v[194:195]" qR qH "v[104:105],v[72:73],v[32:33],v[200:201]" qR qH "v[106:107],v[72:73],v[34:35],v[202:203]" qR "v_mul_f32 v6,v75,v75\n" \
qG "v84,a244\n" qG "v85,a245\n" qG "v86,a246\n" qG "v87,a247\n" \
qG "v92,a252\n" qG "v93,a253\n" qG "v94,a254\n" qG "v95,a255\n" \
qH "v[84:85],v[72:73],v[12:13],v[84:85]" qR qH "v[86:87],v[72:73],v[14:15],v[86:87]" qR qI "v[80:81],v[80:81],v[6:7]" qT qI "v[82:83],v[82:83],v[6:7]" qT \
qH "v[92:93],v[72:73],v[20:21],v[92:93]" qR qH "v[94:95],v[72:73],v[22:23],v[94:95]" qR qI "v[88:89],v[88:89],v[6:7]" qT qI "v[90:91],v[90:91],v[6:7]" qT \
qH "v[100:101],v[72:73],v[28:29],v[196:197]" qR qH "v[102:103],v[72:73],v[30:31],v[198:199]" qR qI "v[96:97],v[96:97],v[6:7]" qT qI "v[98:99],v[98:99],v[6:7]" qT \
qH "v[108:109],v[72:73],v[36:37],v[204:205]" qR qH "v[110:111],v[72:73],v[38:39],v[206:207]" qR qI "v[104:105],v[104:105],v[6:7]" qT qI "v[106:107],v[106:107],v[6:7]" qT \
qI "v[80:81],v[80:81],v[84:85]\n" qI "v[82:83],v[82:83],v[86:87]\n" qI "v[88:89],v[88:89],v[92:93]\n" qI "v[90:91],v[90:91],v[94:95]\n" \
qI "v[96:97],v[96:97],v[100:101]\n" qI "v[98:99],v[98:99],v[102:103]\n" qI "v[104:105],v[104:105],v[108:109]\n" qI "v[106:107],v[106:107],v[110:111]\n" \
qJ "v176,v80,v81\n" qJ "v177,v82,v83\n" qJ "v178,v88,v89\n" qJ "v179,v90,v91\n" \
qJ "v180,v96,v97\n" qJ "v181,v98,v99\n" qJ "v182,v104,v105\n" qJ "v183,v106,v107\n" \
qH "v[80:81],v[76:77],v[8:9],v[216:217]" qR qH "v[82:83],v[76:77],v[10:11],v[218:219]" qR qH "v[88:89],v[76:77],v[16:17],v[224:225]" qR qH "v[90:91],v[76:77],v[18:19],v[226:227]" qR \
qH "v[96:97],v[76:77],v[24:25],v[232:233]" qR qH "v[98:99],v[76:77],v[26:27],v[234:235]" qR qH "v[104:105],v[76:77],v[32:33],v[240:241]" qR qH "v[106:107],v[76:77],v[34:35],v[242:243]" qR \
"v_mul_f32 v7,v79,v79\n" qH "v[84:85],v[76:77],v[12:13],v[220:221]" qR qH "v[86:87],v[76:77],v[14:15],v[222:223]" qR qI "v[80:81],v[80:81],v[6:7]" qS \
qI "v[82:83],v[82:83],v[6:7]" qS qH "v[92:93],v[76:77],v[20:21],v[228:229]" qR qH "v[94:95],v[76:77],v[22:23],v[230:231]" qR qI "v[88:89],v[88:89],v[6:7]" qS \
qI "v[90:91],v[90:91],v[6:7]" qS qH "v[100:101],v[76:77],v[28:29],v[236:237]" qR qH "v[102:103],v[76:77],v[30:31],v[238:239]" qR qI "v[96:97],v[96:97],v[6:7]" qS \
qI "v[98:99],v[98:99],v[6:7]" qS qH "v[108:109],v[76:77],v[36:37],v[244:245]" qR qH "v[110:111],v[76:77],v[38:39],v[246:247]" qR qI "v[104:105],v[104:105],v[6:7]" qS \
qI "v[106:107],v[106:107],v[6:7]" qS qI "v[80:81],v[80:81],v[84:85]\n" qI "v[82:83],v[82:83],v[86:87]\n" qI "v[88:89],v[88:89],v[92:93]\n" \
qI "v[90:91],v[90:91],v[94:95]\n" qI "v[96:97],v[96:97],v[100:101]\n" qI "v[98:99],v[98:99],v[102:103]\n" qI "v[104:105],v[104:105],v[108:109]\n" \
qI "v[106:107],v[106:107],v[110:111]\n" qJ "v184,v80,v81\n" qJ "v185,v82,v83\n" qJ "v186,v88,v89\n" \
qJ "v187,v90,v91\n" qJ "v188,v96,v97\n" qJ "v189,v98,v99\n" qJ "v190,v104,v105\n" \
qJ "v191,v106,v107\n" qK "v176,v178\n" qK "v177,v179\n" qK "v180,v182\n" \
qK "v181,v183\n" qK "v184,v186\n" qK "v185,v187\n" qK "v188,v190\n" \
qK "v189,v191\n" "v_and_b32 v83,0x1f0,v47\n" "v_lshrrev_b32 v86,9,v47\n" "v_bfe_u32 v87,v47,6,2\n" \
"v_xor_b32 v87,v87,v86\n" "v_lshlrev_b32 v87,4,v87\n" "v_lshl_add_u32 v40,v83,2,v87\n" "v_add_u32 v40,0,v40\n" \
"v_xor_b32 v41,32,v40\n" "v_bfe_u32 v87,v47,6,2\n" "v_lshl_add_u32 v87,v86,2,v87\n" "v_lshlrev_b32 v87,6,v87\n" \
"v_bfe_u32 v42,v47,8,1\n" "v_lshl_add_u32 v87,v42,5,v87\n" "v_bfe_u32 v42,v47,4,2\n" "v_lshl_add_u32 v42,v42,3,v87\n" \
"v_add_u32 v42,49152,v42\n" "v_lshl_add_u32 v43,v86,15,v83\n" "v_add_u32 v43,s81,v43\n" "v_lshrrev_b32 v44,4,v83\n" \
"v_lshlrev_b32 v87,2,v86\n" "v_sub_u32 v44,v44,v87\n" "v_and_b32 v83,0xf0,v47\n" "v_bfe_u32 v87,v47,8,1\n" \
"v_lshl_or_b32 v87,v87,1,v86\n" "v_lshl_add_u32 v45,v87,15,v83\n" "v_add_u32 v45,s79,v45\n" "v_add_u32 v46,0x20000,v45\n" \
"v_mov_b32 v47,v46\n" qO "vmcnt(0)\n" qQ "v2,a[0:3] offset:0\n" qQ "v2,a[4:7] offset:4096\n" \
qQ "v2,a[8:11] offset:8192\n" qQ "v2,a[12:15] offset:12288\n" qQ "v3,a[16:19] offset:0\n" qQ "v3,a[20:23] offset:4096\n" \
qQ "v3,a[24:27] offset:8192\n" qQ "v3,a[28:31] offset:12288\n" qO "lgkmcnt(0)\n" "s_barrier\n" \
"s_branch G1END_%=\n" "G1W1_%=:\n" qC "v[48:49],v32 offset:4096\n" qC "v[50:51],v32 offset:4352\n" \
qC "v[52:53],v32 offset:14336\n" qC "v[54:55],v32 offset:14592\n" qC "v[56:57],v33 offset:4096\n" qC "v[58:59],v33 offset:4352\n" \
qC "v[60:61],v33 offset:14336\n" qC "v[62:63],v33 offset:14592\n" qC "v[64:65],v34 offset:8192\n" qC "v[66:67],v34 offset:8448\n" \
qC "v[68:69],v34 offset:9216\n" qC "v[70:71],v34 offset:9472\n" qC "v[72:73],v32 offset:6144\n" qC "v[74:75],v32 offset:6400\n" \
qC "v[76:77],v33 offset:6144\n" qC "v[78:79],v33 offset:6400\n" qC "v[80:81],v32 offset:18432\n" qC "v[82:83],v32 offset:18688\n" \
qC "v[84:85],v33 offset:18432\n" qC "v[86:87],v33 offset:18688\n" qD "v[128:131],v38 offset:0\n" qD "v[132:135],v38 offset:2048\n" \
qD "v[136:139],v38 offset:4096\n" qD "v[140:143],v38 offset:6144\n" qD "v[144:147],v38 offset:8192\n" qD "v[148:151],v38 offset:10240\n" \
qD "v[152:155],v38 offset:12288\n" qD "v[156:159],v38 offset:14336\n" "s_mov_b32 s63,7\n" qO "lgkmcnt(0)\n" \
qA "a[0:3],v[48:51],v[128:131],0\n" qO "vmcnt(14)\n" qA "a[4:7],v[52:55],v[128:131],0\n" "s_barrier\n" \
qA "a[8:11],v[56:59],v[128:131],0\n" qC "v[88:89],v32 offset:24576\n" qN "m0,s62,61440\n" qA "a[12:15],v[60:63],v[128:131],0\n" \
qE "v41,s[44:47],s56" qU qA "a[16:19],v[64:67],v[128:131],0\n" qC "v[90:91],v32 offset:24832\n" qA "a[20:23],v[68:71],v[128:131],0\n" \
qN "m0,s62,63424\n" qA "a[24:27],v[72:75],v[128:131],0\n" qC "v[92:93],v32 offset:34816\n" qE "v41,s[44:47],s56 offen offset:64 lds\n" \
qA "a[28:31],v[76:79],v[128:131],0\n" qA "a[32:35],v[80:83],v[128:131],0\n" qC "v[94:95],v32 offset:35072\n" qN "m0,s62,65408\n" \
qA "a[36:39],v[84:87],v[128:131],0\n" qE "v41,s[44:47],s56 offen offset:128 lds\n" qA "v[8:11],v[48:51],v[28:31],0\n" qC "v[96:97],v33 offset:24576\n" \
qA "a[40:43],v[48:51],v[132:135],0\n" qN "m0,s62,67584\n" qA "a[44:47],v[52:55],v[132:135],0\n" qC "v[98:99],v33 offset:24832\n" \
qE "v42,s[48:51],s57" qU qA "a[48:51],v[56:59],v[132:135],0\n" qA "a[52:55],v[60:63],v[132:135],0\n" qC "v[100:101],v33 offset:34816\n" \
qN "m0,s62,69632\n" qA "a[56:59],v[64:67],v[132:135],0\n" qE "v43,s[52:55],s58" qU qA "a[60:63],v[68:71],v[132:135],0\n" \
qC "v[102:103],v33 offset:35072\n" qN "s56,s56,229376\n" qN "s57,s57,196608\n" qN "s58,s58,s60\n" \
qA "a[64:67],v[72:75],v[132:135],0\n" "s_min_u32 s56,s56,7110656\n" "s_min_u32 s57,s57,6094848\n" "s_min_u32 s58,s58,s72\n" \
qN "m0,s62,0\n" qA "a[68:71],v[76:79],v[132:135],0\n" qC "v[104:105],v34 offset:28672\n" qE "v41,s[44:47],s56" qU \
qA "a[72:75],v[80:83],v[132:135],0\n" qA "a[76:79],v[84:87],v[132:135],0\n" qC "v[106:107],v34 offset:28928\n" qN "m0,s62,1984\n" \
qA "a[80:83],v[48:51],v[136:139],0\n" qE "v41,s[44:47],s56 offen offset:64 lds\n" qA "a[84:87],v[52:55],v[136:139],0\n" qC "v[108:109],v34 offset:29696\n" \
qA "a[88:91],v[56:59],v[136:139],0\n" qN "m0,s62,3968\n" qA "a[92:95],v[60:63],v[136:139],0\n" qC "v[110:111],v34 offset:29952\n" \
qE "v41,s[44:47],s56 offen offset:128 lds\n" qA "a[96:99],v[64:67],v[136:139],0\n" qA "a[100:103],v[68:71],v[136:139],0\n" qC "v[112:113],v32 offset:26624\n" \
qN "m0,s62,6144\n" qA "v[12:15],v[52:55],v[28:31],0\n" qE "v42,s[48:51],s57" qU qA "a[104:107],v[72:75],v[136:139],0\n" \
qC "v[114:115],v32 offset:26880\n" qA "a[108:111],v[76:79],v[136:139],0\n" qN "m0,s62,8192\n" qA "a[112:115],v[80:83],v[136:139],0\n" \
qC "v[116:117],v33 offset:26624\n" qE "v43,s[52:55],s58" qU qA "a[116:119],v[84:87],v[136:139],0\n" qA "a[120:123],v[48:51],v[140:143],0\n" \
qC "v[118:119],v33 offset:26880\n" qN "s56,s56,229376\n" qA "a[124:127],v[52:55],v[140:143],0\n" qN "s57,s57,196608\n" \
qA "a[128:131],v[56:59],v[140:143],0\n" qC "v[120:121],v32 offset:38912\n" qN "s58,s58,s60\n" qA "a[132:135],v[60:63],v[140:143],0\n" \
"s_min_u32 s56,s56,7110656\n" qA "a[136:139],v[64:67],v[140:143],0\n" qC "v[122:123],v32 offset:39168\n" "s_min_u32 s57,s57,6094848\n" \
qA "a[140:143],v[68:71],v[140:143],0\n" "s_min_u32 s58,s58,s72\n" qA "a[144:147],v[72:75],v[140:143],0\n" qC "v[124:125],v33 offset:38912\n" \
qA "a[148:151],v[76:79],v[140:143],0\n" qA "a[152:155],v[80:83],v[140:143],0\n" qC "v[126:127],v33 offset:39168\n" qA "a[156:159],v[84:87],v[140:143],0\n" \
qA "a[160:163],v[48:51],v[144:147],0\n" qD "v[160:163],v39 offset:0\n" qA "a[164:167],v[52:55],v[144:147],0\n" qA "v[16:19],v[56:59],v[28:31],0\n" \
qD "v[164:167],v39 offset:2048\n" qA "a[168:171],v[56:59],v[144:147],0\n" qA "a[172:175],v[60:63],v[144:147],0\n" qD "v[168:171],v39 offset:4096\n" \
qA "a[176:179],v[64:67],v[144:147],0\n" qA "a[180:183],v[68:71],v[144:147],0\n" qD "v[172:175],v39 offset:6144\n" qA "a[184:187],v[72:75],v[144:147],0\n" \
qA "a[188:191],v[76:79],v[144:147],0\n" qD "v[176:179],v39 offset:8192\n" qA "a[192:195],v[80:83],v[144:147],0\n" qA "a[196:199],v[84:87],v[144:147],0\n" \
qD "v[180:183],v39 offset:10240\n" qA "a[200:203],v[48:51],v[148:151],0\n" qA "a[204:207],v[52:55],v[148:151],0\n" qD "v[184:187],v39 offset:12288\n" \
qA "a[208:211],v[56:59],v[148:151],0\n" qA "a[212:215],v[60:63],v[148:151],0\n" qD "v[188:191],v39 offset:14336\n" qA "a[216:219],v[64:67],v[148:151],0\n" \
qA "a[220:223],v[68:71],v[148:151],0\n" qA "a[224:227],v[72:75],v[148:151],0\n" qA "a[228:231],v[76:79],v[148:151],0\n" qA "v[20:23],v[60:63],v[28:31],0\n" \
qA "a[232:235],v[80:83],v[148:151],0\n" qA "a[236:239],v[84:87],v[148:151],0\n" qA "a[240:243],v[48:51],v[152:155],0\n" qA "a[244:247],v[52:55],v[152:155],0\n" \
qA "a[248:251],v[56:59],v[152:155],0\n" qA "a[252:255],v[60:63],v[152:155],0\n" qA "v[192:195],v[64:67],v[152:155],0\n" qA "v[196:199],v[68:71],v[152:155],0\n" \
qA "v[200:203],v[72:75],v[152:155],0\n" qA "v[204:207],v[76:79],v[152:155],0\n" qA "v[208:211],v[80:83],v[152:155],0\n" qA "v[212:215],v[84:87],v[152:155],0\n" \
qA "v[216:219],v[48:51],v[156:159],0\n" qA "v[220:223],v[52:55],v[156:159],0\n" qA "v[224:227],v[56:59],v[156:159],0\n" qA "v[228:231],v[60:63],v[156:159],0\n" \
qA "v[24:27],v[64:67],v[28:31],0\n" qA "v[232:235],v[64:67],v[156:159],0\n" qA "v[236:239],v[68:71],v[156:159],0\n" qA "v[240:243],v[72:75],v[156:159],0\n" \
qA "v[244:247],v[76:79],v[156:159],0\n" qA "v[248:251],v[80:83],v[156:159],0\n" qA "v[252:255],v[84:87],v[156:159],0\n" "G1L1_%=:\n" \
qO "lgkmcnt(0)\n" qA "a[0:3],v[88:91],v[160:163],a[0:3]\n" qO "vmcnt(11)\n" qA "a[4:7],v[92:95],v[160:163],a[4:7]\n" \
"s_barrier\n" qA "a[8:11],v[96:99],v[160:163],a[8:11]\n" qC "v[48:49],v35 offset:4096\n" qA "a[12:15],v[100:103],v[160:163],a[12:15]\n" \
qN "m0,s61,0\n" qA "a[16:19],v[104:107],v[160:163],a[16:19]\n" qC "v[50:51],v35 offset:4352\n" qA "a[20:23],v[108:111],v[160:163],a[20:23]\n" \
qE "v40,s[40:43],0" qU qA "a[24:27],v[112:115],v[160:163],a[24:27]\n" qC "v[52:53],v35 offset:14336\n" qA "a[28:31],v[116:119],v[160:163],a[28:31]\n" \
qN "m0,s61,1024\n" qA "a[32:35],v[120:123],v[160:163],a[32:35]\n" qC "v[54:55],v35 offset:14592\n" qA "a[36:39],v[124:127],v[160:163],a[36:39]\n" \
qE "v40,s[40:43],s65" qU qA "v[8:11],v[88:91],v[28:31],v[8:11]\n" qC "v[56:57],v36 offset:4096\n" qA "a[40:43],v[88:91],v[164:167],a[40:43]\n" \
qN "m0,s61,2048\n" qA "a[44:47],v[92:95],v[164:167],a[44:47]\n" qC "v[58:59],v36 offset:4352\n" qA "a[48:51],v[96:99],v[164:167],a[48:51]\n" \
qE "v40,s[40:43],s66" qU qA "a[52:55],v[100:103],v[164:167],a[52:55]\n" qC "v[60:61],v36 offset:14336\n" qA "a[56:59],v[104:107],v[164:167],a[56:59]\n" \
qN "m0,s61,3072\n" qA "a[60:63],v[108:111],v[164:167],a[60:63]\n" qC "v[62:63],v36 offset:14592\n" qA "a[64:67],v[112:115],v[164:167],a[64:67]\n" \
qE "v40,s[40:43],s67" qU qA "a[68:71],v[116:119],v[164:167],a[68:71]\n" qC "v[64:65],v37 offset:8192\n" qA "a[72:75],v[120:123],v[164:167],a[72:75]\n" \
qN "m0,s61,4096\n" qA "a[76:79],v[124:127],v[164:167],a[76:79]\n" qC "v[66:67],v37 offset:8448\n" qA "a[80:83],v[88:91],v[168:171],a[80:83]\n" \
qE "v40,s[40:43],s68" qU qA "a[84:87],v[92:95],v[168:171],a[84:87]\n" qC "v[68:69],v37 offset:9216\n" qA "a[88:91],v[96:99],v[168:171],a[88:91]\n" \
qN "m0,s61,5120\n" qA "a[92:95],v[100:103],v[168:171],a[92:95]\n" qC "v[70:71],v37 offset:9472\n" qA "a[96:99],v[104:107],v[168:171],a[96:99]\n" \
qE "v40,s[40:43],s69" qU qA "a[100:103],v[108:111],v[168:171],a[100:103]\n" qC "v[72:73],v35 offset:6144\n" qA "v[12:15],v[92:95],v[28:31],v[12:15]\n" \
qN "m0,s61,6144\n" qA "a[104:107],v[112:115],v[168:171],a[104:107]\n" qC "v[74:75],v35 offset:6400\n" qA "a[108:111],v[116:119],v[168:171],a[108:111]\n" \
qE "v40,s[40:43],s70" qU qA "a[112:115],v[120:123],v[168:171],a[112:115]\n" qC "v[76:77],v36 offset:6144\n" qA "a[116:119],v[124:127],v[168:171],a[116:119]\n" \
qN "m0,s61,7168\n" qA "a[120:123],v[88:91],v[172:175],a[120:123]\n" qC "v[78:79],v36 offset:6400\n" qA "a[124:127],v[92:95],v[172:175],a[124:127]\n" \
qE "v40,s[40:43],s71" qU qA "a[128:131],v[96:99],v[172:175],a[128:131]\n" qC "v[80:81],v35 offset:18432\n" qA "a[132:135],v[100:103],v[172:175],a[132:135]\n" \
qN "m0,s62,20480\n" qA "a[136:139],v[104:107],v[172:175],a[136:139]\n" qC "v[82:83],v35 offset:18688\n" qA "a[140:143],v[108:111],v[172:175],a[140:143]\n" \
qE "v41,s[44:47],s56" qU qA "a[144:147],v[112:115],v[172:175],a[144:147]\n" qC "v[84:85],v36 offset:18432\n" qA "a[148:151],v[116:119],v[172:175],a[148:151]\n" \
qN "m0,s62,22464\n" qA "a[152:155],v[120:123],v[172:175],a[152:155]\n" qC "v[86:87],v36 offset:18688\n" qA "a[156:159],v[124:127],v[172:175],a[156:159]\n" \
qE "v41,s[44:47],s56 offen offset:64 lds\n" qA "a[160:163],v[88:91],v[176:179],a[160:163]\n" qD "v[128:131],v38 offset:32768\n" qA "a[164:167],v[92:95],v[176:179],a[164:167]\n" \
qN "m0,s62,24448\n" qA "v[16:19],v[96:99],v[28:31],v[16:19]\n" qD "v[132:135],v38 offset:34816\n" qA "a[168:171],v[96:99],v[176:179],a[168:171]\n" \
qE "v41,s[44:47],s56 offen offset:128 lds\n" qA "a[172:175],v[100:103],v[176:179],a[172:175]\n" qD "v[136:139],v38 offset:36864\n" qA "a[176:179],v[104:107],v[176:179],a[176:179]\n" \
qN "m0,s62,26624\n" qA "a[180:183],v[108:111],v[176:179],a[180:183]\n" qD "v[140:143],v38 offset:38912\n" qA "a[184:187],v[112:115],v[176:179],a[184:187]\n" \
qE "v42,s[48:51],s57" qU qA "a[188:191],v[116:119],v[176:179],a[188:191]\n" qD "v[144:147],v38 offset:40960\n" qA "a[192:195],v[120:123],v[176:179],a[192:195]\n" \
qN "m0,s62,28672\n" qA "a[196:199],v[124:127],v[176:179],a[196:199]\n" qD "v[148:151],v38 offset:43008\n" qA "a[200:203],v[88:91],v[180:183],a[200:203]\n" \
qE "v43,s[52:55],s58" qU qA "a[204:207],v[92:95],v[180:183],a[204:207]\n" qD "v[152:155],v38 offset:45056\n" qA "a[208:211],v[96:99],v[180:183],a[208:211]\n" \
qA "a[212:215],v[100:103],v[180:183],a[212:215]\n" qD "v[156:159],v38 offset:47104\n" qA "a[216:219],v[104:107],v[180:183],a[216:219]\n" qA "a[220:223],v[108:111],v[180:183],a[220:223]\n" \
qA "a[224:227],v[112:115],v[180:183],a[224:227]\n" qA "a[228:231],v[116:119],v[180:183],a[228:231]\n" qN "s40,s40,128\n" qA "v[20:23],v[100:103],v[28:31],v[20:23]\n" \
"s_addc_u32 s41,s41,0\n" qA "a[232:235],v[120:123],v[180:183],a[232:235]\n" qN "s56,s56,229376\n" qA "a[236:239],v[124:127],v[180:183],a[236:239]\n" \
qN "s57,s57,196608\n" qA "a[240:243],v[88:91],v[184:187],a[240:243]\n" qN "s58,s58,s60\n" qA "a[244:247],v[92:95],v[184:187],a[244:247]\n" \
"s_min_u32 s56,s56,7110656\n" qA "a[248:251],v[96:99],v[184:187],a[248:251]\n" "s_min_u32 s57,s57,6094848\n" qA "a[252:255],v[100:103],v[184:187],a[252:255]\n" \
"s_min_u32 s58,s58,s72\n" qA "v[192:195],v[104:107],v[184:187],v[192:195]\n" qA "v[196:199],v[108:111],v[184:187],v[196:199]\n" qA "v[200:203],v[112:115],v[184:187],v[200:203]\n" \
qA "v[204:207],v[116:119],v[184:187],v[204:207]\n" qA "v[208:211],v[120:123],v[184:187],v[208:211]\n" qA "v[212:215],v[124:127],v[184:187],v[212:215]\n" qA "v[216:219],v[88:91],v[188:191],v[216:219]\n" \
qA "v[220:223],v[92:95],v[188:191],v[220:223]\n" qA "v[224:227],v[96:99],v[188:191],v[224:227]\n" qA "v[228:231],v[100:103],v[188:191],v[228:231]\n" qA "v[24:27],v[104:107],v[28:31],v[24:27]\n" \
qA "v[232:235],v[104:107],v[188:191],v[232:235]\n" qA "v[236:239],v[108:111],v[188:191],v[236:239]\n" qA "v[240:243],v[112:115],v[188:191],v[240:243]\n" qA "v[244:247],v[116:119],v[188:191],v[244:247]\n" \
qA "v[248:251],v[120:123],v[188:191],v[248:251]\n" qA "v[252:255],v[124:127],v[188:191],v[252:255]\n" qO "lgkmcnt(0)\n" qA "a[0:3],v[48:51],v[128:131],a[0:3]\n" \
qO "vmcnt(18)\n" qA "a[4:7],v[52:55],v[128:131],a[4:7]\n" "s_barrier\n" qA "a[8:11],v[56:59],v[128:131],a[8:11]\n" \
qC "v[88:89],v35 offset:24576\n" qA "a[12:15],v[60:63],v[128:131],a[12:15]\n" qN "m0,s62,40960\n" qA "a[16:19],v[64:67],v[128:131],a[16:19]\n" \
qC "v[90:91],v35 offset:24832\n" qA "a[20:23],v[68:71],v[128:131],a[20:23]\n" qE "v41,s[44:47],s56" qU qA "a[24:27],v[72:75],v[128:131],a[24:27]\n" \
qC "v[92:93],v35 offset:34816\n" qA "a[28:31],v[76:79],v[128:131],a[28:31]\n" qN "m0,s62,42944\n" qA "a[32:35],v[80:83],v[128:131],a[32:35]\n" \
qC "v[94:95],v35 offset:35072\n" qA "a[36:39],v[84:87],v[128:131],a[36:39]\n" qE "v41,s[44:47],s56 offen offset:64 lds\n" qA "v[8:11],v[48:51],v[28:31],v[8:11]\n" \
qC "v[96:97],v36 offset:24576\n" qA "a[40:43],v[48:51],v[132:135],a[40:43]\n" qN "m0,s62,44928\n" qA "a[44:47],v[52:55],v[132:135],a[44:47]\n" \
qC "v[98:99],v36 offset:24832\n" qA "a[48:51],v[56:59],v[132:135],a[48:51]\n" qE "v41,s[44:47],s56 offen offset:128 lds\n" qA "a[52:55],v[60:63],v[132:135],a[52:55]\n" \
qC "v[100:101],v36 offset:34816\n" qA "a[56:59],v[64:67],v[132:135],a[56:59]\n" qN "m0,s62,47104\n" qA "a[60:63],v[68:71],v[132:135],a[60:63]\n" \
qC "v[102:103],v36 offset:35072\n" qA "a[64:67],v[72:75],v[132:135],a[64:67]\n" qE "v42,s[48:51],s57" qU qA "a[68:71],v[76:79],v[132:135],a[68:71]\n" \
qC "v[104:105],v37 offset:28672\n" qA "a[72:75],v[80:83],v[132:135],a[72:75]\n" qN "m0,s62,49152\n" qA "a[76:79],v[84:87],v[132:135],a[76:79]\n" \
qC "v[106:107],v37 offset:28928\n" qA "a[80:83],v[48:51],v[136:139],a[80:83]\n" qE "v43,s[52:55],s58" qU qA "a[84:87],v[52:55],v[136:139],a[84:87]\n" \
qC "v[108:109],v37 offset:29696\n" qA "a[88:91],v[56:59],v[136:139],a[88:91]\n" qA "a[92:95],v[60:63],v[136:139],a[92:95]\n" qC "v[110:111],v37 offset:29952\n" \
qA "a[96:99],v[64:67],v[136:139],a[96:99]\n" qA "a[100:103],v[68:71],v[136:139],a[100:103]\n" qC "v[112:113],v35 offset:26624\n" qA "v[12:15],v[52:55],v[28:31],v[12:15]\n" \
qA "a[104:107],v[72:75],v[136:139],a[104:107]\n" qC "v[114:115],v35 offset:26880\n" qA "a[108:111],v[76:79],v[136:139],a[108:111]\n" qA "a[112:115],v[80:83],v[136:139],a[112:115]\n" \
qC "v[116:117],v36 offset:26624\n" qA "a[116:119],v[84:87],v[136:139],a[116:119]\n" qA "a[120:123],v[48:51],v[140:143],a[120:123]\n" qC "v[118:119],v36 offset:26880\n" \
qA "a[124:127],v[52:55],v[140:143],a[124:127]\n" qA "a[128:131],v[56:59],v[140:143],a[128:131]\n" qC "v[120:121],v35 offset:38912\n" qA "a[132:135],v[60:63],v[140:143],a[132:135]\n" \
qA "a[136:139],v[64:67],v[140:143],a[136:139]\n" qC "v[122:123],v35 offset:39168\n" qA "a[140:143],v[68:71],v[140:143],a[140:143]\n" qA "a[144:147],v[72:75],v[140:143],a[144:147]\n" \
qC "v[124:125],v36 offset:38912\n" qA "a[148:151],v[76:79],v[140:143],a[148:151]\n" qA "a[152:155],v[80:83],v[140:143],a[152:155]\n" qC "v[126:127],v36 offset:39168\n" \
qA "a[156:159],v[84:87],v[140:143],a[156:159]\n" qA "a[160:163],v[48:51],v[144:147],a[160:163]\n" qD "v[160:163],v39 offset:32768\n" qA "a[164:167],v[52:55],v[144:147],a[164:167]\n" \
qA "v[16:19],v[56:59],v[28:31],v[16:19]\n" qD "v[164:167],v39 offset:34816\n" qA "a[168:171],v[56:59],v[144:147],a[168:171]\n" qA "a[172:175],v[60:63],v[144:147],a[172:175]\n" \
qD "v[168:171],v39 offset:36864\n" qA "a[176:179],v[64:67],v[144:147],a[176:179]\n" qA "a[180:183],v[68:71],v[144:147],a[180:183]\n" qD "v[172:175],v39 offset:38912\n" \
qA "a[184:187],v[72:75],v[144:147],a[184:187]\n" qA "a[188:191],v[76:79],v[144:147],a[188:191]\n" qD "v[176:179],v39 offset:40960\n" qA "a[192:195],v[80:83],v[144:147],a[192:195]\n" \
qA "a[196:199],v[84:87],v[144:147],a[196:199]\n" qD "v[180:183],v39 offset:43008\n" qA "a[200:203],v[48:51],v[148:151],a[200:203]\n" qA "a[204:207],v[52:55],v[148:151],a[204:207]\n" \
qD "v[184:187],v39 offset:45056\n" qA "a[208:211],v[56:59],v[148:151],a[208:211]\n" qA "a[212:215],v[60:63],v[148:151],a[212:215]\n" qD "v[188:191],v39 offset:47104\n" \
qA "a[216:219],v[64:67],v[148:151],a[216:219]\n" qA "a[220:223],v[68:71],v[148:151],a[220:223]\n" qA "a[224:227],v[72:75],v[148:151],a[224:227]\n" qA "a[228:231],v[76:79],v[148:151],a[228:231]\n" \
qN "s56,s56,229376\n" qA "v[20:23],v[60:63],v[28:31],v[20:23]\n" qN "s57,s57,196608\n" qA "a[232:235],v[80:83],v[148:151],a[232:235]\n" \
qN "s58,s58,s60\n" qA "a[236:239],v[84:87],v[148:151],a[236:239]\n" "s_min_u32 s56,s56,7110656\n" qA "a[240:243],v[48:51],v[152:155],a[240:243]\n" \
"s_min_u32 s57,s57,6094848\n" qA "a[244:247],v[52:55],v[152:155],a[244:247]\n" "s_min_u32 s58,s58,s72\n" qA "a[248:251],v[56:59],v[152:155],a[248:251]\n" \
qA "a[252:255],v[60:63],v[152:155],a[252:255]\n" qA "v[192:195],v[64:67],v[152:155],v[192:195]\n" qA "v[196:199],v[68:71],v[152:155],v[196:199]\n" qA "v[200:203],v[72:75],v[152:155],v[200:203]\n" \
qA "v[204:207],v[76:79],v[152:155],v[204:207]\n" qA "v[208:211],v[80:83],v[152:155],v[208:211]\n" qA "v[212:215],v[84:87],v[152:155],v[212:215]\n" qA "v[216:219],v[48:51],v[156:159],v[216:219]\n" \
qA "v[220:223],v[52:55],v[156:159],v[220:223]\n" qA "v[224:227],v[56:59],v[156:159],v[224:227]\n" qA "v[228:231],v[60:63],v[156:159],v[228:231]\n" qA "v[24:27],v[64:67],v[28:31],v[24:27]\n" \
qA "v[232:235],v[64:67],v[156:159],v[232:235]\n" qA "v[236:239],v[68:71],v[156:159],v[236:239]\n" qA "v[240:243],v[72:75],v[156:159],v[240:243]\n" qA "v[244:247],v[76:79],v[156:159],v[244:247]\n" \
qA "v[248:251],v[80:83],v[156:159],v[248:251]\n" qA "v[252:255],v[84:87],v[156:159],v[252:255]\n" qO "lgkmcnt(0)\n" qA "a[0:3],v[88:91],v[160:163],a[0:3]\n" \
qO "vmcnt(10)\n" qA "a[4:7],v[92:95],v[160:163],a[4:7]\n" "s_barrier\n" qA "a[8:11],v[96:99],v[160:163],a[8:11]\n" \
qC "v[48:49],v32 offset:4096\n" qA "a[12:15],v[100:103],v[160:163],a[12:15]\n" qN "m0,s61,32768\n" qA "a[16:19],v[104:107],v[160:163],a[16:19]\n" \
qC "v[50:51],v32 offset:4352\n" qA "a[20:23],v[108:111],v[160:163],a[20:23]\n" qE "v40,s[40:43],0" qU qA "a[24:27],v[112:115],v[160:163],a[24:27]\n" \
qC "v[52:53],v32 offset:14336\n" qA "a[28:31],v[116:119],v[160:163],a[28:31]\n" qN "m0,s61,33792\n" qA "a[32:35],v[120:123],v[160:163],a[32:35]\n" \
qC "v[54:55],v32 offset:14592\n" qA "a[36:39],v[124:127],v[160:163],a[36:39]\n" qE "v40,s[40:43],s65" qU qA "v[8:11],v[88:91],v[28:31],v[8:11]\n" \
qC "v[56:57],v33 offset:4096\n" qA "a[40:43],v[88:91],v[164:167],a[40:43]\n" qN "m0,s61,34816\n" qA "a[44:47],v[92:95],v[164:167],a[44:47]\n" \
qC "v[58:59],v33 offset:4352\n" qA "a[48:51],v[96:99],v[164:167],a[48:51]\n" qE "v40,s[40:43],s66" qU qA "a[52:55],v[100:103],v[164:167],a[52:55]\n" \
qC "v[60:61],v33 offset:14336\n" qA "a[56:59],v[104:107],v[164:167],a[56:59]\n" qN "m0,s61,35840\n" qA "a[60:63],v[108:111],v[164:167],a[60:63]\n" \
qC "v[62:63],v33 offset:14592\n" qA "a[64:67],v[112:115],v[164:167],a[64:67]\n" qE "v40,s[40:43],s67" qU qA "a[68:71],v[116:119],v[164:167],a[68:71]\n" \
qC "v[64:65],v34 offset:8192\n" qA "a[72:75],v[120:123],v[164:167],a[72:75]\n" qN "m0,s61,36864\n" qA "a[76:79],v[124:127],v[164:167],a[76:79]\n" \
qC "v[66:67],v34 offset:8448\n" qA "a[80:83],v[88:91],v[168:171],a[80:83]\n" qE "v40,s[40:43],s68" qU qA "a[84:87],v[92:95],v[168:171],a[84:87]\n" \
qC "v[68:69],v34 offset:9216\n" qA "a[88:91],v[96:99],v[168:171],a[88:91]\n" qN "m0,s61,37888\n" qA "a[92:95],v[100:103],v[168:171],a[92:95]\n" \
qC "v[70:71],v34 offset:9472\n" qA "a[96:99],v[104:107],v[168:171],a[96:99]\n" qE "v40,s[40:43],s69" qU qA "a[100:103],v[108:111],v[168:171],a[100:103]\n" \
qC "v[72:73],v32 offset:6144\n" qA "v[12:15],v[92:95],v[28:31],v[12:15]\n" qN "m0,s61,38912\n" qA "a[104:107],v[112:115],v[168:171],a[104:107]\n" \
qC "v[74:75],v32 offset:6400\n" qA "a[108:111],v[116:119],v[168:171],a[108:111]\n" qE "v40,s[40:43],s70" qU qA "a[112:115],v[120:123],v[168:171],a[112:115]\n" \
qC "v[76:77],v33 offset:6144\n" qA "a[116:119],v[124:127],v[168:171],a[116:119]\n" qN "m0,s61,39936\n" qA "a[120:123],v[88:91],v[172:175],a[120:123]\n" \
qC "v[78:79],v33 offset:6400\n" qA "a[124:127],v[92:95],v[172:175],a[124:127]\n" qE "v40,s[40:43],s71" qU qA "a[128:131],v[96:99],v[172:175],a[128:131]\n" \
qC "v[80:81],v32 offset:18432\n" qA "a[132:135],v[100:103],v[172:175],a[132:135]\n" qN "m0,s62,61440\n" qA "a[136:139],v[104:107],v[172:175],a[136:139]\n" \
qC "v[82:83],v32 offset:18688\n" qA "a[140:143],v[108:111],v[172:175],a[140:143]\n" qE "v41,s[44:47],s56" qU qA "a[144:147],v[112:115],v[172:175],a[144:147]\n" \
qC "v[84:85],v33 offset:18432\n" qA "a[148:151],v[116:119],v[172:175],a[148:151]\n" qN "m0,s62,63424\n" qA "a[152:155],v[120:123],v[172:175],a[152:155]\n" \
qC "v[86:87],v33 offset:18688\n" qA "a[156:159],v[124:127],v[172:175],a[156:159]\n" qE "v41,s[44:47],s56 offen offset:64 lds\n" qA "a[160:163],v[88:91],v[176:179],a[160:163]\n" \
qD "v[128:131],v38 offset:0\n" qA "a[164:167],v[92:95],v[176:179],a[164:167]\n" qN "m0,s62,65408\n" qA "v[16:19],v[96:99],v[28:31],v[16:19]\n" \
qD "v[132:135],v38 offset:2048\n" qA "a[168:171],v[96:99],v[176:179],a[168:171]\n" qE "v41,s[44:47],s56 offen offset:128 lds\n" qA "a[172:175],v[100:103],v[176:179],a[172:175]\n" \
qD "v[136:139],v38 offset:4096\n" qA "a[176:179],v[104:107],v[176:179],a[176:179]\n" qN "m0,s62,67584\n" qA "a[180:183],v[108:111],v[176:179],a[180:183]\n" \
qD "v[140:143],v38 offset:6144\n" qA "a[184:187],v[112:115],v[176:179],a[184:187]\n" qE "v42,s[48:51],s57" qU qA "a[188:191],v[116:119],v[176:179],a[188:191]\n" \
qD "v[144:147],v38 offset:8192\n" qA "a[192:195],v[120:123],v[176:179],a[192:195]\n" qN "m0,s62,69632\n" qA "a[196:199],v[124:127],v[176:179],a[196:199]\n" \
qD "v[148:151],v38 offset:10240\n" qA "a[200:203],v[88:91],v[180:183],a[200:203]\n" qE "v43,s[52:55],s58" qU qA "a[204:207],v[92:95],v[180:183],a[204:207]\n" \
qD "v[152:155],v38 offset:12288\n" qA "a[208:211],v[96:99],v[180:183],a[208:211]\n" qA "a[212:215],v[100:103],v[180:183],a[212:215]\n" qD "v[156:159],v38 offset:14336\n" \
qA "a[216:219],v[104:107],v[180:183],a[216:219]\n" qA "a[220:223],v[108:111],v[180:183],a[220:223]\n" qA "a[224:227],v[112:115],v[180:183],a[224:227]\n" qA "a[228:231],v[116:119],v[180:183],a[228:231]\n" \
qN "s40,s40,128\n" qA "v[20:23],v[100:103],v[28:31],v[20:23]\n" "s_addc_u32 s41,s41,0\n" qA "a[232:235],v[120:123],v[180:183],a[232:235]\n" \
qN "s56,s56,229376\n" qA "a[236:239],v[124:127],v[180:183],a[236:239]\n" qN "s57,s57,196608\n" qA "a[240:243],v[88:91],v[184:187],a[240:243]\n" \
qN "s58,s58,s60\n" qA "a[244:247],v[92:95],v[184:187],a[244:247]\n" "s_min_u32 s56,s56,7110656\n" qA "a[248:251],v[96:99],v[184:187],a[248:251]\n" \
"s_min_u32 s57,s57,6094848\n" qA "a[252:255],v[100:103],v[184:187],a[252:255]\n" "s_min_u32 s58,s58,s72\n" qA "v[192:195],v[104:107],v[184:187],v[192:195]\n" \
qA "v[196:199],v[108:111],v[184:187],v[196:199]\n" qA "v[200:203],v[112:115],v[184:187],v[200:203]\n" qA "v[204:207],v[116:119],v[184:187],v[204:207]\n" qA "v[208:211],v[120:123],v[184:187],v[208:211]\n" \
qA "v[212:215],v[124:127],v[184:187],v[212:215]\n" qA "v[216:219],v[88:91],v[188:191],v[216:219]\n" qA "v[220:223],v[92:95],v[188:191],v[220:223]\n" qA "v[224:227],v[96:99],v[188:191],v[224:227]\n" \
qA "v[228:231],v[100:103],v[188:191],v[228:231]\n" qA "v[24:27],v[104:107],v[28:31],v[24:27]\n" qA "v[232:235],v[104:107],v[188:191],v[232:235]\n" qA "v[236:239],v[108:111],v[188:191],v[236:239]\n" \
qA "v[240:243],v[112:115],v[188:191],v[240:243]\n" qA "v[244:247],v[116:119],v[188:191],v[244:247]\n" qA "v[248:251],v[120:123],v[188:191],v[248:251]\n" qA "v[252:255],v[124:127],v[188:191],v[252:255]\n" \
qO "lgkmcnt(0)\n" qA "a[0:3],v[48:51],v[128:131],a[0:3]\n" qO "vmcnt(18)\n" qA "a[4:7],v[52:55],v[128:131],a[4:7]\n" \
"s_barrier\n" qA "a[8:11],v[56:59],v[128:131],a[8:11]\n" qC "v[88:89],v32 offset:24576\n" qA "a[12:15],v[60:63],v[128:131],a[12:15]\n" \
qN "m0,s62,0\n" qA "a[16:19],v[64:67],v[128:131],a[16:19]\n" qC "v[90:91],v32 offset:24832\n" qA "a[20:23],v[68:71],v[128:131],a[20:23]\n" \
qE "v41,s[44:47],s56" qU qA "a[24:27],v[72:75],v[128:131],a[24:27]\n" qC "v[92:93],v32 offset:34816\n" qA "a[28:31],v[76:79],v[128:131],a[28:31]\n" \
qN "m0,s62,1984\n" qA "a[32:35],v[80:83],v[128:131],a[32:35]\n" qC "v[94:95],v32 offset:35072\n" qA "a[36:39],v[84:87],v[128:131],a[36:39]\n" \
qE "v41,s[44:47],s56 offen offset:64 lds\n" qA "v[8:11],v[48:51],v[28:31],v[8:11]\n" qC "v[96:97],v33 offset:24576\n" qA "a[40:43],v[48:51],v[132:135],a[40:43]\n" \
qN "m0,s62,3968\n" qA "a[44:47],v[52:55],v[132:135],a[44:47]\n" qC "v[98:99],v33 offset:24832\n" qA "a[48:51],v[56:59],v[132:135],a[48:51]\n" \
qE "v41,s[44:47],s56 offen offset:128 lds\n" qA "a[52:55],v[60:63],v[132:135],a[52:55]\n" qC "v[100:101],v33 offset:34816\n" qA "a[56:59],v[64:67],v[132:135],a[56:59]\n" \
qN "m0,s62,6144\n" qA "a[60:63],v[68:71],v[132:135],a[60:63]\n" qC "v[102:103],v33 offset:35072\n" qA "a[64:67],v[72:75],v[132:135],a[64:67]\n" \
qE "v42,s[48:51],s57" qU qA "a[68:71],v[76:79],v[132:135],a[68:71]\n" qC "v[104:105],v34 offset:28672\n" qA "a[72:75],v[80:83],v[132:135],a[72:75]\n" \
qN "m0,s62,8192\n" qA "a[76:79],v[84:87],v[132:135],a[76:79]\n" qC "v[106:107],v34 offset:28928\n" qA "a[80:83],v[48:51],v[136:139],a[80:83]\n" \
qE "v43,s[52:55],s58" qU qA "a[84:87],v[52:55],v[136:139],a[84:87]\n" qC "v[108:109],v34 offset:29696\n" qA "a[88:91],v[56:59],v[136:139],a[88:91]\n" \
qA "a[92:95],v[60:63],v[136:139],a[92:95]\n" qC "v[110:111],v34 offset:29952\n" qA "a[96:99],v[64:67],v[136:139],a[96:99]\n" qA "a[100:103],v[68:71],v[136:139],a[100:103]\n" \
qC "v[112:113],v32 offset:26624\n" qA "v[12:15],v[52:55],v[28:31],v[12:15]\n" qA "a[104:107],v[72:75],v[136:139],a[104:107]\n" qC "v[114:115],v32 offset:26880\n" \
qA "a[108:111],v[76:79],v[136:139],a[108:111]\n" qA "a[112:115],v[80:83],v[136:139],a[112:115]\n" qC "v[116:117],v33 offset:26624\n" qA "a[116:119],v[84:87],v[136:139],a[116:119]\n" \
qA "a[120:123],v[48:51],v[140:143],a[120:123]\n" qC "v[118:119],v33 offset:26880\n" qA "a[124:127],v[52:55],v[140:143],a[124:127]\n" qA "a[128:131],v[56:59],v[140:143],a[128:131]\n" \
qC "v[120:121],v32 offset:38912\n" qA "a[132:135],v[60:63],v[140:143],a[132:135]\n" qA "a[136:139],v[64:67],v[140:143],a[136:139]\n" qC "v[122:123],v32 offset:39168\n" \
qA "a[140:143],v[68:71],v[140:143],a[140:143]\n" qA "a[144:147],v[72:75],v[140:143],a[144:147]\n" qC "v[124:125],v33 offset:38912\n" qA "a[148:151],v[76:79],v[140:143],a[148:151]\n" \
qA "a[152:155],v[80:83],v[140:143],a[152:155]\n" qC "v[126:127],v33 offset:39168\n" qA "a[156:159],v[84:87],v[140:143],a[156:159]\n" qA "a[160:163],v[48:51],v[144:147],a[160:163]\n" \
qD "v[160:163],v39 offset:0\n" qA "a[164:167],v[52:55],v[144:147],a[164:167]\n" qA "v[16:19],v[56:59],v[28:31],v[16:19]\n" qD "v[164:167],v39 offset:2048\n" \
qA "a[168:171],v[56:59],v[144:147],a[168:171]\n" qA "a[172:175],v[60:63],v[144:147],a[172:175]\n" qD "v[168:171],v39 offset:4096\n" qA "a[176:179],v[64:67],v[144:147],a[176:179]\n" \
qA "a[180:183],v[68:71],v[144:147],a[180:183]\n" qD "v[172:175],v39 offset:6144\n" qA "a[184:187],v[72:75],v[144:147],a[184:187]\n" qA "a[188:191],v[76:79],v[144:147],a[188:191]\n" \
qD "v[176:179],v39 offset:8192\n" qA "a[192:195],v[80:83],v[144:147],a[192:195]\n" qA "a[196:199],v[84:87],v[144:147],a[196:199]\n" qD "v[180:183],v39 offset:10240\n" \
qA "a[200:203],v[48:51],v[148:151],a[200:203]\n" qA "a[204:207],v[52:55],v[148:151],a[204:207]\n" qD "v[184:187],v39 offset:12288\n" qA "a[208:211],v[56:59],v[148:151],a[208:211]\n" \
qA "a[212:215],v[60:63],v[148:151],a[212:215]\n" qD "v[188:191],v39 offset:14336\n" qA "a[216:219],v[64:67],v[148:151],a[216:219]\n" qA "a[220:223],v[68:71],v[148:151],a[220:223]\n" \
qA "a[224:227],v[72:75],v[148:151],a[224:227]\n" qA "a[228:231],v[76:79],v[148:151],a[228:231]\n" qN "s56,s56,229376\n" qA "v[20:23],v[60:63],v[28:31],v[20:23]\n" \
qN "s57,s57,196608\n" qA "a[232:235],v[80:83],v[148:151],a[232:235]\n" qN "s58,s58,s60\n" qA "a[236:239],v[84:87],v[148:151],a[236:239]\n" \
"s_min_u32 s56,s56,7110656\n" qA "a[240:243],v[48:51],v[152:155],a[240:243]\n" "s_min_u32 s57,s57,6094848\n" qA "a[244:247],v[52:55],v[152:155],a[244:247]\n" \
"s_min_u32 s58,s58,s72\n" qA "a[248:251],v[56:59],v[152:155],a[248:251]\n" qA "a[252:255],v[60:63],v[152:155],a[252:255]\n" qA "v[192:195],v[64:67],v[152:155],v[192:195]\n" \
qA "v[196:199],v[68:71],v[152:155],v[196:199]\n" qA "v[200:203],v[72:75],v[152:155],v[200:203]\n" "s_mov_b32 m0,s64\n" qA "v[204:207],v[76:79],v[152:155],v[204:207]\n" \
qA "v[208:211],v[80:83],v[152:155],v[208:211]\n" qE "v46,s[84:87],0" qV qA "v[212:215],v[84:87],v[152:155],v[212:215]\n" qA "v[216:219],v[48:51],v[156:159],v[216:219]\n" \
qA "v[220:223],v[52:55],v[156:159],v[220:223]\n" qA "v[224:227],v[56:59],v[156:159],v[224:227]\n" qA "v[228:231],v[60:63],v[156:159],v[228:231]\n" qA "v[24:27],v[64:67],v[28:31],v[24:27]\n" \
qA "v[232:235],v[64:67],v[156:159],v[232:235]\n" qA "v[236:239],v[68:71],v[156:159],v[236:239]\n" qA "v[240:243],v[72:75],v[156:159],v[240:243]\n" qA "v[244:247],v[76:79],v[156:159],v[244:247]\n" \
qA "v[248:251],v[80:83],v[156:159],v[248:251]\n" "s_sub_u32 s63,s63,1\n" "s_cmp_lg_u32 s63,0\n" qA "v[252:255],v[84:87],v[156:159],v[252:255]\n" \
"s_cbranch_scc1 G1L1_%=\n" "G1TAIL1_%=:\n" qO "lgkmcnt(0)\n" qA "a[0:3],v[88:91],v[160:163],a[0:3]\n" \
qO "vmcnt(11)\n" qA "a[4:7],v[92:95],v[160:163],a[4:7]\n" "s_barrier\n" qA "a[8:11],v[96:99],v[160:163],a[8:11]\n" \
qC "v[48:49],v35 offset:4096\n" qA "a[12:15],v[100:103],v[160:163],a[12:15]\n" "v_mov_b32 v2,-1\n" qA "a[16:19],v[104:107],v[160:163],a[16:19]\n" \
qC "v[50:51],v35 offset:4352\n" "v_mov_b32 v3,-1\n" qA "a[20:23],v[108:111],v[160:163],a[20:23]\n" "v_mov_b32 v4,-1\n" \
qA "a[24:27],v[112:115],v[160:163],a[24:27]\n" qC "v[52:53],v35 offset:14336\n" "v_mov_b32 v5,-1\n" qA "a[28:31],v[116:119],v[160:163],a[28:31]\n" \
qA "a[32:35],v[120:123],v[160:163],a[32:35]\n" qC "v[54:55],v35 offset:14592\n" qA "a[36:39],v[124:127],v[160:163],a[36:39]\n" qF "v[2:5],v47,s[84:87],s90 offen offset:0 sc1\n" \
qA "v[8:11],v[88:91],v[28:31],v[8:11]\n" qC "v[56:57],v36 offset:4096\n" qA "a[40:43],v[88:91],v[164:167],a[40:43]\n" qA "a[44:47],v[92:95],v[164:167],a[44:47]\n" \
qC "v[58:59],v36 offset:4352\n" qA "a[48:51],v[96:99],v[164:167],a[48:51]\n" qA "a[52:55],v[100:103],v[164:167],a[52:55]\n" qC "v[60:61],v36 offset:14336\n" \
qA "a[56:59],v[104:107],v[164:167],a[56:59]\n" qA "a[60:63],v[108:111],v[164:167],a[60:63]\n" qC "v[62:63],v36 offset:14592\n" qA "a[64:67],v[112:115],v[164:167],a[64:67]\n" \
qA "a[68:71],v[116:119],v[164:167],a[68:71]\n" qC "v[64:65],v37 offset:8192\n" qA "a[72:75],v[120:123],v[164:167],a[72:75]\n" qA "a[76:79],v[124:127],v[164:167],a[76:79]\n" \
qC "v[66:67],v37 offset:8448\n" qA "a[80:83],v[88:91],v[168:171],a[80:83]\n" qF "v[2:5],v47,s[84:87],s90 offen offset:1024 sc1\n" qA "a[84:87],v[92:95],v[168:171],a[84:87]\n" \
qC "v[68:69],v37 offset:9216\n" qA "a[88:91],v[96:99],v[168:171],a[88:91]\n" qA "a[92:95],v[100:103],v[168:171],a[92:95]\n" qC "v[70:71],v37 offset:9472\n" \
qA "a[96:99],v[104:107],v[168:171],a[96:99]\n" qA "a[100:103],v[108:111],v[168:171],a[100:103]\n" qC "v[72:73],v35 offset:6144\n" qA "v[12:15],v[92:95],v[28:31],v[12:15]\n" \
qA "a[104:107],v[112:115],v[168:171],a[104:107]\n" qC "v[74:75],v35 offset:6400\n" qA "a[108:111],v[116:119],v[168:171],a[108:111]\n" qA "a[112:115],v[120:123],v[168:171],a[112:115]\n" \
qC "v[76:77],v36 offset:6144\n" qA "a[116:119],v[124:127],v[168:171],a[116:119]\n" qA "a[120:123],v[88:91],v[172:175],a[120:123]\n" qC "v[78:79],v36 offset:6400\n" \
qA "a[124:127],v[92:95],v[172:175],a[124:127]\n" qF "v[2:5],v47,s[84:87],s90 offen offset:2048 sc1\n" qA "a[128:131],v[96:99],v[172:175],a[128:131]\n" qC "v[80:81],v35 offset:18432\n" \
qA "a[132:135],v[100:103],v[172:175],a[132:135]\n" qA "a[136:139],v[104:107],v[172:175],a[136:139]\n" qC "v[82:83],v35 offset:18688\n" qA "a[140:143],v[108:111],v[172:175],a[140:143]\n" \
qA "a[144:147],v[112:115],v[172:175],a[144:147]\n" qC "v[84:85],v36 offset:18432\n" qA "a[148:151],v[116:119],v[172:175],a[148:151]\n" qA "a[152:155],v[120:123],v[172:175],a[152:155]\n" \
qC "v[86:87],v36 offset:18688\n" qA "a[156:159],v[124:127],v[172:175],a[156:159]\n" qA "a[160:163],v[88:91],v[176:179],a[160:163]\n" qD "v[128:131],v38 offset:32768\n" \
qA "a[164:167],v[92:95],v[176:179],a[164:167]\n" qA "v[16:19],v[96:99],v[28:31],v[16:19]\n" qD "v[132:135],v38 offset:34816\n" qA "a[168:171],v[96:99],v[176:179],a[168:171]\n" \
qF "v[2:5],v47,s[84:87],s90 offen offset:3072 sc1\n" qA "a[172:175],v[100:103],v[176:179],a[172:175]\n" qD "v[136:139],v38 offset:36864\n" qA "a[176:179],v[104:107],v[176:179],a[176:179]\n" \
qA "a[180:183],v[108:111],v[176:179],a[180:183]\n" qD "v[140:143],v38 offset:38912\n" qA "a[184:187],v[112:115],v[176:179],a[184:187]\n" qA "a[188:191],v[116:119],v[176:179],a[188:191]\n" \
qD "v[144:147],v38 offset:40960\n" qA "a[192:195],v[120:123],v[176:179],a[192:195]\n" qA "a[196:199],v[124:127],v[176:179],a[196:199]\n" qD "v[148:151],v38 offset:43008\n" \
qA "a[200:203],v[88:91],v[180:183],a[200:203]\n" qA "a[204:207],v[92:95],v[180:183],a[204:207]\n" qD "v[152:155],v38 offset:45056\n" qA "a[208:211],v[96:99],v[180:183],a[208:211]\n" \
qA "a[212:215],v[100:103],v[180:183],a[212:215]\n" qD "v[156:159],v38 offset:47104\n" qA "a[216:219],v[104:107],v[180:183],a[216:219]\n" qA "a[220:223],v[108:111],v[180:183],a[220:223]\n" \
"s_mov_b32 m0,s73\n" qA "a[224:227],v[112:115],v[180:183],a[224:227]\n" qA "a[228:231],v[116:119],v[180:183],a[228:231]\n" "global_load_lds_dwordx4 v47,s[100:101] offset:0\n" \
qA "v[20:23],v[100:103],v[28:31],v[20:23]\n" qA "a[232:235],v[120:123],v[180:183],a[232:235]\n" "global_load_lds_dwordx4 v47,s[100:101] offset:1024\n" qA "a[236:239],v[124:127],v[180:183],a[236:239]\n" \
qA "a[240:243],v[88:91],v[184:187],a[240:243]\n" "global_load_lds_dwordx4 v47,s[100:101] offset:2048\n" qA "a[244:247],v[92:95],v[184:187],a[244:247]\n" qA "a[248:251],v[96:99],v[184:187],a[248:251]\n" \
"global_load_lds_dwordx4 v47,s[100:101] offset:3072\n" qA "a[252:255],v[100:103],v[184:187],a[252:255]\n" qA "v[192:195],v[104:107],v[184:187],v[192:195]\n" "global_load_lds_dwordx4 v44,s[100:101] offset:0\n" \
qA "v[196:199],v[108:111],v[184:187],v[196:199]\n" qA "v[200:203],v[112:115],v[184:187],v[200:203]\n" "global_load_lds_dwordx4 v44,s[100:101] offset:1024\n" qA "v[204:207],v[116:119],v[184:187],v[204:207]\n" \
qA "v[208:211],v[120:123],v[184:187],v[208:211]\n" "global_load_lds_dwordx4 v44,s[100:101] offset:2048\n" qA "v[212:215],v[124:127],v[184:187],v[212:215]\n" qA "v[216:219],v[88:91],v[188:191],v[216:219]\n" \
"global_load_lds_dwordx4 v47,s[82:83] offset:0\n" qA "v[220:223],v[92:95],v[188:191],v[220:223]\n" qA "v[224:227],v[96:99],v[188:191],v[224:227]\n" "global_load_lds_dwordx4 v47,s[82:83] offset:1024\n" \
qA "v[228:231],v[100:103],v[188:191],v[228:231]\n" qA "v[24:27],v[104:107],v[28:31],v[24:27]\n" qA "v[232:235],v[104:107],v[188:191],v[232:235]\n" qA "v[236:239],v[108:111],v[188:191],v[236:239]\n" \
qA "v[240:243],v[112:115],v[188:191],v[240:243]\n" qA "v[244:247],v[116:119],v[188:191],v[244:247]\n" qA "v[248:251],v[120:123],v[188:191],v[248:251]\n" qA "v[252:255],v[124:127],v[188:191],v[252:255]\n" \
qO "lgkmcnt(0)\n" qA "a[0:3],v[48:51],v[128:131],a[0:3]\n" qO "vmcnt(13)\n" qA "a[4:7],v[52:55],v[128:131],a[4:7]\n" \
"s_barrier\n" qA "a[8:11],v[56:59],v[128:131],a[8:11]\n" qC "v[88:89],v35 offset:24576\n" qA "a[12:15],v[60:63],v[128:131],a[12:15]\n" \
qA "a[16:19],v[64:67],v[128:131],a[16:19]\n" qC "v[90:91],v35 offset:24832\n" qA "a[20:23],v[68:71],v[128:131],a[20:23]\n" qA "a[24:27],v[72:75],v[128:131],a[24:27]\n" \
qC "v[92:93],v35 offset:34816\n" qA "a[28:31],v[76:79],v[128:131],a[28:31]\n" qA "a[32:35],v[80:83],v[128:131],a[32:35]\n" qC "v[94:95],v35 offset:35072\n" \
qA "a[36:39],v[84:87],v[128:131],a[36:39]\n" qA "v[8:11],v[48:51],v[28:31],v[8:11]\n" qC "v[96:97],v36 offset:24576\n" qA "a[40:43],v[48:51],v[132:135],a[40:43]\n" \
qA "a[44:47],v[52:55],v[132:135],a[44:47]\n" qC "v[98:99],v36 offset:24832\n" qA "a[48:51],v[56:59],v[132:135],a[48:51]\n" qA "a[52:55],v[60:63],v[132:135],a[52:55]\n" \
qC "v[100:101],v36 offset:34816\n" qA "a[56:59],v[64:67],v[132:135],a[56:59]\n" qA "a[60:63],v[68:71],v[132:135],a[60:63]\n" qC "v[102:103],v36 offset:35072\n" \
qA "a[64:67],v[72:75],v[132:135],a[64:67]\n" qA "a[68:71],v[76:79],v[132:135],a[68:71]\n" qC "v[104:105],v37 offset:28672\n" qA "a[72:75],v[80:83],v[132:135],a[72:75]\n" \
qA "a[76:79],v[84:87],v[132:135],a[76:79]\n" qC "v[106:107],v37 offset:28928\n" qA "a[80:83],v[48:51],v[136:139],a[80:83]\n" qA "a[84:87],v[52:55],v[136:139],a[84:87]\n" \
qC "v[108:109],v37 offset:29696\n" qA "a[88:91],v[56:59],v[136:139],a[88:91]\n" qA "a[92:95],v[60:63],v[136:139],a[92:95]\n" qC "v[110:111],v37 offset:29952\n" \
qA "a[96:99],v[64:67],v[136:139],a[96:99]\n" qA "a[100:103],v[68:71],v[136:139],a[100:103]\n" qC "v[112:113],v35 offset:26624\n" qA "v[12:15],v[52:55],v[28:31],v[12:15]\n" \
qA "a[104:107],v[72:75],v[136:139],a[104:107]\n" qC "v[114:115],v35 offset:26880\n" qA "a[108:111],v[76:79],v[136:139],a[108:111]\n" qA "a[112:115],v[80:83],v[136:139],a[112:115]\n" \
qC "v[116:117],v36 offset:26624\n" qA "a[116:119],v[84:87],v[136:139],a[116:119]\n" qA "a[120:123],v[48:51],v[140:143],a[120:123]\n" qC "v[118:119],v36 offset:26880\n" \
qA "a[124:127],v[52:55],v[140:143],a[124:127]\n" qA "a[128:131],v[56:59],v[140:143],a[128:131]\n" qC "v[120:121],v35 offset:38912\n" qA "a[132:135],v[60:63],v[140:143],a[132:135]\n" \
qA "a[136:139],v[64:67],v[140:143],a[136:139]\n" qC "v[122:123],v35 offset:39168\n" qA "a[140:143],v[68:71],v[140:143],a[140:143]\n" qA "a[144:147],v[72:75],v[140:143],a[144:147]\n" \
qC "v[124:125],v36 offset:38912\n" qA "a[148:151],v[76:79],v[140:143],a[148:151]\n" qA "a[152:155],v[80:83],v[140:143],a[152:155]\n" qC "v[126:127],v36 offset:39168\n" \
qA "a[156:159],v[84:87],v[140:143],a[156:159]\n" qA "a[160:163],v[48:51],v[144:147],a[160:163]\n" qD "v[160:163],v39 offset:32768\n" qA "a[164:167],v[52:55],v[144:147],a[164:167]\n" \
qA "v[16:19],v[56:59],v[28:31],v[16:19]\n" qD "v[164:167],v39 offset:34816\n" qA "a[168:171],v[56:59],v[144:147],a[168:171]\n" qA "a[172:175],v[60:63],v[144:147],a[172:175]\n" \
qD "v[168:171],v39 offset:36864\n" qA "a[176:179],v[64:67],v[144:147],a[176:179]\n" qA "a[180:183],v[68:71],v[144:147],a[180:183]\n" qD "v[172:175],v39 offset:38912\n" \
qA "a[184:187],v[72:75],v[144:147],a[184:187]\n" qA "a[188:191],v[76:79],v[144:147],a[188:191]\n" qD "v[176:179],v39 offset:40960\n" qA "a[192:195],v[80:83],v[144:147],a[192:195]\n" \
qA "a[196:199],v[84:87],v[144:147],a[196:199]\n" qD "v[180:183],v39 offset:43008\n" qA "a[200:203],v[48:51],v[148:151],a[200:203]\n" qA "a[204:207],v[52:55],v[148:151],a[204:207]\n" \
qD "v[184:187],v39 offset:45056\n" qA "a[208:211],v[56:59],v[148:151],a[208:211]\n" qA "a[212:215],v[60:63],v[148:151],a[212:215]\n" qD "v[188:191],v39 offset:47104\n" \
qA "a[216:219],v[64:67],v[148:151],a[216:219]\n" qA "a[220:223],v[68:71],v[148:151],a[220:223]\n" qA "a[224:227],v[72:75],v[148:151],a[224:227]\n" qA "a[228:231],v[76:79],v[148:151],a[228:231]\n" \
qA "v[20:23],v[60:63],v[28:31],v[20:23]\n" qA "a[232:235],v[80:83],v[148:151],a[232:235]\n" qA "a[236:239],v[84:87],v[148:151],a[236:239]\n" qA "a[240:243],v[48:51],v[152:155],a[240:243]\n" \
qA "a[244:247],v[52:55],v[152:155],a[244:247]\n" qA "a[248:251],v[56:59],v[152:155],a[248:251]\n" qA "a[252:255],v[60:63],v[152:155],a[252:255]\n" qA "v[192:195],v[64:67],v[152:155],v[192:195]\n" \
qA "v[196:199],v[68:71],v[152:155],v[196:199]\n" qA "v[200:203],v[72:75],v[152:155],v[200:203]\n" qA "v[204:207],v[76:79],v[152:155],v[204:207]\n" qA "v[208:211],v[80:83],v[152:155],v[208:211]\n" \
qA "v[212:215],v[84:87],v[152:155],v[212:215]\n" qA "v[216:219],v[48:51],v[156:159],v[216:219]\n" qA "v[220:223],v[52:55],v[156:159],v[220:223]\n" qA "v[224:227],v[56:59],v[156:159],v[224:227]\n" \
qA "v[228:231],v[60:63],v[156:159],v[228:231]\n" qA "v[24:27],v[64:67],v[28:31],v[24:27]\n" qA "v[232:235],v[64:67],v[156:159],v[232:235]\n" qA "v[236:239],v[68:71],v[156:159],v[236:239]\n" \
qA "v[240:243],v[72:75],v[156:159],v[240:243]\n" qA "v[244:247],v[76:79],v[156:159],v[244:247]\n" qA "v[248:251],v[80:83],v[156:159],v[248:251]\n" qA "v[252:255],v[84:87],v[156:159],v[252:255]\n" \
qO "lgkmcnt(0)\n" qA "a[0:3],v[88:91],v[160:163],a[0:3]\n" qA "a[4:7],v[92:95],v[160:163],a[4:7]\n" qA "a[8:11],v[96:99],v[160:163],a[8:11]\n" \
qD "v[48:51],v7 offset:0\n" qA "a[12:15],v[100:103],v[160:163],a[12:15]\n" qD "v[52:55],v7 offset:256\n" qA "a[16:19],v[104:107],v[160:163],a[16:19]\n" \
qD "v[56:59],v7 offset:512\n" qA "a[20:23],v[108:111],v[160:163],a[20:23]\n" qD "v[60:63],v7 offset:768\n" qA "a[24:27],v[112:115],v[160:163],a[24:27]\n" \
qD "v[64:67],v7 offset:1024\n" qA "a[28:31],v[116:119],v[160:163],a[28:31]\n" qD "v[68:71],v7 offset:1280\n" qA "a[32:35],v[120:123],v[160:163],a[32:35]\n" \
qD "v[72:75],v7 offset:1536\n" qA "a[36:39],v[124:127],v[160:163],a[36:39]\n" qD "v[76:79],v7 offset:1792\n" qA "v[8:11],v[88:91],v[28:31],v[8:11]\n" \
qA "a[40:43],v[88:91],v[164:167],a[40:43]\n" qA "a[44:47],v[92:95],v[164:167],a[44:47]\n" qA "a[48:51],v[96:99],v[164:167],a[48:51]\n" qA "a[52:55],v[100:103],v[164:167],a[52:55]\n" \
qA "a[56:59],v[104:107],v[164:167],a[56:59]\n" qA "a[60:63],v[108:111],v[164:167],a[60:63]\n" qA "a[64:67],v[112:115],v[164:167],a[64:67]\n" qA "a[68:71],v[116:119],v[164:167],a[68:71]\n" \
qA "a[72:75],v[120:123],v[164:167],a[72:75]\n" qA "a[76:79],v[124:127],v[164:167],a[76:79]\n" qA "a[80:83],v[88:91],v[168:171],a[80:83]\n" qA "a[84:87],v[92:95],v[168:171],a[84:87]\n" \
qA "a[88:91],v[96:99],v[168:171],a[88:91]\n" qA "a[92:95],v[100:103],v[168:171],a[92:95]\n" qA "a[96:99],v[104:107],v[168:171],a[96:99]\n" qA "a[100:103],v[108:111],v[168:171],a[100:103]\n" \
qA "v[12:15],v[92:95],v[28:31],v[12:15]\n" qA "a[104:107],v[112:115],v[168:171],a[104:107]\n" qA "a[108:111],v[116:119],v[168:171],a[108:111]\n" qA "a[112:115],v[120:123],v[168:171],a[112:115]\n" \
qO "lgkmcnt(0)\n" qA "a[116:119],v[124:127],v[168:171],a[116:119]\n" "v_min3_u32 v84,v48,v50,v52\n" qA "a[120:123],v[88:91],v[172:175],a[120:123]\n" \
"v_max3_u32 v85,v48,v50,v52\n" qA "a[124:127],v[92:95],v[172:175],a[124:127]\n" "v_min3_u32 v84,v84,v54,v56\n" qA "a[128:131],v[96:99],v[172:175],a[128:131]\n" \
"v_max3_u32 v85,v85,v54,v56\n" qA "a[132:135],v[100:103],v[172:175],a[132:135]\n" "v_min3_u32 v84,v84,v58,v60\n" qA "a[136:139],v[104:107],v[172:175],a[136:139]\n" \
"v_max3_u32 v85,v85,v58,v60\n" qA "a[140:143],v[108:111],v[172:175],a[140:143]\n" "v_min3_u32 v84,v84,v62,v64\n" qA "a[144:147],v[112:115],v[172:175],a[144:147]\n" \
"v_max3_u32 v85,v85,v62,v64\n" qA "a[148:151],v[116:119],v[172:175],a[148:151]\n" "v_min3_u32 v84,v84,v66,v68\n" qA "a[152:155],v[120:123],v[172:175],a[152:155]\n" \
"v_max3_u32 v85,v85,v66,v68\n" qA "a[156:159],v[124:127],v[172:175],a[156:159]\n" "v_min3_u32 v84,v84,v70,v72\n" qA "a[160:163],v[88:91],v[176:179],a[160:163]\n" \
"v_max3_u32 v85,v85,v70,v72\n" qA "a[164:167],v[92:95],v[176:179],a[164:167]\n" "v_min3_u32 v84,v84,v74,v76\n" qA "v[16:19],v[96:99],v[28:31],v[16:19]\n" \
"v_max3_u32 v85,v85,v74,v76\n" qA "a[168:171],v[96:99],v[176:179],a[168:171]\n" "v_min_u32 v84,v84,v78\n" qA "a[172:175],v[100:103],v[176:179],a[172:175]\n" \
"v_max_u32 v85,v85,v78\n" qA "a[176:179],v[104:107],v[176:179],a[176:179]\n" qA "a[180:183],v[108:111],v[176:179],a[180:183]\n" qA "a[184:187],v[112:115],v[176:179],a[184:187]\n" \
"v_cmp_ne_u32 s[92:93],s75,v84\n" qA "a[188:191],v[116:119],v[176:179],a[188:191]\n" "v_cmp_ne_u32 vcc,s75,v85\n" qA "a[192:195],v[120:123],v[176:179],a[192:195]\n" \
qA "a[196:199],v[124:127],v[176:179],a[196:199]\n" qA "a[200:203],v[88:91],v[180:183],a[200:203]\n" qA "a[204:207],v[92:95],v[180:183],a[204:207]\n" "s_or_b64 s[92:93],s[92:93],vcc\n" \
qA "a[208:211],v[96:99],v[180:183],a[208:211]\n" qA "a[212:215],v[100:103],v[180:183],a[212:215]\n" "v_and_b32 v86,0xf0,v47\n" qA "a[216:219],v[104:107],v[180:183],a[216:219]\n" \
"v_lshlrev_b32 v86,2,v86\n" qA "a[220:223],v[108:111],v[180:183],a[220:223]\n" "v_bfe_u32 v87,v47,8,1\n" qA "a[224:227],v[112:115],v[180:183],a[224:227]\n" \
"v_bfe_u32 v83,v47,9,1\n" qA "a[228:231],v[116:119],v[180:183],a[228:231]\n" "v_lshl_or_b32 v87,v87,1,v83\n" qA "v[20:23],v[100:103],v[28:31],v[20:23]\n" \
"v_bfe_u32 v83,v47,6,2\n" qA "a[232:235],v[120:123],v[180:183],a[232:235]\n" "v_xor_b32 v83,v83,v87\n" qA "a[236:239],v[124:127],v[180:183],a[236:239]\n" \
"v_lshl_add_u32 v83,v83,4,v86\n" qA "a[240:243],v[88:91],v[184:187],a[240:243]\n" "v_add_u32 v3,0,v83\n" qA "a[244:247],v[92:95],v[184:187],a[244:247]\n" \
"v_lshl_add_u32 v87,v87,4,v86\n" qA "a[248:251],v[96:99],v[184:187],a[248:251]\n" "v_add_u32 v1,65536,v87\n" qA "a[252:255],v[100:103],v[184:187],a[252:255]\n" \
"v_add_u32 v2,16384,v87\n" qA "v[192:195],v[104:107],v[184:187],v[192:195]\n" "v_lshrrev_b32 v83,8,v47\n" qA "v[196:199],v[108:111],v[184:187],v[196:199]\n" \
"v_lshlrev_b32 v83,4,v83\n" qA "v[200:203],v[112:115],v[184:187],v[200:203]\n" "v_add_u32 v5,148096,v83\n" qA "v[204:207],v[116:119],v[184:187],v[204:207]\n" \
"v_add_u32 v4,0,v5\n" qA "v[208:211],v[120:123],v[184:187],v[208:211]\n" qN "s56,s76,4096\n" qA "v[212:215],v[124:127],v[184:187],v[212:215]\n" \
qA "v[216:219],v[88:91],v[188:191],v[216:219]\n" qA "v[220:223],v[92:95],v[188:191],v[220:223]\n" qA "v[224:227],v[96:99],v[188:191],v[224:227]\n" qA "v[228:231],v[100:103],v[188:191],v[228:231]\n" \
qA "v[24:27],v[104:107],v[28:31],v[24:27]\n" qA "v[232:235],v[104:107],v[188:191],v[232:235]\n" qA "v[236:239],v[108:111],v[188:191],v[236:239]\n" qA "v[240:243],v[112:115],v[188:191],v[240:243]\n" \
qA "v[244:247],v[116:119],v[188:191],v[244:247]\n" qA "v[248:251],v[120:123],v[188:191],v[248:251]\n" qA "v[252:255],v[124:127],v[188:191],v[252:255]\n" "s_cmp_eq_u64 s[92:93],0\n" \
"s_cbranch_scc1 G1OK1_%=\n" "s_mov_b32 s63,0x100000\n" "v_subrev_u32 v82,s64,v7\n" "v_add_u32 v82,1024,v82\n" \
"G1RE1_%=:\n" "s_sleep 1\n" qE "v[48:51],v82,s[84:87],0 offen offset:0 sc1\n" qE "v[52:55],v82,s[84:87],0 offen offset:256 sc1\n" \
qE "v[56:59],v82,s[84:87],0 offen offset:512 sc1\n" qE "v[60:63],v82,s[84:87],0 offen offset:768 sc1\n" qE "v[64:67],v82,s[84:87],0 offen offset:1024 sc1\n" qE "v[68:71],v82,s[84:87],0 offen offset:1280 sc1\n" \
qE "v[72:75],v82,s[84:87],0 offen offset:1536 sc1\n" qE "v[76:79],v82,s[84:87],0 offen offset:1792 sc1\n" qO "vmcnt(0)\n" "v_min3_u32 v84,v48,v50,v52\n" \
"v_max3_u32 v85,v48,v50,v52\n" "v_min3_u32 v84,v84,v54,v56\n" "v_max3_u32 v85,v85,v54,v56\n" "v_min3_u32 v84,v84,v58,v60\n" \
"v_max3_u32 v85,v85,v58,v60\n" "v_min3_u32 v84,v84,v62,v64\n" "v_max3_u32 v85,v85,v62,v64\n" "v_min3_u32 v84,v84,v66,v68\n" \
"v_max3_u32 v85,v85,v66,v68\n" "v_min3_u32 v84,v84,v70,v72\n" "v_max3_u32 v85,v85,v70,v72\n" "v_min3_u32 v84,v84,v74,v76\n" \
"v_max3_u32 v85,v85,v74,v76\n" "v_min_u32 v84,v84,v78\n" "v_max_u32 v85,v85,v78\n" "v_cmp_ne_u32 s[92:93],s75,v84\n" \
"v_cmp_ne_u32 vcc,s75,v85\n" "s_nop 3\n" "s_or_b64 s[92:93],s[92:93],vcc\n" "s_sub_u32 s63,s63,1\n" \
"s_cmp_eq_u32 s63,0\n" "s_cbranch_scc1 G1OK1_%=\n" "s_cmp_eq_u64 s[92:93],0\n" "s_cbranch_scc0 G1RE1_%=\n" \
"G1OK1_%=:\n" "s_nop 7\n" "s_mov_b32 exec_lo,0x00010001\n" "s_mov_b32 exec_hi,0x00010001\n" \
qQ "v4,v[8:11] offset:0\n" qQ "v4,v[12:15] offset:64\n" qQ "v4,v[16:19] offset:128\n" qQ "v4,v[20:23] offset:192\n" \
qQ "v4,v[24:27] offset:256\n" "s_mov_b64 exec,-1\n" qO "lgkmcnt(0)\n" "s_barrier\n" \
qD "v[8:11],v5 offset:0\n" qD "v[12:15],v5 offset:64\n" qD "v[16:19],v5 offset:128\n" qD "v[20:23],v5 offset:192\n" \
qD "v[24:27],v5 offset:256\n" qD "v[28:31],v5 offset:320\n" qD "v[32:35],v5 offset:384\n" qD "v[36:39],v5 offset:448\n" \
qD "v[40:43],v5 offset:512\n" qD "v[44:47],v5 offset:576\n" qO "lgkmcnt(0)\n" qG "v80,a32\n" \
qG "v81,a33\n" qG "v82,a34\n" qG "v83,a35\n" qG "v84,a36\n" \
qG "v85,a37\n" qG "v86,a38\n" qG "v87,a39\n" qG "v88,a72\n" \
qG "v89,a73\n" qG "v90,a74\n" qG "v91,a75\n" qG "v92,a76\n" \
qG "v93,a77\n" qG "v94,a78\n" qG "v95,a79\n" qH "v[80:81],v[48:49],v[40:41],v[80:81]" qR \
qH "v[82:83],v[48:49],v[42:43],v[82:83]" qR qH "v[84:85],v[48:49],v[44:45],v[84:85]" qR qH "v[86:87],v[48:49],v[46:47],v[86:87]" qR qG "v96,a112\n" \
qG "v97,a113\n" qG "v98,a114\n" qG "v99,a115\n" qG "v100,a116\n" \
qG "v101,a117\n" qG "v102,a118\n" qG "v103,a119\n" qH "v[88:89],v[52:53],v[40:41],v[88:89]" qR \
qH "v[90:91],v[52:53],v[42:43],v[90:91]" qR qH "v[92:93],v[52:53],v[44:45],v[92:93]" qR qH "v[94:95],v[52:53],v[46:47],v[94:95]" qR qI "v[80:81],v[80:81],v[50:51]" qS \
qI "v[82:83],v[82:83],v[50:51]" qS qI "v[84:85],v[84:85],v[50:51]" qS qI "v[86:87],v[86:87],v[50:51]" qS qG "v104,a152\n" \
qG "v105,a153\n" qG "v106,a154\n" qG "v107,a155\n" qG "v108,a156\n" \
qG "v109,a157\n" qG "v110,a158\n" qG "v111,a159\n" qH "v[96:97],v[56:57],v[40:41],v[96:97]" qR \
qH "v[98:99],v[56:57],v[42:43],v[98:99]" qR qH "v[100:101],v[56:57],v[44:45],v[100:101]" qR qH "v[102:103],v[56:57],v[46:47],v[102:103]" qR qI "v[88:89],v[88:89],v[54:55]" qS \
qI "v[90:91],v[90:91],v[54:55]" qS qI "v[92:93],v[92:93],v[54:55]" qS qI "v[94:95],v[94:95],v[54:55]" qS qJ "v112,v80,v81\n" \
qJ "v113,v82,v83\n" qJ "v114,v84,v85\n" qJ "v115,v86,v87\n" qH "v[104:105],v[60:61],v[40:41],v[104:105]" qR \
qH "v[106:107],v[60:61],v[42:43],v[106:107]" qR qH "v[108:109],v[60:61],v[44:45],v[108:109]" qR qH "v[110:111],v[60:61],v[46:47],v[110:111]" qR qI "v[96:97],v[96:97],v[58:59]" qS \
qI "v[98:99],v[98:99],v[58:59]" qS qI "v[100:101],v[100:101],v[58:59]" qS qI "v[102:103],v[102:103],v[58:59]" qS qJ "v116,v88,v89\n" \
qJ "v117,v90,v91\n" qJ "v118,v92,v93\n" qJ "v119,v94,v95\n" qK "v112,v114\n" \
qK "v113,v115\n" qI "v[104:105],v[104:105],v[62:63]" qS qI "v[106:107],v[106:107],v[62:63]" qS qI "v[108:109],v[108:109],v[62:63]" qS \
qI "v[110:111],v[110:111],v[62:63]" qS qJ "v120,v96,v97\n" qJ "v121,v98,v99\n" qJ "v122,v100,v101\n" \
qJ "v123,v102,v103\n" qK "v116,v118\n" qK "v117,v119\n" qJ "v124,v104,v105\n" \
qJ "v125,v106,v107\n" qJ "v126,v108,v109\n" qJ "v127,v110,v111\n" qF "v[112:115],v2,s[84:87],s76 offen offset:0 sc1\n" \
qK "v120,v122\n" qK "v121,v123\n" qK "v124,v126\n" qK "v125,v127\n" \
qG "v80,a192\n" qG "v81,a193\n" qF "v[116:119],v2,s[84:87],s76 offen offset:1024 sc1\n" qG "v82,a194\n" \
qG "v83,a195\n" qG "v84,a196\n" qG "v85,a197\n" qG "v86,a198\n" \
qG "v87,a199\n" qG "v88,a232\n" qG "v89,a233\n" qF "v[120:123],v2,s[84:87],s76 offen offset:2048 sc1\n" \
qG "v90,a234\n" qG "v91,a235\n" qG "v92,a236\n" qG "v93,a237\n" \
qG "v94,a238\n" qG "v95,a239\n" qH "v[80:81],v[64:65],v[40:41],v[80:81]" qR qH "v[82:83],v[64:65],v[42:43],v[82:83]" qR \
qF "v[124:127],v2,s[84:87],s76 offen offset:3072 sc1\n" qH "v[84:85],v[64:65],v[44:45],v[84:85]" qR qH "v[86:87],v[64:65],v[46:47],v[86:87]" qR qQ "v1,v[112:115] offset:0\n" \
qH "v[88:89],v[68:69],v[40:41],v[88:89]" qR qH "v[90:91],v[68:69],v[42:43],v[90:91]" qR qH "v[92:93],v[68:69],v[44:45],v[92:93]" qR qH "v[94:95],v[68:69],v[46:47],v[94:95]" qR \
qI "v[80:81],v[80:81],v[66:67]" qS qI "v[82:83],v[82:83],v[66:67]" qS qI "v[84:85],v[84:85],v[66:67]" qS qI "v[86:87],v[86:87],v[66:67]" qS \
qQ "v1,v[116:119] offset:1024\n" qH "v[96:97],v[72:73],v[40:41],v[208:209]" qR qH "v[98:99],v[72:73],v[42:43],v[210:211]" qR qH "v[100:101],v[72:73],v[44:45],v[212:213]" qR \
qH "v[102:103],v[72:73],v[46:47],v[214:215]" qR qI "v[88:89],v[88:89],v[70:71]" qS qI "v[90:91],v[90:91],v[70:71]" qS qI "v[92:93],v[92:93],v[70:71]" qS \
qI "v[94:95],v[94:95],v[70:71]" qS qJ "v112,v80,v81\n" qJ "v113,v82,v83\n" qJ "v114,v84,v85\n" \
qJ "v115,v86,v87\n" qQ "v1,v[120:123] offset:2048\n" qH "v[104:105],v[76:77],v[40:41],v[248:249]" qR qH "v[106:107],v[76:77],v[42:43],v[250:251]" qR \
qH "v[108:109],v[76:77],v[44:45],v[252:253]" qR qH "v[110:111],v[76:77],v[46:47],v[254:255]" qR qI "v[96:97],v[96:97],v[74:75]" qS qI "v[98:99],v[98:99],v[74:75]" qS \
qI "v[100:101],v[100:101],v[74:75]" qS qI "v[102:103],v[102:103],v[74:75]" qS qJ "v116,v88,v89\n" qJ "v117,v90,v91\n" \
qJ "v118,v92,v93\n" qJ "v119,v94,v95\n" qQ "v1,v[124:127] offset:3072\n" qK "v112,v114\n" \
qK "v113,v115\n" qI "v[104:105],v[104:105],v[78:79]" qS qI "v[106:107],v[106:107],v[78:79]" qS qI "v[108:109],v[108:109],v[78:79]" qS \
qI "v[110:111],v[110:111],v[78:79]" qS qJ "v120,v96,v97\n" qJ "v121,v98,v99\n" qJ "v122,v100,v101\n" \
qJ "v123,v102,v103\n" qK "v116,v118\n" qK "v117,v119\n" qJ "v124,v104,v105\n" \
qJ "v125,v106,v107\n" qJ "v126,v108,v109\n" qJ "v127,v110,v111\n" qF "v[112:115],v2,s[84:87],s56 offen offset:0 sc1\n" \
qK "v120,v122\n" qK "v121,v123\n" qK "v124,v126\n" qK "v125,v127\n" \
qG "v80,a24\n" qG "v81,a25\n" qF "v[116:119],v2,s[84:87],s56 offen offset:1024 sc1\n" qG "v82,a26\n" \
qG "v83,a27\n" qG "v84,a28\n" qG "v85,a29\n" qG "v86,a30\n" \
qG "v87,a31\n" qG "v88,a64\n" qG "v89,a65\n" qF "v[120:123],v2,s[84:87],s56 offen offset:2048 sc1\n" \
qG "v90,a66\n" qG "v91,a67\n" qG "v92,a68\n" qG "v93,a69\n" \
qG "v94,a70\n" qG "v95,a71\n" qH "v[80:81],v[48:49],v[32:33],v[80:81]" qR qH "v[82:83],v[48:49],v[34:35],v[82:83]" qR \
qF "v[124:127],v2,s[84:87],s56 offen offset:3072 sc1\n" qH "v[84:85],v[48:49],v[36:37],v[84:85]" qR qH "v[86:87],v[48:49],v[38:39],v[86:87]" qR "v_mul_f32 v4,0x3e8293ee,v51\n" \
qQ "v1,v[112:115] offset:4096\n" qG "v96,a104\n" qG "v97,a105\n" qG "v98,a106\n" \
qG "v99,a107\n" qG "v100,a108\n" qG "v101,a109\n" qG "v102,a110\n" \
qG "v103,a111\n" qH "v[88:89],v[52:53],v[32:33],v[88:89]" qR qH "v[90:91],v[52:53],v[34:35],v[90:91]" qR qH "v[92:93],v[52:53],v[36:37],v[92:93]" qR \
qH "v[94:95],v[52:53],v[38:39],v[94:95]" qR "v_mul_f32 v5,0x3e8293ee,v55\n" qI "v[80:81],v[80:81],v[4:5]" qT qI "v[82:83],v[82:83],v[4:5]" qT \
qI "v[84:85],v[84:85],v[4:5]" qT qI "v[86:87],v[86:87],v[4:5]" qT qQ "v1,v[116:119] offset:5120\n" qG "v104,a144\n" \
qG "v105,a145\n" qG "v106,a146\n" qG "v107,a147\n" qG "v108,a148\n" \
qG "v109,a149\n" qG "v110,a150\n" qG "v111,a151\n" qH "v[96:97],v[56:57],v[32:33],v[96:97]" qR \
qH "v[98:99],v[56:57],v[34:35],v[98:99]" qR qH "v[100:101],v[56:57],v[36:37],v[100:101]" qR qH "v[102:103],v[56:57],v[38:39],v[102:103]" qR "v_mul_f32 v4,0x3e8293ee,v59\n" \
qI "v[88:89],v[88:89],v[4:5]" qS qI "v[90:91],v[90:91],v[4:5]" qS qI "v[92:93],v[92:93],v[4:5]" qS qI "v[94:95],v[94:95],v[4:5]" qS \
qJ "v112,v80,v81\n" qJ "v113,v82,v83\n" qJ "v114,v84,v85\n" qJ "v115,v86,v87\n" \
qQ "v1,v[120:123] offset:6144\n" qH "v[104:105],v[60:61],v[32:33],v[104:105]" qR qH "v[106:107],v[60:61],v[34:35],v[106:107]" qR qH "v[108:109],v[60:61],v[36:37],v[108:109]" qR \
qH "v[110:111],v[60:61],v[38:39],v[110:111]" qR "v_mul_f32 v5,0x3e8293ee,v63\n" qI "v[96:97],v[96:97],v[4:5]" qT qI "v[98:99],v[98:99],v[4:5]" qT \
qI "v[100:101],v[100:101],v[4:5]" qT qI "v[102:103],v[102:103],v[4:5]" qT qJ "v116,v88,v89\n" qJ "v117,v90,v91\n" \
qJ "v118,v92,v93\n" qJ "v119,v94,v95\n" qQ "v1,v[124:127] offset:7168\n" qG "v80,a184\n" \
qG "v81,a185\n" qG "v82,a186\n" qG "v83,a187\n" qG "v84,a188\n" \
qG "v85,a189\n" qG "v86,a190\n" qG "v87,a191\n" qK "v112,v114\n" \
qK "v113,v115\n" qI "v[104:105],v[104:105],v[4:5]" qS qI "v[106:107],v[106:107],v[4:5]" qS qI "v[108:109],v[108:109],v[4:5]" qS \
qI "v[110:111],v[110:111],v[4:5]" qS qJ "v120,v96,v97\n" qJ "v121,v98,v99\n" qJ "v122,v100,v101\n" \
qJ "v123,v102,v103\n" qG "v88,a224\n" qG "v89,a225\n" qG "v90,a226\n" \
qG "v91,a227\n" qG "v92,a228\n" qG "v93,a229\n" qG "v94,a230\n" \
qG "v95,a231\n" qH "v[80:81],v[64:65],v[32:33],v[80:81]" qR qH "v[82:83],v[64:65],v[34:35],v[82:83]" qR qH "v[84:85],v[64:65],v[36:37],v[84:85]" qR \
qH "v[86:87],v[64:65],v[38:39],v[86:87]" qR "v_mul_f32 v4,0x3e8293ee,v67\n" qK "v116,v118\n" qK "v117,v119\n" \
qJ "v124,v104,v105\n" qJ "v125,v106,v107\n" qJ "v126,v108,v109\n" qJ "v127,v110,v111\n" \
qQ "v3,v[112:115] offset:0\n" qH "v[88:89],v[68:69],v[32:33],v[88:89]" qR qH "v[90:91],v[68:69],v[34:35],v[90:91]" qR qH "v[92:93],v[68:69],v[36:37],v[92:93]" qR \
qH "v[94:95],v[68:69],v[38:39],v[94:95]" qR "v_mul_f32 v5,0x3e8293ee,v71\n" qK "v120,v122\n" qK "v121,v123\n" \
qI "v[80:81],v[80:81],v[4:5]" qT qI "v[82:83],v[82:83],v[4:5]" qT qI "v[84:85],v[84:85],v[4:5]" qT qI "v[86:87],v[86:87],v[4:5]" qT \
qQ "v3,v[116:119] offset:1024\n" qH "v[96:97],v[72:73],v[32:33],v[200:201]" qR qH "v[98:99],v[72:73],v[34:35],v[202:203]" qR qH "v[100:101],v[72:73],v[36:37],v[204:205]" qR \
qH "v[102:103],v[72:73],v[38:39],v[206:207]" qR "v_mul_f32 v4,0x3e8293ee,v75\n" qK "v124,v126\n" qK "v125,v127\n" \
qI "v[88:89],v[88:89],v[4:5]" qS qI "v[90:91],v[90:91],v[4:5]" qS qI "v[92:93],v[92:93],v[4:5]" qS qI "v[94:95],v[94:95],v[4:5]" qS \
qJ "v112,v80,v81\n" qJ "v113,v82,v83\n" qJ "v114,v84,v85\n" qJ "v115,v86,v87\n" \
qQ "v3,v[120:123] offset:2048\n" qH "v[104:105],v[76:77],v[32:33],v[240:241]" qR qH "v[106:107],v[76:77],v[34:35],v[242:243]" qR qH "v[108:109],v[76:77],v[36:37],v[244:245]" qR \
qH "v[110:111],v[76:77],v[38:39],v[246:247]" qR "v_mul_f32 v5,0x3e8293ee,v79\n" qI "v[96:97],v[96:97],v[4:5]" qT qI "v[98:99],v[98:99],v[4:5]" qT \
qI "v[100:101],v[100:101],v[4:5]" qT qI "v[102:103],v[102:103],v[4:5]" qT qJ "v116,v88,v89\n" qJ "v117,v90,v91\n" \
qJ "v118,v92,v93\n" qJ "v119,v94,v95\n" qQ "v3,v[124:127] offset:3072\n" qK "v112,v114\n" \
qK "v113,v115\n" qI "v[104:105],v[104:105],v[4:5]" qS qI "v[106:107],v[106:107],v[4:5]" qS qI "v[108:109],v[108:109],v[4:5]" qS \
qI "v[110:111],v[110:111],v[4:5]" qS qJ "v120,v96,v97\n" qJ "v121,v98,v99\n" qJ "v122,v100,v101\n" \
qJ "v123,v102,v103\n" qK "v116,v118\n" qK "v117,v119\n" qJ "v124,v104,v105\n" \
qJ "v125,v106,v107\n" qJ "v126,v108,v109\n" qJ "v127,v110,v111\n" qQ "v3,v[112:115] offset:4096\n" \
qK "v120,v122\n" qK "v121,v123\n" qQ "v3,v[116:119] offset:5120\n" qK "v124,v126\n" \
qK "v125,v127\n" qQ "v3,v[120:123] offset:6144\n" qQ "v3,v[124:127] offset:7168\n" "v_mbcnt_lo_u32_b32 v82,-1,0\n" \
"v_mbcnt_hi_u32_b32 v82,-1,v82\n" "v_lshlrev_b32 v82,4,v82\n" "v_and_b32 v1,48,v82\n" "v_lshrrev_b32 v1,1,v1\n" \
"v_mov_b32 v3,s75\n" "v_xor_b32 v2,0x9e3779b9,v3\n" qO "vmcnt(0) lgkmcnt(0)\n" "s_mov_b64 exec,1\n" \
"buffer_store_dwordx2 v[2:3],v1,s[84:87],s77 offen sc1\n" "s_mov_b64 exec,-1\n" qG "v80,a0\n" qG "v81,a1\n" \
qG "v82,a2\n" qG "v83,a3\n" qG "v88,a8\n" qG "v89,a9\n" \
qG "v90,a10\n" qG "v91,a11\n" qG "v96,a16\n" qG "v97,a17\n" \
qG "v98,a18\n" qG "v99,a19\n" qG "v104,a40\n" qG "v105,a41\n" \
qG "v106,a42\n" qG "v107,a43\n" qH "v[80:81],v[48:49],v[8:9],v[80:81]" qR qH "v[82:83],v[48:49],v[10:11],v[82:83]" qR \
qH "v[88:89],v[48:49],v[16:17],v[88:89]" qR qH "v[90:91],v[48:49],v[18:19],v[90:91]" qR qH "v[96:97],v[48:49],v[24:25],v[96:97]" qR qH "v[98:99],v[48:49],v[26:27],v[98:99]" qR \
qH "v[104:105],v[52:53],v[8:9],v[104:105]" qR qH "v[106:107],v[52:53],v[10:11],v[106:107]" qR "v_mul_f32 v6,v51,v51\n" qG "v84,a4\n" \
qG "v85,a5\n" qG "v86,a6\n" qG "v87,a7\n" qG "v92,a12\n" \
qG "v93,a13\n" qG "v94,a14\n" qG "v95,a15\n" qG "v100,a20\n" \
qG "v101,a21\n" qG "v102,a22\n" qG "v103,a23\n" "v_mul_f32 v7,v55,v55\n" \
qG "v108,a44\n" qG "v109,a45\n" qG "v110,a46\n" qG "v111,a47\n" \
qH "v[84:85],v[48:49],v[12:13],v[84:85]" qR qH "v[86:87],v[48:49],v[14:15],v[86:87]" qR qI "v[80:81],v[80:81],v[6:7]" qT qI "v[82:83],v[82:83],v[6:7]" qT \
qH "v[92:93],v[48:49],v[20:21],v[92:93]" qR qH "v[94:95],v[48:49],v[22:23],v[94:95]" qR qI "v[88:89],v[88:89],v[6:7]" qT qI "v[90:91],v[90:91],v[6:7]" qT \
qH "v[100:101],v[48:49],v[28:29],v[100:101]" qR qH "v[102:103],v[48:49],v[30:31],v[102:103]" qR qI "v[96:97],v[96:97],v[6:7]" qT qI "v[98:99],v[98:99],v[6:7]" qT \
qH "v[108:109],v[52:53],v[12:13],v[108:109]" qR qH "v[110:111],v[52:53],v[14:15],v[110:111]" qR qI "v[104:105],v[104:105],v[6:7]" qS qI "v[106:107],v[106:107],v[6:7]" qS \
qI "v[80:81],v[80:81],v[84:85]\n" qI "v[82:83],v[82:83],v[86:87]\n" qI "v[88:89],v[88:89],v[92:93]\n" qI "v[90:91],v[90:91],v[94:95]\n" \
qI "v[96:97],v[96:97],v[100:101]\n" qI "v[98:99],v[98:99],v[102:103]\n" qI "v[104:105],v[104:105],v[108:109]\n" qI "v[106:107],v[106:107],v[110:111]\n" \
qJ "v128,v80,v81\n" qJ "v129,v82,v83\n" qJ "v130,v88,v89\n" qJ "v131,v90,v91\n" \
qJ "v136,v104,v105\n" qJ "v137,v106,v107\n" qJ "v132,v96,v97\n" qJ "v133,v98,v99\n" \
qG "v80,a48\n" qG "v81,a49\n" qG "v82,a50\n" qG "v83,a51\n" \
qG "v88,a56\n" qG "v89,a57\n" qG "v90,a58\n" qG "v91,a59\n" \
qG "v96,a80\n" qG "v97,a81\n" qG "v98,a82\n" qG "v99,a83\n" \
qG "v104,a88\n" qG "v105,a89\n" qG "v106,a90\n" qG "v107,a91\n" \
qH "v[80:81],v[52:53],v[16:17],v[80:81]" qR qH "v[82:83],v[52:53],v[18:19],v[82:83]" qR qH "v[88:89],v[52:53],v[24:25],v[88:89]" qR qH "v[90:91],v[52:53],v[26:27],v[90:91]" qR \
qH "v[96:97],v[56:57],v[8:9],v[96:97]" qR qH "v[98:99],v[56:57],v[10:11],v[98:99]" qR qH "v[104:105],v[56:57],v[16:17],v[104:105]" qR qH "v[106:107],v[56:57],v[18:19],v[106:107]" qR \
qG "v84,a52\n" qG "v85,a53\n" qG "v86,a54\n" qG "v87,a55\n" \
qG "v92,a60\n" qG "v93,a61\n" qG "v94,a62\n" qG "v95,a63\n" \
"v_mul_f32 v6,v59,v59\n" qG "v100,a84\n" qG "v101,a85\n" qG "v102,a86\n" \
qG "v103,a87\n" qG "v108,a92\n" qG "v109,a93\n" qG "v110,a94\n" \
qG "v111,a95\n" qH "v[84:85],v[52:53],v[20:21],v[84:85]" qR qH "v[86:87],v[52:53],v[22:23],v[86:87]" qR qI "v[80:81],v[80:81],v[6:7]" qS \
qI "v[82:83],v[82:83],v[6:7]" qS qH "v[92:93],v[52:53],v[28:29],v[92:93]" qR qH "v[94:95],v[52:53],v[30:31],v[94:95]" qR qI "v[88:89],v[88:89],v[6:7]" qS \
qI "v[90:91],v[90:91],v[6:7]" qS qH "v[100:101],v[56:57],v[12:13],v[100:101]" qR qH "v[102:103],v[56:57],v[14:15],v[102:103]" qR qI "v[96:97],v[96:97],v[6:7]" qT \
qI "v[98:99],v[98:99],v[6:7]" qT qH "v[108:109],v[56:57],v[20:21],v[108:109]" qR qH "v[110:111],v[56:57],v[22:23],v[110:111]" qR qI "v[104:105],v[104:105],v[6:7]" qT \
qI "v[106:107],v[106:107],v[6:7]" qT qI "v[80:81],v[80:81],v[84:85]\n" qI "v[82:83],v[82:83],v[86:87]\n" qI "v[88:89],v[88:89],v[92:93]\n" \
qI "v[90:91],v[90:91],v[94:95]\n" qI "v[96:97],v[96:97],v[100:101]\n" qI "v[98:99],v[98:99],v[102:103]\n" qI "v[104:105],v[104:105],v[108:109]\n" \
qI "v[106:107],v[106:107],v[110:111]\n" qJ "v138,v80,v81\n" qJ "v139,v82,v83\n" qJ "v134,v88,v89\n" \
qJ "v135,v90,v91\n" qJ "v144,v96,v97\n" qJ "v145,v98,v99\n" qJ "v146,v104,v105\n" \
qJ "v147,v106,v107\n" qG "v80,a96\n" qG "v81,a97\n" qG "v82,a98\n" \
qG "v83,a99\n" qG "v88,a120\n" qG "v89,a121\n" qG "v90,a122\n" \
qG "v91,a123\n" qG "v96,a128\n" qG "v97,a129\n" qG "v98,a130\n" \
qG "v99,a131\n" qG "v104,a136\n" qG "v105,a137\n" qG "v106,a138\n" \
qG "v107,a139\n" qH "v[80:81],v[56:57],v[24:25],v[80:81]" qR qH "v[82:83],v[56:57],v[26:27],v[82:83]" qR qH "v[88:89],v[60:61],v[8:9],v[88:89]" qR \
qH "v[90:91],v[60:61],v[10:11],v[90:91]" qR qH "v[96:97],v[60:61],v[16:17],v[96:97]" qR qH "v[98:99],v[60:61],v[18:19],v[98:99]" qR qH "v[104:105],v[60:61],v[24:25],v[104:105]" qR \
qH "v[106:107],v[60:61],v[26:27],v[106:107]" qR qG "v84,a100\n" qG "v85,a101\n" qG "v86,a102\n" \
qG "v87,a103\n" "v_mul_f32 v7,v63,v63\n" qG "v92,a124\n" qG "v93,a125\n" \
qG "v94,a126\n" qG "v95,a127\n" qG "v100,a132\n" qG "v101,a133\n" \
qG "v102,a134\n" qG "v103,a135\n" qG "v108,a140\n" qG "v109,a141\n" \
qG "v110,a142\n" qG "v111,a143\n" qH "v[84:85],v[56:57],v[28:29],v[84:85]" qR qH "v[86:87],v[56:57],v[30:31],v[86:87]" qR \
qI "v[80:81],v[80:81],v[6:7]" qT qI "v[82:83],v[82:83],v[6:7]" qT qH "v[92:93],v[60:61],v[12:13],v[92:93]" qR qH "v[94:95],v[60:61],v[14:15],v[94:95]" qR \
qI "v[88:89],v[88:89],v[6:7]" qS qI "v[90:91],v[90:91],v[6:7]" qS qH "v[100:101],v[60:61],v[20:21],v[100:101]" qR qH "v[102:103],v[60:61],v[22:23],v[102:103]" qR \
qI "v[96:97],v[96:97],v[6:7]" qS qI "v[98:99],v[98:99],v[6:7]" qS qH "v[108:109],v[60:61],v[28:29],v[108:109]" qR qH "v[110:111],v[60:61],v[30:31],v[110:111]" qR \
qI "v[104:105],v[104:105],v[6:7]" qS qI "v[106:107],v[106:107],v[6:7]" qS qI "v[80:81],v[80:81],v[84:85]\n" qI "v[82:83],v[82:83],v[86:87]\n" \
qI "v[88:89],v[88:89],v[92:93]\n" qI "v[90:91],v[90:91],v[94:95]\n" qI "v[96:97],v[96:97],v[100:101]\n" qI "v[98:99],v[98:99],v[102:103]\n" \
qI "v[104:105],v[104:105],v[108:109]\n" qI "v[106:107],v[106:107],v[110:111]\n" qJ "v152,v88,v89\n" qJ "v153,v90,v91\n" \
qJ "v154,v96,v97\n" qJ "v155,v98,v99\n" qJ "v148,v80,v81\n" qJ "v149,v82,v83\n" \
qJ "v150,v104,v105\n" qJ "v151,v106,v107\n" qK "v128,v130\n" qK "v129,v131\n" \
qK "v136,v138\n" qK "v137,v139\n" qK "v132,v134\n" qK "v133,v135\n" \
qK "v144,v146\n" qK "v145,v147\n" qK "v152,v154\n" qK "v153,v155\n" \
qK "v148,v150\n" qK "v149,v151\n" "s_bitcmp1_b32 s59,9\n" "s_cbranch_scc1 EPP1_%=\n" \
"s_xor_b32 s94,s75,0x9e3779b9\n" "s_mov_b32 s95,s75\n" "s_mov_b32 s58,0x400000\n" "EPQ1_%=:\n" \
"buffer_load_dwordx2 v[2:3],v1,s[84:87],s78 offen sc1\n" qO "vmcnt(0)\n" "v_cmp_ne_u64 vcc,s[94:95],v[2:3]\n" "s_nop 3\n" \
"s_cmp_eq_u64 vcc,0\n" "s_cbranch_scc1 EPP1_%=\n" "s_sleep 1\n" "s_sub_u32 s58,s58,1\n" \
"s_cmp_eq_u32 s58,0\n" "s_cbranch_scc0 EPQ1_%=\n" "EPP1_%=:\n" "v_mbcnt_lo_u32_b32 v82,-1,0\n" \
"v_mbcnt_hi_u32_b32 v82,-1,v82\n" "v_lshlrev_b32 v82,4,v82\n" "v_mov_b32 v47,v82\n" "v_add_u32 v1,1024,v82\n" \
"v_lshrrev_b32 v2,6,v82\n" "v_lshlrev_b32 v2,6,v2\n" "v_bfe_u32 v3,v82,4,2\n" "v_lshrrev_b32 v83,8,v82\n" \
"v_xor_b32 v83,v83,v3\n" "v_lshl_add_u32 v3,v3,4,v2\n" "v_lshl_add_u32 v2,v83,4,v2\n" "v_add_u32 v2,17408,v2\n" \
"v_add_u32 v3,50176,v3\n" "s_bitcmp1_b32 s59,9\n" "s_cbranch_scc1 EPZ1_%=\n" "s_sub_u32 s57,s76,1048576\n" \
qE "a[0:3],v1,s[84:87],s57 offen sc1\n" qN "s57,s57,4096\n" qE "a[4:7],v1,s[84:87],s57 offen sc1\n" qN "s57,s57,4096\n" \
qE "a[8:11],v1,s[84:87],s57 offen sc1\n" qN "s57,s57,4096\n" qE "a[12:15],v1,s[84:87],s57 offen sc1\n" qN "s57,s57,4096\n" \
qE "a[16:19],v1,s[84:87],s57 offen sc1\n" qN "s57,s57,4096\n" qE "a[20:23],v1,s[84:87],s57 offen sc1\n" qN "s57,s57,4096\n" \
qE "a[24:27],v1,s[84:87],s57 offen sc1\n" qN "s57,s57,4096\n" qE "a[28:31],v1,s[84:87],s57 offen sc1\n" "s_branch EPL1_%=\n" \
"EPZ1_%=:\n" "v_accvgpr_write_b32 a0,0\n" "v_accvgpr_write_b32 a1,0\n" "v_accvgpr_write_b32 a2,0\n" \
"v_accvgpr_write_b32 a3,0\n" "v_accvgpr_write_b32 a4,0\n" "v_accvgpr_write_b32 a5,0\n" "v_accvgpr_write_b32 a6,0\n" \
"v_accvgpr_write_b32 a7,0\n" "v_accvgpr_write_b32 a8,0\n" "v_accvgpr_write_b32 a9,0\n" "v_accvgpr_write_b32 a10,0\n" \
"v_accvgpr_write_b32 a11,0\n" "v_accvgpr_write_b32 a12,0\n" "v_accvgpr_write_b32 a13,0\n" "v_accvgpr_write_b32 a14,0\n" \
"v_accvgpr_write_b32 a15,0\n" "v_accvgpr_write_b32 a16,0\n" "v_accvgpr_write_b32 a17,0\n" "v_accvgpr_write_b32 a18,0\n" \
"v_accvgpr_write_b32 a19,0\n" "v_accvgpr_write_b32 a20,0\n" "v_accvgpr_write_b32 a21,0\n" "v_accvgpr_write_b32 a22,0\n" \
"v_accvgpr_write_b32 a23,0\n" "v_accvgpr_write_b32 a24,0\n" "v_accvgpr_write_b32 a25,0\n" "v_accvgpr_write_b32 a26,0\n" \
"v_accvgpr_write_b32 a27,0\n" "v_accvgpr_write_b32 a28,0\n" "v_accvgpr_write_b32 a29,0\n" "v_accvgpr_write_b32 a30,0\n" \
"v_accvgpr_write_b32 a31,0\n" "EPL1_%=:\n" qG "v80,a160\n" qG "v81,a161\n" \
qG "v82,a162\n" qG "v83,a163\n" qG "v88,a168\n" qG "v89,a169\n" \
qG "v90,a170\n" qG "v91,a171\n" qG "v96,a176\n" qG "v97,a177\n" \
qG "v98,a178\n" qG "v99,a179\n" qG "v104,a200\n" qG "v105,a201\n" \
qG "v106,a202\n" qG "v107,a203\n" qH "v[80:81],v[64:65],v[8:9],v[80:81]" qR qH "v[82:83],v[64:65],v[10:11],v[82:83]" qR \
qH "v[88:89],v[64:65],v[16:17],v[88:89]" qR qH "v[90:91],v[64:65],v[18:19],v[90:91]" qR qH "v[96:97],v[64:65],v[24:25],v[96:97]" qR qH "v[98:99],v[64:65],v[26:27],v[98:99]" qR \
qH "v[104:105],v[68:69],v[8:9],v[104:105]" qR qH "v[106:107],v[68:69],v[10:11],v[106:107]" qR "v_mul_f32 v6,v67,v67\n" qG "v84,a164\n" \
qG "v85,a165\n" qG "v86,a166\n" qG "v87,a167\n" qG "v92,a172\n" \
qG "v93,a173\n" qG "v94,a174\n" qG "v95,a175\n" qG "v100,a180\n" \
qG "v101,a181\n" qG "v102,a182\n" qG "v103,a183\n" "v_mul_f32 v7,v71,v71\n" \
qG "v108,a204\n" qG "v109,a205\n" qG "v110,a206\n" qG "v111,a207\n" \
qH "v[84:85],v[64:65],v[12:13],v[84:85]" qR qH "v[86:87],v[64:65],v[14:15],v[86:87]" qR qI "v[80:81],v[80:81],v[6:7]" qT qI "v[82:83],v[82:83],v[6:7]" qT \
qH "v[92:93],v[64:65],v[20:21],v[92:93]" qR qH "v[94:95],v[64:65],v[22:23],v[94:95]" qR qI "v[88:89],v[88:89],v[6:7]" qT qI "v[90:91],v[90:91],v[6:7]" qT \
qH "v[100:101],v[64:65],v[28:29],v[100:101]" qR qH "v[102:103],v[64:65],v[30:31],v[102:103]" qR qI "v[96:97],v[96:97],v[6:7]" qT qI "v[98:99],v[98:99],v[6:7]" qT \
qH "v[108:109],v[68:69],v[12:13],v[108:109]" qR qH "v[110:111],v[68:69],v[14:15],v[110:111]" qR qI "v[104:105],v[104:105],v[6:7]" qS qI "v[106:107],v[106:107],v[6:7]" qS \
qI "v[80:81],v[80:81],v[84:85]\n" qI "v[82:83],v[82:83],v[86:87]\n" qI "v[88:89],v[88:89],v[92:93]\n" qI "v[90:91],v[90:91],v[94:95]\n" \
qI "v[96:97],v[96:97],v[100:101]\n" qI "v[98:99],v[98:99],v[102:103]\n" qI "v[104:105],v[104:105],v[108:109]\n" qI "v[106:107],v[106:107],v[110:111]\n" \
qJ "v160,v80,v81\n" qJ "v161,v82,v83\n" qJ "v162,v88,v89\n" qJ "v163,v90,v91\n" \
qJ "v168,v104,v105\n" qJ "v169,v106,v107\n" qJ "v164,v96,v97\n" qJ "v165,v98,v99\n" \
qG "v80,a208\n" qG "v81,a209\n" qG "v82,a210\n" qG "v83,a211\n" \
qG "v88,a216\n" qG "v89,a217\n" qG "v90,a218\n" qG "v91,a219\n" \
qG "v96,a240\n" qG "v97,a241\n" qG "v98,a242\n" qG "v99,a243\n" \
qG "v104,a248\n" qG "v105,a249\n" qG "v106,a250\n" qG "v107,a251\n" \
qH "v[80:81],v[68:69],v[16:17],v[80:81]" qR qH "v[82:83],v[68:69],v[18:19],v[82:83]" qR qH "v[88:89],v[68:69],v[24:25],v[88:89]" qR qH "v[90:91],v[68:69],v[26:27],v[90:91]" qR \
qH "v[96:97],v[72:73],v[8:9],v[96:97]" qR qH "v[98:99],v[72:73],v[10:11],v[98:99]" qR qH "v[104:105],v[72:73],v[16:17],v[104:105]" qR qH "v[106:107],v[72:73],v[18:19],v[106:107]" qR \
qG "v84,a212\n" qG "v85,a213\n" qG "v86,a214\n" qG "v87,a215\n" \
qG "v92,a220\n" qG "v93,a221\n" qG "v94,a222\n" qG "v95,a223\n" \
"v_mul_f32 v6,v75,v75\n" qG "v100,a244\n" qG "v101,a245\n" qG "v102,a246\n" \
qG "v103,a247\n" qG "v108,a252\n" qG "v109,a253\n" qG "v110,a254\n" \
qG "v111,a255\n" qH "v[84:85],v[68:69],v[20:21],v[84:85]" qR qH "v[86:87],v[68:69],v[22:23],v[86:87]" qR qI "v[80:81],v[80:81],v[6:7]" qS \
qI "v[82:83],v[82:83],v[6:7]" qS qH "v[92:93],v[68:69],v[28:29],v[92:93]" qR qH "v[94:95],v[68:69],v[30:31],v[94:95]" qR qI "v[88:89],v[88:89],v[6:7]" qS \
qI "v[90:91],v[90:91],v[6:7]" qS qH "v[100:101],v[72:73],v[12:13],v[100:101]" qR qH "v[102:103],v[72:73],v[14:15],v[102:103]" qR qI "v[96:97],v[96:97],v[6:7]" qT \
qI "v[98:99],v[98:99],v[6:7]" qT qH "v[108:109],v[72:73],v[20:21],v[108:109]" qR qH "v[110:111],v[72:73],v[22:23],v[110:111]" qR qI "v[104:105],v[104:105],v[6:7]" qT \
qI "v[106:107],v[106:107],v[6:7]" qT qI "v[80:81],v[80:81],v[84:85]\n" qI "v[82:83],v[82:83],v[86:87]\n" qI "v[88:89],v[88:89],v[92:93]\n" \
qI "v[90:91],v[90:91],v[94:95]\n" qI "v[96:97],v[96:97],v[100:101]\n" qI "v[98:99],v[98:99],v[102:103]\n" qI "v[104:105],v[104:105],v[108:109]\n" \
qI "v[106:107],v[106:107],v[110:111]\n" qJ "v170,v80,v81\n" qJ "v171,v82,v83\n" qJ "v166,v88,v89\n" \
qJ "v167,v90,v91\n" qJ "v176,v96,v97\n" qJ "v177,v98,v99\n" qJ "v178,v104,v105\n" \
qJ "v179,v106,v107\n" qH "v[80:81],v[72:73],v[24:25],v[192:193]" qR qH "v[82:83],v[72:73],v[26:27],v[194:195]" qR qH "v[88:89],v[76:77],v[8:9],v[216:217]" qR \
qH "v[90:91],v[76:77],v[10:11],v[218:219]" qR qH "v[96:97],v[76:77],v[16:17],v[224:225]" qR qH "v[98:99],v[76:77],v[18:19],v[226:227]" qR qH "v[104:105],v[76:77],v[24:25],v[232:233]" qR \
qH "v[106:107],v[76:77],v[26:27],v[234:235]" qR "v_mul_f32 v7,v79,v79\n" qH "v[84:85],v[72:73],v[28:29],v[196:197]" qR qH "v[86:87],v[72:73],v[30:31],v[198:199]" qR \
qI "v[80:81],v[80:81],v[6:7]" qT qI "v[82:83],v[82:83],v[6:7]" qT qH "v[92:93],v[76:77],v[12:13],v[220:221]" qR qH "v[94:95],v[76:77],v[14:15],v[222:223]" qR \
qI "v[88:89],v[88:89],v[6:7]" qS qI "v[90:91],v[90:91],v[6:7]" qS qH "v[100:101],v[76:77],v[20:21],v[228:229]" qR qH "v[102:103],v[76:77],v[22:23],v[230:231]" qR \
qI "v[96:97],v[96:97],v[6:7]" qS qI "v[98:99],v[98:99],v[6:7]" qS qH "v[108:109],v[76:77],v[28:29],v[236:237]" qR qH "v[110:111],v[76:77],v[30:31],v[238:239]" qR \
qI "v[104:105],v[104:105],v[6:7]" qS qI "v[106:107],v[106:107],v[6:7]" qS qI "v[80:81],v[80:81],v[84:85]\n" qI "v[82:83],v[82:83],v[86:87]\n" \
qI "v[88:89],v[88:89],v[92:93]\n" qI "v[90:91],v[90:91],v[94:95]\n" qI "v[96:97],v[96:97],v[100:101]\n" qI "v[98:99],v[98:99],v[102:103]\n" \
qI "v[104:105],v[104:105],v[108:109]\n" qI "v[106:107],v[106:107],v[110:111]\n" qJ "v184,v88,v89\n" qJ "v185,v90,v91\n" \
qJ "v186,v96,v97\n" qJ "v187,v98,v99\n" qJ "v180,v80,v81\n" qJ "v181,v82,v83\n" \
qJ "v182,v104,v105\n" qJ "v183,v106,v107\n" qK "v160,v162\n" qK "v161,v163\n" \
qK "v168,v170\n" qK "v169,v171\n" qK "v164,v166\n" qK "v165,v167\n" \
qK "v176,v178\n" qK "v177,v179\n" qK "v184,v186\n" qK "v185,v187\n" \
qK "v180,v182\n" qK "v181,v183\n" "v_and_b32 v83,0x1f0,v47\n" "v_lshrrev_b32 v86,9,v47\n" \
"v_bfe_u32 v87,v47,6,2\n" "v_xor_b32 v87,v87,v86\n" "v_lshlrev_b32 v87,4,v87\n" "v_lshl_add_u32 v40,v83,2,v87\n" \
"v_add_u32 v40,4096,v40\n" "v_xor_b32 v41,32,v40\n" "v_bfe_u32 v87,v47,6,2\n" "v_lshl_add_u32 v87,v86,2,v87\n" \
"v_lshlrev_b32 v87,6,v87\n" "v_bfe_u32 v42,v47,8,1\n" "v_lshl_add_u32 v87,v42,5,v87\n" "v_bfe_u32 v42,v47,4,2\n" \
"v_lshl_add_u32 v42,v42,3,v87\n" "v_add_u32 v42,53248,v42\n" "v_lshl_add_u32 v43,v86,15,v83\n" "v_add_u32 v43,s81,v43\n" \
"v_lshrrev_b32 v44,4,v83\n" "v_lshlrev_b32 v87,2,v86\n" "v_sub_u32 v44,v44,v87\n" "v_and_b32 v83,0xf0,v47\n" \
"v_bfe_u32 v87,v47,8,1\n" "v_lshl_or_b32 v87,v87,1,v86\n" "v_lshl_add_u32 v45,v87,15,v83\n" "v_add_u32 v45,s79,v45\n" \
"v_bfe_u32 v87,v47,8,1\n" "v_lshl_add_u32 v83,v87,8,v83\n" "v_lshl_add_u32 v46,v86,15,v83\n" "v_add_u32 v46,s80,v46\n" \
"v_mov_b32 v47,0x70000000\n" qO "vmcnt(0)\n" qQ "v2,a[0:3] offset:0\n" qQ "v2,a[4:7] offset:4096\n" \
qQ "v2,a[8:11] offset:8192\n" qQ "v2,a[12:15] offset:12288\n" qQ "v3,a[16:19] offset:0\n" qQ "v3,a[20:23] offset:4096\n" \
qQ "v3,a[24:27] offset:8192\n" qQ "v3,a[28:31] offset:12288\n" qO "lgkmcnt(0)\n" "s_barrier\n" \
"s_branch G1END_%=\n" "G1W23_%=:\n" "s_cmp_eq_u32 s63,2\n" "s_cbranch_scc0 G1W3_%=\n" \
qC "v[48:49],v32 offset:0\n" qC "v[50:51],v32 offset:256\n" qC "v[52:53],v32 offset:10240\n" qC "v[54:55],v32 offset:10496\n" \
qC "v[56:57],v33 offset:0\n" qC "v[58:59],v33 offset:256\n" qC "v[60:61],v33 offset:10240\n" qC "v[62:63],v33 offset:10496\n" \
qC "v[64:65],v32 offset:2048\n" qC "v[66:67],v32 offset:2304\n" qC "v[68:69],v32 offset:12288\n" qC "v[70:71],v32 offset:12544\n" \
qC "v[72:73],v33 offset:2048\n" qC "v[74:75],v33 offset:2304\n" qC "v[76:77],v33 offset:12288\n" qC "v[78:79],v33 offset:12544\n" \
qC "v[80:81],v32 offset:16384\n" qC "v[82:83],v32 offset:16640\n" qC "v[84:85],v33 offset:16384\n" qC "v[86:87],v33 offset:16640\n" \
qD "v[128:131],v38 offset:0\n" qD "v[132:135],v38 offset:2048\n" qD "v[136:139],v38 offset:4096\n" qD "v[140:143],v38 offset:6144\n" \
qD "v[144:147],v38 offset:8192\n" qD "v[148:151],v38 offset:10240\n" qD "v[152:155],v38 offset:12288\n" qD "v[156:159],v38 offset:14336\n" \
"s_mov_b32 s63,7\n" qO "lgkmcnt(0)\n" qA "a[0:3],v[48:51],v[128:131],0\n" qO "vmcnt(14)\n" \
qA "a[4:7],v[52:55],v[128:131],0\n" "s_barrier\n" qA "a[8:11],v[56:59],v[128:131],0\n" qC "v[88:89],v32 offset:20480\n" \
qN "m0,s62,61440\n" qA "a[12:15],v[60:63],v[128:131],0\n" qE "v41,s[44:47],s56" qU qA "a[16:19],v[64:67],v[128:131],0\n" \
qC "v[90:91],v32 offset:20736\n" qA "a[20:23],v[68:71],v[128:131],0\n" qN "m0,s62,63424\n" qA "a[24:27],v[72:75],v[128:131],0\n" \
qC "v[92:93],v32 offset:30720\n" qE "v41,s[44:47],s56 offen offset:64 lds\n" qA "a[28:31],v[76:79],v[128:131],0\n" qA "a[32:35],v[80:83],v[128:131],0\n" \
qC "v[94:95],v32 offset:30976\n" qN "m0,s62,65408\n" qA "a[36:39],v[84:87],v[128:131],0\n" qE "v41,s[44:47],s56 offen offset:128 lds\n" \
qA "v[8:11],v[68:71],v[28:31],0\n" qC "v[96:97],v33 offset:20480\n" qA "a[40:43],v[48:51],v[132:135],0\n" qN "m0,s62,67584\n" \
qA "a[44:47],v[52:55],v[132:135],0\n" qC "v[98:99],v33 offset:20736\n" qE "v42,s[48:51],s57" qU qA "a[48:51],v[56:59],v[132:135],0\n" \
qA "a[52:55],v[60:63],v[132:135],0\n" qC "v[100:101],v33 offset:30720\n" qN "m0,s62,69632\n" qA "a[56:59],v[64:67],v[132:135],0\n" \
qE "v43,s[52:55],s58" qU qA "a[60:63],v[68:71],v[132:135],0\n" qC "v[102:103],v33 offset:30976\n" qN "s56,s56,229376\n" \
qN "s57,s57,196608\n" qN "s58,s58,s60\n" qA "a[64:67],v[72:75],v[132:135],0\n" "s_min_u32 s56,s56,7110656\n" \
"s_min_u32 s57,s57,6094848\n" "s_min_u32 s58,s58,s72\n" qN "m0,s62,0\n" qA "a[68:71],v[76:79],v[132:135],0\n" \
qC "v[104:105],v32 offset:22528\n" qE "v41,s[44:47],s56" qU qA "a[72:75],v[80:83],v[132:135],0\n" qA "a[76:79],v[84:87],v[132:135],0\n" \
qC "v[106:107],v32 offset:22784\n" qN "m0,s62,1984\n" qA "a[80:83],v[48:51],v[136:139],0\n" qE "v41,s[44:47],s56 offen offset:64 lds\n" \
qA "a[84:87],v[52:55],v[136:139],0\n" qC "v[108:109],v32 offset:32768\n" qA "a[88:91],v[56:59],v[136:139],0\n" qN "m0,s62,3968\n" \
qA "a[92:95],v[60:63],v[136:139],0\n" qC "v[110:111],v32 offset:33024\n" qE "v41,s[44:47],s56 offen offset:128 lds\n" qA "a[96:99],v[64:67],v[136:139],0\n" \
qA "a[100:103],v[68:71],v[136:139],0\n" qC "v[112:113],v33 offset:22528\n" qN "m0,s62,6144\n" qA "v[12:15],v[72:75],v[28:31],0\n" \
qE "v42,s[48:51],s57" qU qA "a[104:107],v[72:75],v[136:139],0\n" qC "v[114:115],v33 offset:22784\n" qA "a[108:111],v[76:79],v[136:139],0\n" \
qN "m0,s62,8192\n" qA "a[112:115],v[80:83],v[136:139],0\n" qC "v[116:117],v33 offset:32768\n" qE "v43,s[52:55],s58" qU \
qA "a[116:119],v[84:87],v[136:139],0\n" qA "a[120:123],v[48:51],v[140:143],0\n" qC "v[118:119],v33 offset:33024\n" qN "s56,s56,229376\n" \
qA "a[124:127],v[52:55],v[140:143],0\n" qN "s57,s57,196608\n" qA "a[128:131],v[56:59],v[140:143],0\n" qC "v[120:121],v32 offset:36864\n" \
qN "s58,s58,s60\n" qA "a[132:135],v[60:63],v[140:143],0\n" "s_min_u32 s56,s56,7110656\n" qA "a[136:139],v[64:67],v[140:143],0\n" \
qC "v[122:123],v32 offset:37120\n" "s_min_u32 s57,s57,6094848\n" qA "a[140:143],v[68:71],v[140:143],0\n" "s_min_u32 s58,s58,s72\n" \
qA "a[144:147],v[72:75],v[140:143],0\n" qC "v[124:125],v33 offset:36864\n" qA "a[148:151],v[76:79],v[140:143],0\n" qA "a[152:155],v[80:83],v[140:143],0\n" \
qC "v[126:127],v33 offset:37120\n" qA "a[156:159],v[84:87],v[140:143],0\n" qA "a[160:163],v[48:51],v[144:147],0\n" qD "v[160:163],v39 offset:0\n" \
qA "a[164:167],v[52:55],v[144:147],0\n" qA "v[16:19],v[76:79],v[28:31],0\n" qD "v[164:167],v39 offset:2048\n" qA "a[168:171],v[56:59],v[144:147],0\n" \
qA "a[172:175],v[60:63],v[144:147],0\n" qD "v[168:171],v39 offset:4096\n" qA "a[176:179],v[64:67],v[144:147],0\n" qA "a[180:183],v[68:71],v[144:147],0\n" \
qD "v[172:175],v39 offset:6144\n" qA "a[184:187],v[72:75],v[144:147],0\n" qA "a[188:191],v[76:79],v[144:147],0\n" qD "v[176:179],v39 offset:8192\n" \
qA "a[192:195],v[80:83],v[144:147],0\n" qA "a[196:199],v[84:87],v[144:147],0\n" qD "v[180:183],v39 offset:10240\n" qA "a[200:203],v[48:51],v[148:151],0\n" \
qA "a[204:207],v[52:55],v[148:151],0\n" qD "v[184:187],v39 offset:12288\n" qA "a[208:211],v[56:59],v[148:151],0\n" qA "a[212:215],v[60:63],v[148:151],0\n" \
qD "v[188:191],v39 offset:14336\n" qA "a[216:219],v[64:67],v[148:151],0\n" qA "a[220:223],v[68:71],v[148:151],0\n" qA "a[224:227],v[72:75],v[148:151],0\n" \
qA "a[228:231],v[76:79],v[148:151],0\n" qA "v[20:23],v[80:83],v[28:31],0\n" qA "a[232:235],v[80:83],v[148:151],0\n" qA "a[236:239],v[84:87],v[148:151],0\n" \
qA "a[240:243],v[48:51],v[152:155],0\n" qA "a[244:247],v[52:55],v[152:155],0\n" qA "a[248:251],v[56:59],v[152:155],0\n" qA "a[252:255],v[60:63],v[152:155],0\n" \
qA "v[192:195],v[64:67],v[152:155],0\n" qA "v[196:199],v[68:71],v[152:155],0\n" qA "v[200:203],v[72:75],v[152:155],0\n" qA "v[204:207],v[76:79],v[152:155],0\n" \
qA "v[208:211],v[80:83],v[152:155],0\n" qA "v[212:215],v[84:87],v[152:155],0\n" qA "v[216:219],v[48:51],v[156:159],0\n" qA "v[220:223],v[52:55],v[156:159],0\n" \
qA "v[224:227],v[56:59],v[156:159],0\n" qA "v[228:231],v[60:63],v[156:159],0\n" qA "v[24:27],v[84:87],v[28:31],0\n" qA "v[232:235],v[64:67],v[156:159],0\n" \
qA "v[236:239],v[68:71],v[156:159],0\n" qA "v[240:243],v[72:75],v[156:159],0\n" qA "v[244:247],v[76:79],v[156:159],0\n" qA "v[248:251],v[80:83],v[156:159],0\n" \
qA "v[252:255],v[84:87],v[156:159],0\n" "G1L2_%=:\n" qO "lgkmcnt(0)\n" qA "a[0:3],v[88:91],v[160:163],a[0:3]\n" \
qO "vmcnt(11)\n" qA "a[4:7],v[92:95],v[160:163],a[4:7]\n" "s_barrier\n" qA "a[8:11],v[96:99],v[160:163],a[8:11]\n" \
qC "v[48:49],v35 offset:0\n" qA "a[12:15],v[100:103],v[160:163],a[12:15]\n" qN "m0,s61,0\n" qA "a[16:19],v[104:107],v[160:163],a[16:19]\n" \
qC "v[50:51],v35 offset:256\n" qA "a[20:23],v[108:111],v[160:163],a[20:23]\n" qE "v40,s[40:43],0" qU qA "a[24:27],v[112:115],v[160:163],a[24:27]\n" \
qC "v[52:53],v35 offset:10240\n" qA "a[28:31],v[116:119],v[160:163],a[28:31]\n" qN "m0,s61,1024\n" qA "a[32:35],v[120:123],v[160:163],a[32:35]\n" \
qC "v[54:55],v35 offset:10496\n" qA "a[36:39],v[124:127],v[160:163],a[36:39]\n" qE "v40,s[40:43],s65" qU qA "v[8:11],v[108:111],v[28:31],v[8:11]\n" \
qC "v[56:57],v36 offset:0\n" qA "a[40:43],v[88:91],v[164:167],a[40:43]\n" qN "m0,s61,2048\n" qA "a[44:47],v[92:95],v[164:167],a[44:47]\n" \
qC "v[58:59],v36 offset:256\n" qA "a[48:51],v[96:99],v[164:167],a[48:51]\n" qE "v40,s[40:43],s66" qU qA "a[52:55],v[100:103],v[164:167],a[52:55]\n" \
qC "v[60:61],v36 offset:10240\n" qA "a[56:59],v[104:107],v[164:167],a[56:59]\n" qN "m0,s61,3072\n" qA "a[60:63],v[108:111],v[164:167],a[60:63]\n" \
qC "v[62:63],v36 offset:10496\n" qA "a[64:67],v[112:115],v[164:167],a[64:67]\n" qE "v40,s[40:43],s67" qU qA "a[68:71],v[116:119],v[164:167],a[68:71]\n" \
qC "v[64:65],v35 offset:2048\n" qA "a[72:75],v[120:123],v[164:167],a[72:75]\n" qN "m0,s61,4096\n" qA "a[76:79],v[124:127],v[164:167],a[76:79]\n" \
qC "v[66:67],v35 offset:2304\n" qA "a[80:83],v[88:91],v[168:171],a[80:83]\n" qE "v40,s[40:43],s68" qU qA "a[84:87],v[92:95],v[168:171],a[84:87]\n" \
qC "v[68:69],v35 offset:12288\n" qA "a[88:91],v[96:99],v[168:171],a[88:91]\n" qN "m0,s61,5120\n" qA "a[92:95],v[100:103],v[168:171],a[92:95]\n" \
qC "v[70:71],v35 offset:12544\n" qA "a[96:99],v[104:107],v[168:171],a[96:99]\n" qE "v40,s[40:43],s69" qU qA "a[100:103],v[108:111],v[168:171],a[100:103]\n" \
qC "v[72:73],v36 offset:2048\n" qA "v[12:15],v[112:115],v[28:31],v[12:15]\n" qN "m0,s61,6144\n" qA "a[104:107],v[112:115],v[168:171],a[104:107]\n" \
qC "v[74:75],v36 offset:2304\n" qA "a[108:111],v[116:119],v[168:171],a[108:111]\n" qE "v40,s[40:43],s70" qU qA "a[112:115],v[120:123],v[168:171],a[112:115]\n" \
qC "v[76:77],v36 offset:12288\n" qA "a[116:119],v[124:127],v[168:171],a[116:119]\n" qN "m0,s61,7168\n" qA "a[120:123],v[88:91],v[172:175],a[120:123]\n" \
qC "v[78:79],v36 offset:12544\n" qA "a[124:127],v[92:95],v[172:175],a[124:127]\n" qE "v40,s[40:43],s71" qU qA "a[128:131],v[96:99],v[172:175],a[128:131]\n" \
qC "v[80:81],v35 offset:16384\n" qA "a[132:135],v[100:103],v[172:175],a[132:135]\n" qN "m0,s62,20480\n" qA "a[136:139],v[104:107],v[172:175],a[136:139]\n" \
qC "v[82:83],v35 offset:16640\n" qA "a[140:143],v[108:111],v[172:175],a[140:143]\n" qE "v41,s[44:47],s56" qU qA "a[144:147],v[112:115],v[172:175],a[144:147]\n" \
qC "v[84:85],v36 offset:16384\n" qA "a[148:151],v[116:119],v[172:175],a[148:151]\n" qN "m0,s62,22464\n" qA "a[152:155],v[120:123],v[172:175],a[152:155]\n" \
qC "v[86:87],v36 offset:16640\n" qA "a[156:159],v[124:127],v[172:175],a[156:159]\n" qE "v41,s[44:47],s56 offen offset:64 lds\n" qA "a[160:163],v[88:91],v[176:179],a[160:163]\n" \
qD "v[128:131],v38 offset:32768\n" qA "a[164:167],v[92:95],v[176:179],a[164:167]\n" qN "m0,s62,24448\n" qA "v[16:19],v[116:119],v[28:31],v[16:19]\n" \
qD "v[132:135],v38 offset:34816\n" qA "a[168:171],v[96:99],v[176:179],a[168:171]\n" qE "v41,s[44:47],s56 offen offset:128 lds\n" qA "a[172:175],v[100:103],v[176:179],a[172:175]\n" \
qD "v[136:139],v38 offset:36864\n" qA "a[176:179],v[104:107],v[176:179],a[176:179]\n" qN "m0,s62,26624\n" qA "a[180:183],v[108:111],v[176:179],a[180:183]\n" \
qD "v[140:143],v38 offset:38912\n" qA "a[184:187],v[112:115],v[176:179],a[184:187]\n" qE "v42,s[48:51],s57" qU qA "a[188:191],v[116:119],v[176:179],a[188:191]\n" \
qD "v[144:147],v38 offset:40960\n" qA "a[192:195],v[120:123],v[176:179],a[192:195]\n" qN "m0,s62,28672\n" qA "a[196:199],v[124:127],v[176:179],a[196:199]\n" \
qD "v[148:151],v38 offset:43008\n" qA "a[200:203],v[88:91],v[180:183],a[200:203]\n" qE "v43,s[52:55],s58" qU qA "a[204:207],v[92:95],v[180:183],a[204:207]\n" \
qD "v[152:155],v38 offset:45056\n" qA "a[208:211],v[96:99],v[180:183],a[208:211]\n" qA "a[212:215],v[100:103],v[180:183],a[212:215]\n" qD "v[156:159],v38 offset:47104\n" \
qA "a[216:219],v[104:107],v[180:183],a[216:219]\n" qA "a[220:223],v[108:111],v[180:183],a[220:223]\n" qA "a[224:227],v[112:115],v[180:183],a[224:227]\n" qA "a[228:231],v[116:119],v[180:183],a[228:231]\n" \
qN "s40,s40,128\n" qA "v[20:23],v[120:123],v[28:31],v[20:23]\n" "s_addc_u32 s41,s41,0\n" qA "a[232:235],v[120:123],v[180:183],a[232:235]\n" \
qN "s56,s56,229376\n" qA "a[236:239],v[124:127],v[180:183],a[236:239]\n" qN "s57,s57,196608\n" qA "a[240:243],v[88:91],v[184:187],a[240:243]\n" \
qN "s58,s58,s60\n" qA "a[244:247],v[92:95],v[184:187],a[244:247]\n" "s_min_u32 s56,s56,7110656\n" qA "a[248:251],v[96:99],v[184:187],a[248:251]\n" \
"s_min_u32 s57,s57,6094848\n" qA "a[252:255],v[100:103],v[184:187],a[252:255]\n" "s_min_u32 s58,s58,s72\n" qA "v[192:195],v[104:107],v[184:187],v[192:195]\n" \
qA "v[196:199],v[108:111],v[184:187],v[196:199]\n" qA "v[200:203],v[112:115],v[184:187],v[200:203]\n" qA "v[204:207],v[116:119],v[184:187],v[204:207]\n" qA "v[208:211],v[120:123],v[184:187],v[208:211]\n" \
qA "v[212:215],v[124:127],v[184:187],v[212:215]\n" qA "v[216:219],v[88:91],v[188:191],v[216:219]\n" qA "v[220:223],v[92:95],v[188:191],v[220:223]\n" qA "v[224:227],v[96:99],v[188:191],v[224:227]\n" \
qA "v[228:231],v[100:103],v[188:191],v[228:231]\n" qA "v[24:27],v[124:127],v[28:31],v[24:27]\n" qA "v[232:235],v[104:107],v[188:191],v[232:235]\n" qA "v[236:239],v[108:111],v[188:191],v[236:239]\n" \
qA "v[240:243],v[112:115],v[188:191],v[240:243]\n" qA "v[244:247],v[116:119],v[188:191],v[244:247]\n" qA "v[248:251],v[120:123],v[188:191],v[248:251]\n" qA "v[252:255],v[124:127],v[188:191],v[252:255]\n" \
qO "lgkmcnt(0)\n" qA "a[0:3],v[48:51],v[128:131],a[0:3]\n" qO "vmcnt(18)\n" qA "a[4:7],v[52:55],v[128:131],a[4:7]\n" \
"s_barrier\n" qA "a[8:11],v[56:59],v[128:131],a[8:11]\n" qC "v[88:89],v35 offset:20480\n" qA "a[12:15],v[60:63],v[128:131],a[12:15]\n" \
qN "m0,s62,40960\n" qA "a[16:19],v[64:67],v[128:131],a[16:19]\n" qC "v[90:91],v35 offset:20736\n" qA "a[20:23],v[68:71],v[128:131],a[20:23]\n" \
qE "v41,s[44:47],s56" qU qA "a[24:27],v[72:75],v[128:131],a[24:27]\n" qC "v[92:93],v35 offset:30720\n" qA "a[28:31],v[76:79],v[128:131],a[28:31]\n" \
qN "m0,s62,42944\n" qA "a[32:35],v[80:83],v[128:131],a[32:35]\n" qC "v[94:95],v35 offset:30976\n" qA "a[36:39],v[84:87],v[128:131],a[36:39]\n" \
qE "v41,s[44:47],s56 offen offset:64 lds\n" qA "v[8:11],v[68:71],v[28:31],v[8:11]\n" qC "v[96:97],v36 offset:20480\n" qA "a[40:43],v[48:51],v[132:135],a[40:43]\n" \
qN "m0,s62,44928\n" qA "a[44:47],v[52:55],v[132:135],a[44:47]\n" qC "v[98:99],v36 offset:20736\n" qA "a[48:51],v[56:59],v[132:135],a[48:51]\n" \
qE "v41,s[44:47],s56 offen offset:128 lds\n" qA "a[52:55],v[60:63],v[132:135],a[52:55]\n" qC "v[100:101],v36 offset:30720\n" qA "a[56:59],v[64:67],v[132:135],a[56:59]\n" \
qN "m0,s62,47104\n" qA "a[60:63],v[68:71],v[132:135],a[60:63]\n" qC "v[102:103],v36 offset:30976\n" qA "a[64:67],v[72:75],v[132:135],a[64:67]\n" \
qE "v42,s[48:51],s57" qU qA "a[68:71],v[76:79],v[132:135],a[68:71]\n" qC "v[104:105],v35 offset:22528\n" qA "a[72:75],v[80:83],v[132:135],a[72:75]\n" \
qN "m0,s62,49152\n" qA "a[76:79],v[84:87],v[132:135],a[76:79]\n" qC "v[106:107],v35 offset:22784\n" qA "a[80:83],v[48:51],v[136:139],a[80:83]\n" \
qE "v43,s[52:55],s58" qU qA "a[84:87],v[52:55],v[136:139],a[84:87]\n" qC "v[108:109],v35 offset:32768\n" qA "a[88:91],v[56:59],v[136:139],a[88:91]\n" \
qA "a[92:95],v[60:63],v[136:139],a[92:95]\n" qC "v[110:111],v35 offset:33024\n" qA "a[96:99],v[64:67],v[136:139],a[96:99]\n" qA "a[100:103],v[68:71],v[136:139],a[100:103]\n" \
qC "v[112:113],v36 offset:22528\n" qA "v[12:15],v[72:75],v[28:31],v[12:15]\n" qA "a[104:107],v[72:75],v[136:139],a[104:107]\n" qC "v[114:115],v36 offset:22784\n" \
qA "a[108:111],v[76:79],v[136:139],a[108:111]\n" qA "a[112:115],v[80:83],v[136:139],a[112:115]\n" qC "v[116:117],v36 offset:32768\n" qA "a[116:119],v[84:87],v[136:139],a[116:119]\n" \
qA "a[120:123],v[48:51],v[140:143],a[120:123]\n" qC "v[118:119],v36 offset:33024\n" qA "a[124:127],v[52:55],v[140:143],a[124:127]\n" qA "a[128:131],v[56:59],v[140:143],a[128:131]\n" \
qC "v[120:121],v35 offset:36864\n" qA "a[132:135],v[60:63],v[140:143],a[132:135]\n" qA "a[136:139],v[64:67],v[140:143],a[136:139]\n" qC "v[122:123],v35 offset:37120\n" \
qA "a[140:143],v[68:71],v[140:143],a[140:143]\n" qA "a[144:147],v[72:75],v[140:143],a[144:147]\n" qC "v[124:125],v36 offset:36864\n" qA "a[148:151],v[76:79],v[140:143],a[148:151]\n" \
qA "a[152:155],v[80:83],v[140:143],a[152:155]\n" qC "v[126:127],v36 offset:37120\n" qA "a[156:159],v[84:87],v[140:143],a[156:159]\n" qA "a[160:163],v[48:51],v[144:147],a[160:163]\n" \
qD "v[160:163],v39 offset:32768\n" qA "a[164:167],v[52:55],v[144:147],a[164:167]\n" qA "v[16:19],v[76:79],v[28:31],v[16:19]\n" qD "v[164:167],v39 offset:34816\n" \
qA "a[168:171],v[56:59],v[144:147],a[168:171]\n" qA "a[172:175],v[60:63],v[144:147],a[172:175]\n" qD "v[168:171],v39 offset:36864\n" qA "a[176:179],v[64:67],v[144:147],a[176:179]\n" \
qA "a[180:183],v[68:71],v[144:147],a[180:183]\n" qD "v[172:175],v39 offset:38912\n" qA "a[184:187],v[72:75],v[144:147],a[184:187]\n" qA "a[188:191],v[76:79],v[144:147],a[188:191]\n" \
qD "v[176:179],v39 offset:40960\n" qA "a[192:195],v[80:83],v[144:147],a[192:195]\n" qA "a[196:199],v[84:87],v[144:147],a[196:199]\n" qD "v[180:183],v39 offset:43008\n" \
qA "a[200:203],v[48:51],v[148:151],a[200:203]\n" qA "a[204:207],v[52:55],v[148:151],a[204:207]\n" qD "v[184:187],v39 offset:45056\n" qA "a[208:211],v[56:59],v[148:151],a[208:211]\n" \
qA "a[212:215],v[60:63],v[148:151],a[212:215]\n" qD "v[188:191],v39 offset:47104\n" qA "a[216:219],v[64:67],v[148:151],a[216:219]\n" qA "a[220:223],v[68:71],v[148:151],a[220:223]\n" \
qA "a[224:227],v[72:75],v[148:151],a[224:227]\n" qA "a[228:231],v[76:79],v[148:151],a[228:231]\n" qN "s56,s56,229376\n" qA "v[20:23],v[80:83],v[28:31],v[20:23]\n" \
qN "s57,s57,196608\n" qA "a[232:235],v[80:83],v[148:151],a[232:235]\n" qN "s58,s58,s60\n" qA "a[236:239],v[84:87],v[148:151],a[236:239]\n" \
"s_min_u32 s56,s56,7110656\n" qA "a[240:243],v[48:51],v[152:155],a[240:243]\n" "s_min_u32 s57,s57,6094848\n" qA "a[244:247],v[52:55],v[152:155],a[244:247]\n" \
"s_min_u32 s58,s58,s72\n" qA "a[248:251],v[56:59],v[152:155],a[248:251]\n" qA "a[252:255],v[60:63],v[152:155],a[252:255]\n" qA "v[192:195],v[64:67],v[152:155],v[192:195]\n" \
qA "v[196:199],v[68:71],v[152:155],v[196:199]\n" qA "v[200:203],v[72:75],v[152:155],v[200:203]\n" qA "v[204:207],v[76:79],v[152:155],v[204:207]\n" qA "v[208:211],v[80:83],v[152:155],v[208:211]\n" \
qA "v[212:215],v[84:87],v[152:155],v[212:215]\n" qA "v[216:219],v[48:51],v[156:159],v[216:219]\n" qA "v[220:223],v[52:55],v[156:159],v[220:223]\n" qA "v[224:227],v[56:59],v[156:159],v[224:227]\n" \
qA "v[228:231],v[60:63],v[156:159],v[228:231]\n" qA "v[24:27],v[84:87],v[28:31],v[24:27]\n" qA "v[232:235],v[64:67],v[156:159],v[232:235]\n" qA "v[236:239],v[68:71],v[156:159],v[236:239]\n" \
qA "v[240:243],v[72:75],v[156:159],v[240:243]\n" qA "v[244:247],v[76:79],v[156:159],v[244:247]\n" qA "v[248:251],v[80:83],v[156:159],v[248:251]\n" qA "v[252:255],v[84:87],v[156:159],v[252:255]\n" \
qO "lgkmcnt(0)\n" qA "a[0:3],v[88:91],v[160:163],a[0:3]\n" qO "vmcnt(10)\n" qA "a[4:7],v[92:95],v[160:163],a[4:7]\n" \
"s_barrier\n" qA "a[8:11],v[96:99],v[160:163],a[8:11]\n" qC "v[48:49],v32 offset:0\n" qA "a[12:15],v[100:103],v[160:163],a[12:15]\n" \
qN "m0,s61,32768\n" qA "a[16:19],v[104:107],v[160:163],a[16:19]\n" qC "v[50:51],v32 offset:256\n" qA "a[20:23],v[108:111],v[160:163],a[20:23]\n" \
qE "v40,s[40:43],0" qU qA "a[24:27],v[112:115],v[160:163],a[24:27]\n" qC "v[52:53],v32 offset:10240\n" qA "a[28:31],v[116:119],v[160:163],a[28:31]\n" \
qN "m0,s61,33792\n" qA "a[32:35],v[120:123],v[160:163],a[32:35]\n" qC "v[54:55],v32 offset:10496\n" qA "a[36:39],v[124:127],v[160:163],a[36:39]\n" \
qE "v40,s[40:43],s65" qU qA "v[8:11],v[108:111],v[28:31],v[8:11]\n" qC "v[56:57],v33 offset:0\n" qA "a[40:43],v[88:91],v[164:167],a[40:43]\n" \
qN "m0,s61,34816\n" qA "a[44:47],v[92:95],v[164:167],a[44:47]\n" qC "v[58:59],v33 offset:256\n" qA "a[48:51],v[96:99],v[164:167],a[48:51]\n" \
qE "v40,s[40:43],s66" qU qA "a[52:55],v[100:103],v[164:167],a[52:55]\n" qC "v[60:61],v33 offset:10240\n" qA "a[56:59],v[104:107],v[164:167],a[56:59]\n" \
qN "m0,s61,35840\n" qA "a[60:63],v[108:111],v[164:167],a[60:63]\n" qC "v[62:63],v33 offset:10496\n" qA "a[64:67],v[112:115],v[164:167],a[64:67]\n" \
qE "v40,s[40:43],s67" qU qA "a[68:71],v[116:119],v[164:167],a[68:71]\n" qC "v[64:65],v32 offset:2048\n" qA "a[72:75],v[120:123],v[164:167],a[72:75]\n" \
qN "m0,s61,36864\n" qA "a[76:79],v[124:127],v[164:167],a[76:79]\n" qC "v[66:67],v32 offset:2304\n" qA "a[80:83],v[88:91],v[168:171],a[80:83]\n" \
qE "v40,s[40:43],s68" qU qA "a[84:87],v[92:95],v[168:171],a[84:87]\n" qC "v[68:69],v32 offset:12288\n" qA "a[88:91],v[96:99],v[168:171],a[88:91]\n" \
qN "m0,s61,37888\n" qA "a[92:95],v[100:103],v[168:171],a[92:95]\n" qC "v[70:71],v32 offset:12544\n" qA "a[96:99],v[104:107],v[168:171],a[96:99]\n" \
qE "v40,s[40:43],s69" qU qA "a[100:103],v[108:111],v[168:171],a[100:103]\n" qC "v[72:73],v33 offset:2048\n" qA "v[12:15],v[112:115],v[28:31],v[12:15]\n" \
qN "m0,s61,38912\n" qA "a[104:107],v[112:115],v[168:171],a[104:107]\n" qC "v[74:75],v33 offset:2304\n" qA "a[108:111],v[116:119],v[168:171],a[108:111]\n" \
qE "v40,s[40:43],s70" qU qA "a[112:115],v[120:123],v[168:171],a[112:115]\n" qC "v[76:77],v33 offset:12288\n" qA "a[116:119],v[124:127],v[168:171],a[116:119]\n" \
qN "m0,s61,39936\n" qA "a[120:123],v[88:91],v[172:175],a[120:123]\n" qC "v[78:79],v33 offset:12544\n" qA "a[124:127],v[92:95],v[172:175],a[124:127]\n" \
qE "v40,s[40:43],s71" qU qA "a[128:131],v[96:99],v[172:175],a[128:131]\n" qC "v[80:81],v32 offset:16384\n" qA "a[132:135],v[100:103],v[172:175],a[132:135]\n" \
qN "m0,s62,61440\n" qA "a[136:139],v[104:107],v[172:175],a[136:139]\n" qC "v[82:83],v32 offset:16640\n" qA "a[140:143],v[108:111],v[172:175],a[140:143]\n" \
qE "v41,s[44:47],s56" qU qA "a[144:147],v[112:115],v[172:175],a[144:147]\n" qC "v[84:85],v33 offset:16384\n" qA "a[148:151],v[116:119],v[172:175],a[148:151]\n" \
qN "m0,s62,63424\n" qA "a[152:155],v[120:123],v[172:175],a[152:155]\n" qC "v[86:87],v33 offset:16640\n" qA "a[156:159],v[124:127],v[172:175],a[156:159]\n" \
qE "v41,s[44:47],s56 offen offset:64 lds\n" qA "a[160:163],v[88:91],v[176:179],a[160:163]\n" qD "v[128:131],v38 offset:0\n" qA "a[164:167],v[92:95],v[176:179],a[164:167]\n" \
qN "m0,s62,65408\n" qA "v[16:19],v[116:119],v[28:31],v[16:19]\n" qD "v[132:135],v38 offset:2048\n" qA "a[168:171],v[96:99],v[176:179],a[168:171]\n" \
qE "v41,s[44:47],s56 offen offset:128 lds\n" qA "a[172:175],v[100:103],v[176:179],a[172:175]\n" qD "v[136:139],v38 offset:4096\n" qA "a[176:179],v[104:107],v[176:179],a[176:179]\n" \
qN "m0,s62,67584\n" qA "a[180:183],v[108:111],v[176:179],a[180:183]\n" qD "v[140:143],v38 offset:6144\n" qA "a[184:187],v[112:115],v[176:179],a[184:187]\n" \
qE "v42,s[48:51],s57" qU qA "a[188:191],v[116:119],v[176:179],a[188:191]\n" qD "v[144:147],v38 offset:8192\n" qA "a[192:195],v[120:123],v[176:179],a[192:195]\n" \
qN "m0,s62,69632\n" qA "a[196:199],v[124:127],v[176:179],a[196:199]\n" qD "v[148:151],v38 offset:10240\n" qA "a[200:203],v[88:91],v[180:183],a[200:203]\n" \
qE "v43,s[52:55],s58" qU qA "a[204:207],v[92:95],v[180:183],a[204:207]\n" qD "v[152:155],v38 offset:12288\n" qA "a[208:211],v[96:99],v[180:183],a[208:211]\n" \
qA "a[212:215],v[100:103],v[180:183],a[212:215]\n" qD "v[156:159],v38 offset:14336\n" qA "a[216:219],v[104:107],v[180:183],a[216:219]\n" qA "a[220:223],v[108:111],v[180:183],a[220:223]\n" \
qA "a[224:227],v[112:115],v[180:183],a[224:227]\n" qA "a[228:231],v[116:119],v[180:183],a[228:231]\n" qN "s40,s40,128\n" qA "v[20:23],v[120:123],v[28:31],v[20:23]\n" \
"s_addc_u32 s41,s41,0\n" qA "a[232:235],v[120:123],v[180:183],a[232:235]\n" qN "s56,s56,229376\n" qA "a[236:239],v[124:127],v[180:183],a[236:239]\n" \
qN "s57,s57,196608\n" qA "a[240:243],v[88:91],v[184:187],a[240:243]\n" qN "s58,s58,s60\n" qA "a[244:247],v[92:95],v[184:187],a[244:247]\n" \
"s_min_u32 s56,s56,7110656\n" qA "a[248:251],v[96:99],v[184:187],a[248:251]\n" "s_min_u32 s57,s57,6094848\n" qA "a[252:255],v[100:103],v[184:187],a[252:255]\n" \
"s_min_u32 s58,s58,s72\n" qA "v[192:195],v[104:107],v[184:187],v[192:195]\n" qA "v[196:199],v[108:111],v[184:187],v[196:199]\n" qA "v[200:203],v[112:115],v[184:187],v[200:203]\n" \
qA "v[204:207],v[116:119],v[184:187],v[204:207]\n" qA "v[208:211],v[120:123],v[184:187],v[208:211]\n" qA "v[212:215],v[124:127],v[184:187],v[212:215]\n" qA "v[216:219],v[88:91],v[188:191],v[216:219]\n" \
qA "v[220:223],v[92:95],v[188:191],v[220:223]\n" qA "v[224:227],v[96:99],v[188:191],v[224:227]\n" qA "v[228:231],v[100:103],v[188:191],v[228:231]\n" qA "v[24:27],v[124:127],v[28:31],v[24:27]\n" \
qA "v[232:235],v[104:107],v[188:191],v[232:235]\n" qA "v[236:239],v[108:111],v[188:191],v[236:239]\n" qA "v[240:243],v[112:115],v[188:191],v[240:243]\n" qA "v[244:247],v[116:119],v[188:191],v[244:247]\n" \
qA "v[248:251],v[120:123],v[188:191],v[248:251]\n" qA "v[252:255],v[124:127],v[188:191],v[252:255]\n" qO "lgkmcnt(0)\n" qA "a[0:3],v[48:51],v[128:131],a[0:3]\n" \
qO "vmcnt(18)\n" qA "a[4:7],v[52:55],v[128:131],a[4:7]\n" "s_barrier\n" qA "a[8:11],v[56:59],v[128:131],a[8:11]\n" \
qC "v[88:89],v32 offset:20480\n" qA "a[12:15],v[60:63],v[128:131],a[12:15]\n" qN "m0,s62,0\n" qA "a[16:19],v[64:67],v[128:131],a[16:19]\n" \
qC "v[90:91],v32 offset:20736\n" qA "a[20:23],v[68:71],v[128:131],a[20:23]\n" qE "v41,s[44:47],s56" qU qA "a[24:27],v[72:75],v[128:131],a[24:27]\n" \
qC "v[92:93],v32 offset:30720\n" qA "a[28:31],v[76:79],v[128:131],a[28:31]\n" qN "m0,s62,1984\n" qA "a[32:35],v[80:83],v[128:131],a[32:35]\n" \
qC "v[94:95],v32 offset:30976\n" qA "a[36:39],v[84:87],v[128:131],a[36:39]\n" qE "v41,s[44:47],s56 offen offset:64 lds\n" qA "v[8:11],v[68:71],v[28:31],v[8:11]\n" \
qC "v[96:97],v33 offset:20480\n" qA "a[40:43],v[48:51],v[132:135],a[40:43]\n" qN "m0,s62,3968\n" qA "a[44:47],v[52:55],v[132:135],a[44:47]\n" \
qC "v[98:99],v33 offset:20736\n" qA "a[48:51],v[56:59],v[132:135],a[48:51]\n" qE "v41,s[44:47],s56 offen offset:128 lds\n" qA "a[52:55],v[60:63],v[132:135],a[52:55]\n" \
qC "v[100:101],v33 offset:30720\n" qA "a[56:59],v[64:67],v[132:135],a[56:59]\n" qN "m0,s62,6144\n" qA "a[60:63],v[68:71],v[132:135],a[60:63]\n" \
qC "v[102:103],v33 offset:30976\n" qA "a[64:67],v[72:75],v[132:135],a[64:67]\n" qE "v42,s[48:51],s57" qU qA "a[68:71],v[76:79],v[132:135],a[68:71]\n" \
qC "v[104:105],v32 offset:22528\n" qA "a[72:75],v[80:83],v[132:135],a[72:75]\n" qN "m0,s62,8192\n" qA "a[76:79],v[84:87],v[132:135],a[76:79]\n" \
qC "v[106:107],v32 offset:22784\n" qA "a[80:83],v[48:51],v[136:139],a[80:83]\n" qE "v43,s[52:55],s58" qU qA "a[84:87],v[52:55],v[136:139],a[84:87]\n" \
qC "v[108:109],v32 offset:32768\n" qA "a[88:91],v[56:59],v[136:139],a[88:91]\n" qA "a[92:95],v[60:63],v[136:139],a[92:95]\n" qC "v[110:111],v32 offset:33024\n" \
qA "a[96:99],v[64:67],v[136:139],a[96:99]\n" qA "a[100:103],v[68:71],v[136:139],a[100:103]\n" qC "v[112:113],v33 offset:22528\n" qA "v[12:15],v[72:75],v[28:31],v[12:15]\n" \
qA "a[104:107],v[72:75],v[136:139],a[104:107]\n" qC "v[114:115],v33 offset:22784\n" qA "a[108:111],v[76:79],v[136:139],a[108:111]\n" qA "a[112:115],v[80:83],v[136:139],a[112:115]\n" \
qC "v[116:117],v33 offset:32768\n" qA "a[116:119],v[84:87],v[136:139],a[116:119]\n" qA "a[120:123],v[48:51],v[140:143],a[120:123]\n" qC "v[118:119],v33 offset:33024\n" \
qA "a[124:127],v[52:55],v[140:143],a[124:127]\n" qA "a[128:131],v[56:59],v[140:143],a[128:131]\n" qC "v[120:121],v32 offset:36864\n" qA "a[132:135],v[60:63],v[140:143],a[132:135]\n" \
qA "a[136:139],v[64:67],v[140:143],a[136:139]\n" qC "v[122:123],v32 offset:37120\n" qA "a[140:143],v[68:71],v[140:143],a[140:143]\n" qA "a[144:147],v[72:75],v[140:143],a[144:147]\n" \
qC "v[124:125],v33 offset:36864\n" qA "a[148:151],v[76:79],v[140:143],a[148:151]\n" qA "a[152:155],v[80:83],v[140:143],a[152:155]\n" qC "v[126:127],v33 offset:37120\n" \
qA "a[156:159],v[84:87],v[140:143],a[156:159]\n" qA "a[160:163],v[48:51],v[144:147],a[160:163]\n" qD "v[160:163],v39 offset:0\n" qA "a[164:167],v[52:55],v[144:147],a[164:167]\n" \
qA "v[16:19],v[76:79],v[28:31],v[16:19]\n" qD "v[164:167],v39 offset:2048\n" qA "a[168:171],v[56:59],v[144:147],a[168:171]\n" qA "a[172:175],v[60:63],v[144:147],a[172:175]\n" \
qD "v[168:171],v39 offset:4096\n" qA "a[176:179],v[64:67],v[144:147],a[176:179]\n" qA "a[180:183],v[68:71],v[144:147],a[180:183]\n" qD "v[172:175],v39 offset:6144\n" \
qA "a[184:187],v[72:75],v[144:147],a[184:187]\n" qA "a[188:191],v[76:79],v[144:147],a[188:191]\n" qD "v[176:179],v39 offset:8192\n" qA "a[192:195],v[80:83],v[144:147],a[192:195]\n" \
qA "a[196:199],v[84:87],v[144:147],a[196:199]\n" qD "v[180:183],v39 offset:10240\n" qA "a[200:203],v[48:51],v[148:151],a[200:203]\n" qA "a[204:207],v[52:55],v[148:151],a[204:207]\n" \
qD "v[184:187],v39 offset:12288\n" qA "a[208:211],v[56:59],v[148:151],a[208:211]\n" qA "a[212:215],v[60:63],v[148:151],a[212:215]\n" qD "v[188:191],v39 offset:14336\n" \
qA "a[216:219],v[64:67],v[148:151],a[216:219]\n" qA "a[220:223],v[68:71],v[148:151],a[220:223]\n" qA "a[224:227],v[72:75],v[148:151],a[224:227]\n" qA "a[228:231],v[76:79],v[148:151],a[228:231]\n" \
qN "s56,s56,229376\n" qA "v[20:23],v[80:83],v[28:31],v[20:23]\n" qN "s57,s57,196608\n" qA "a[232:235],v[80:83],v[148:151],a[232:235]\n" \
qN "s58,s58,s60\n" qA "a[236:239],v[84:87],v[148:151],a[236:239]\n" "s_min_u32 s56,s56,7110656\n" qA "a[240:243],v[48:51],v[152:155],a[240:243]\n" \
"s_min_u32 s57,s57,6094848\n" qA "a[244:247],v[52:55],v[152:155],a[244:247]\n" "s_min_u32 s58,s58,s72\n" qA "a[248:251],v[56:59],v[152:155],a[248:251]\n" \
qA "a[252:255],v[60:63],v[152:155],a[252:255]\n" qA "v[192:195],v[64:67],v[152:155],v[192:195]\n" qA "v[196:199],v[68:71],v[152:155],v[196:199]\n" qA "v[200:203],v[72:75],v[152:155],v[200:203]\n" \
"s_mov_b32 m0,s64\n" qA "v[204:207],v[76:79],v[152:155],v[204:207]\n" qA "v[208:211],v[80:83],v[152:155],v[208:211]\n" qE "v46,s[84:87],0" qV \
qA "v[212:215],v[84:87],v[152:155],v[212:215]\n" qA "v[216:219],v[48:51],v[156:159],v[216:219]\n" qA "v[220:223],v[52:55],v[156:159],v[220:223]\n" qA "v[224:227],v[56:59],v[156:159],v[224:227]\n" \
qA "v[228:231],v[60:63],v[156:159],v[228:231]\n" qA "v[24:27],v[84:87],v[28:31],v[24:27]\n" qA "v[232:235],v[64:67],v[156:159],v[232:235]\n" qA "v[236:239],v[68:71],v[156:159],v[236:239]\n" \
qA "v[240:243],v[72:75],v[156:159],v[240:243]\n" qA "v[244:247],v[76:79],v[156:159],v[244:247]\n" qA "v[248:251],v[80:83],v[156:159],v[248:251]\n" "s_sub_u32 s63,s63,1\n" \
"s_cmp_lg_u32 s63,0\n" qA "v[252:255],v[84:87],v[156:159],v[252:255]\n" "s_cbranch_scc1 G1L2_%=\n" "G1TAIL2_%=:\n" \
qO "lgkmcnt(0)\n" qA "a[0:3],v[88:91],v[160:163],a[0:3]\n" qO "vmcnt(11)\n" qA "a[4:7],v[92:95],v[160:163],a[4:7]\n" \
"s_barrier\n" qA "a[8:11],v[96:99],v[160:163],a[8:11]\n" qC "v[48:49],v35 offset:0\n" qA "a[12:15],v[100:103],v[160:163],a[12:15]\n" \
"v_mov_b32 v2,-1\n" qA "a[16:19],v[104:107],v[160:163],a[16:19]\n" qC "v[50:51],v35 offset:256\n" "v_mov_b32 v3,-1\n" \
qA "a[20:23],v[108:111],v[160:163],a[20:23]\n" "v_mov_b32 v4,-1\n" qA "a[24:27],v[112:115],v[160:163],a[24:27]\n" qC "v[52:53],v35 offset:10240\n" \
"v_mov_b32 v5,-1\n" qA "a[28:31],v[116:119],v[160:163],a[28:31]\n" qA "a[32:35],v[120:123],v[160:163],a[32:35]\n" qC "v[54:55],v35 offset:10496\n" \
qA "a[36:39],v[124:127],v[160:163],a[36:39]\n" qF "v[2:5],v47,s[84:87],s90 offen offset:0 sc1\n" qA "v[8:11],v[108:111],v[28:31],v[8:11]\n" qC "v[56:57],v36 offset:0\n" \
qA "a[40:43],v[88:91],v[164:167],a[40:43]\n" qA "a[44:47],v[92:95],v[164:167],a[44:47]\n" qC "v[58:59],v36 offset:256\n" qA "a[48:51],v[96:99],v[164:167],a[48:51]\n" \
qA "a[52:55],v[100:103],v[164:167],a[52:55]\n" qC "v[60:61],v36 offset:10240\n" qA "a[56:59],v[104:107],v[164:167],a[56:59]\n" qA "a[60:63],v[108:111],v[164:167],a[60:63]\n" \
qC "v[62:63],v36 offset:10496\n" qA "a[64:67],v[112:115],v[164:167],a[64:67]\n" qA "a[68:71],v[116:119],v[164:167],a[68:71]\n" qC "v[64:65],v35 offset:2048\n" \
qA "a[72:75],v[120:123],v[164:167],a[72:75]\n" qA "a[76:79],v[124:127],v[164:167],a[76:79]\n" qC "v[66:67],v35 offset:2304\n" qA "a[80:83],v[88:91],v[168:171],a[80:83]\n" \
qF "v[2:5],v47,s[84:87],s90 offen offset:1024 sc1\n" qA "a[84:87],v[92:95],v[168:171],a[84:87]\n" qC "v[68:69],v35 offset:12288\n" qA "a[88:91],v[96:99],v[168:171],a[88:91]\n" \
qA "a[92:95],v[100:103],v[168:171],a[92:95]\n" qC "v[70:71],v35 offset:12544\n" qA "a[96:99],v[104:107],v[168:171],a[96:99]\n" qA "a[100:103],v[108:111],v[168:171],a[100:103]\n" \
qC "v[72:73],v36 offset:2048\n" qA "v[12:15],v[112:115],v[28:31],v[12:15]\n" qA "a[104:107],v[112:115],v[168:171],a[104:107]\n" qC "v[74:75],v36 offset:2304\n" \
qA "a[108:111],v[116:119],v[168:171],a[108:111]\n" qA "a[112:115],v[120:123],v[168:171],a[112:115]\n" qC "v[76:77],v36 offset:12288\n" qA "a[116:119],v[124:127],v[168:171],a[116:119]\n" \
qA "a[120:123],v[88:91],v[172:175],a[120:123]\n" qC "v[78:79],v36 offset:12544\n" qA "a[124:127],v[92:95],v[172:175],a[124:127]\n" qF "v[2:5],v47,s[84:87],s90 offen offset:2048 sc1\n" \
qA "a[128:131],v[96:99],v[172:175],a[128:131]\n" qC "v[80:81],v35 offset:16384\n" qA "a[132:135],v[100:103],v[172:175],a[132:135]\n" qA "a[136:139],v[104:107],v[172:175],a[136:139]\n" \
qC "v[82:83],v35 offset:16640\n" qA "a[140:143],v[108:111],v[172:175],a[140:143]\n" qA "a[144:147],v[112:115],v[172:175],a[144:147]\n" qC "v[84:85],v36 offset:16384\n" \
qA "a[148:151],v[116:119],v[172:175],a[148:151]\n" qA "a[152:155],v[120:123],v[172:175],a[152:155]\n" qC "v[86:87],v36 offset:16640\n" qA "a[156:159],v[124:127],v[172:175],a[156:159]\n" \
qA "a[160:163],v[88:91],v[176:179],a[160:163]\n" qD "v[128:131],v38 offset:32768\n" qA "a[164:167],v[92:95],v[176:179],a[164:167]\n" qA "v[16:19],v[116:119],v[28:31],v[16:19]\n" \
qD "v[132:135],v38 offset:34816\n" qA "a[168:171],v[96:99],v[176:179],a[168:171]\n" qF "v[2:5],v47,s[84:87],s90 offen offset:3072 sc1\n" qA "a[172:175],v[100:103],v[176:179],a[172:175]\n" \
qD "v[136:139],v38 offset:36864\n" qA "a[176:179],v[104:107],v[176:179],a[176:179]\n" qA "a[180:183],v[108:111],v[176:179],a[180:183]\n" qD "v[140:143],v38 offset:38912\n" \
qA "a[184:187],v[112:115],v[176:179],a[184:187]\n" qA "a[188:191],v[116:119],v[176:179],a[188:191]\n" qD "v[144:147],v38 offset:40960\n" qA "a[192:195],v[120:123],v[176:179],a[192:195]\n" \
qA "a[196:199],v[124:127],v[176:179],a[196:199]\n" qD "v[148:151],v38 offset:43008\n" qA "a[200:203],v[88:91],v[180:183],a[200:203]\n" qA "a[204:207],v[92:95],v[180:183],a[204:207]\n" \
qD "v[152:155],v38 offset:45056\n" qA "a[208:211],v[96:99],v[180:183],a[208:211]\n" qA "a[212:215],v[100:103],v[180:183],a[212:215]\n" qD "v[156:159],v38 offset:47104\n" \
qA "a[216:219],v[104:107],v[180:183],a[216:219]\n" qA "a[220:223],v[108:111],v[180:183],a[220:223]\n" "s_mov_b32 m0,s73\n" qA "a[224:227],v[112:115],v[180:183],a[224:227]\n" \
qA "a[228:231],v[116:119],v[180:183],a[228:231]\n" "global_load_lds_dwordx4 v47,s[100:101] offset:0\n" qA "v[20:23],v[120:123],v[28:31],v[20:23]\n" qA "a[232:235],v[120:123],v[180:183],a[232:235]\n" \
"global_load_lds_dwordx4 v47,s[100:101] offset:1024\n" qA "a[236:239],v[124:127],v[180:183],a[236:239]\n" qA "a[240:243],v[88:91],v[184:187],a[240:243]\n" "global_load_lds_dwordx4 v47,s[100:101] offset:2048\n" \
qA "a[244:247],v[92:95],v[184:187],a[244:247]\n" qA "a[248:251],v[96:99],v[184:187],a[248:251]\n" "global_load_lds_dwordx4 v47,s[100:101] offset:3072\n" qA "a[252:255],v[100:103],v[184:187],a[252:255]\n" \
qA "v[192:195],v[104:107],v[184:187],v[192:195]\n" "global_load_lds_dwordx4 v44,s[100:101] offset:0\n" qA "v[196:199],v[108:111],v[184:187],v[196:199]\n" qA "v[200:203],v[112:115],v[184:187],v[200:203]\n" \
"global_load_lds_dwordx4 v44,s[100:101] offset:1024\n" qA "v[204:207],v[116:119],v[184:187],v[204:207]\n" qA "v[208:211],v[120:123],v[184:187],v[208:211]\n" "global_load_lds_dwordx4 v44,s[100:101] offset:2048\n" \
qA "v[212:215],v[124:127],v[184:187],v[212:215]\n" qA "v[216:219],v[88:91],v[188:191],v[216:219]\n" "global_load_lds_dwordx4 v47,s[82:83] offset:0\n" qA "v[220:223],v[92:95],v[188:191],v[220:223]\n" \
qA "v[224:227],v[96:99],v[188:191],v[224:227]\n" "global_load_lds_dwordx4 v47,s[82:83] offset:1024\n" qA "v[228:231],v[100:103],v[188:191],v[228:231]\n" qA "v[24:27],v[124:127],v[28:31],v[24:27]\n" \
qA "v[232:235],v[104:107],v[188:191],v[232:235]\n" qA "v[236:239],v[108:111],v[188:191],v[236:239]\n" qA "v[240:243],v[112:115],v[188:191],v[240:243]\n" qA "v[244:247],v[116:119],v[188:191],v[244:247]\n" \
qA "v[248:251],v[120:123],v[188:191],v[248:251]\n" qA "v[252:255],v[124:127],v[188:191],v[252:255]\n" qO "lgkmcnt(0)\n" qA "a[0:3],v[48:51],v[128:131],a[0:3]\n" \
qO "vmcnt(13)\n" qA "a[4:7],v[52:55],v[128:131],a[4:7]\n" "s_barrier\n" qA "a[8:11],v[56:59],v[128:131],a[8:11]\n" \
qC "v[88:89],v35 offset:20480\n" qA "a[12:15],v[60:63],v[128:131],a[12:15]\n" qA "a[16:19],v[64:67],v[128:131],a[16:19]\n" qC "v[90:91],v35 offset:20736\n" \
qA "a[20:23],v[68:71],v[128:131],a[20:23]\n" qA "a[24:27],v[72:75],v[128:131],a[24:27]\n" qC "v[92:93],v35 offset:30720\n" qA "a[28:31],v[76:79],v[128:131],a[28:31]\n" \
qA "a[32:35],v[80:83],v[128:131],a[32:35]\n" qC "v[94:95],v35 offset:30976\n" qA "a[36:39],v[84:87],v[128:131],a[36:39]\n" qA "v[8:11],v[68:71],v[28:31],v[8:11]\n" \
qC "v[96:97],v36 offset:20480\n" qA "a[40:43],v[48:51],v[132:135],a[40:43]\n" qA "a[44:47],v[52:55],v[132:135],a[44:47]\n" qC "v[98:99],v36 offset:20736\n" \
qA "a[48:51],v[56:59],v[132:135],a[48:51]\n" qA "a[52:55],v[60:63],v[132:135],a[52:55]\n" qC "v[100:101],v36 offset:30720\n" qA "a[56:59],v[64:67],v[132:135],a[56:59]\n" \
qA "a[60:63],v[68:71],v[132:135],a[60:63]\n" qC "v[102:103],v36 offset:30976\n" qA "a[64:67],v[72:75],v[132:135],a[64:67]\n" qA "a[68:71],v[76:79],v[132:135],a[68:71]\n" \
qC "v[104:105],v35 offset:22528\n" qA "a[72:75],v[80:83],v[132:135],a[72:75]\n" qA "a[76:79],v[84:87],v[132:135],a[76:79]\n" qC "v[106:107],v35 offset:22784\n" \
qA "a[80:83],v[48:51],v[136:139],a[80:83]\n" qA "a[84:87],v[52:55],v[136:139],a[84:87]\n" qC "v[108:109],v35 offset:32768\n" qA "a[88:91],v[56:59],v[136:139],a[88:91]\n" \
qA "a[92:95],v[60:63],v[136:139],a[92:95]\n" qC "v[110:111],v35 offset:33024\n" qA "a[96:99],v[64:67],v[136:139],a[96:99]\n" qA "a[100:103],v[68:71],v[136:139],a[100:103]\n" \
qC "v[112:113],v36 offset:22528\n" qA "v[12:15],v[72:75],v[28:31],v[12:15]\n" qA "a[104:107],v[72:75],v[136:139],a[104:107]\n" qC "v[114:115],v36 offset:22784\n" \
qA "a[108:111],v[76:79],v[136:139],a[108:111]\n" qA "a[112:115],v[80:83],v[136:139],a[112:115]\n" qC "v[116:117],v36 offset:32768\n" qA "a[116:119],v[84:87],v[136:139],a[116:119]\n" \
qA "a[120:123],v[48:51],v[140:143],a[120:123]\n" qC "v[118:119],v36 offset:33024\n" qA "a[124:127],v[52:55],v[140:143],a[124:127]\n" qA "a[128:131],v[56:59],v[140:143],a[128:131]\n" \
qC "v[120:121],v35 offset:36864\n" qA "a[132:135],v[60:63],v[140:143],a[132:135]\n" qA "a[136:139],v[64:67],v[140:143],a[136:139]\n" qC "v[122:123],v35 offset:37120\n" \
qA "a[140:143],v[68:71],v[140:143],a[140:143]\n" qA "a[144:147],v[72:75],v[140:143],a[144:147]\n" qC "v[124:125],v36 offset:36864\n" qA "a[148:151],v[76:79],v[140:143],a[148:151]\n" \
qA "a[152:155],v[80:83],v[140:143],a[152:155]\n" qC "v[126:127],v36 offset:37120\n" qA "a[156:159],v[84:87],v[140:143],a[156:159]\n" qA "a[160:163],v[48:51],v[144:147],a[160:163]\n" \
qD "v[160:163],v39 offset:32768\n" qA "a[164:167],v[52:55],v[144:147],a[164:167]\n" qA "v[16:19],v[76:79],v[28:31],v[16:19]\n" qD "v[164:167],v39 offset:34816\n" \
qA "a[168:171],v[56:59],v[144:147],a[168:171]\n" qA "a[172:175],v[60:63],v[144:147],a[172:175]\n" qD "v[168:171],v39 offset:36864\n" qA "a[176:179],v[64:67],v[144:147],a[176:179]\n" \
qA "a[180:183],v[68:71],v[144:147],a[180:183]\n" qD "v[172:175],v39 offset:38912\n" qA "a[184:187],v[72:75],v[144:147],a[184:187]\n" qA "a[188:191],v[76:79],v[144:147],a[188:191]\n" \
qD "v[176:179],v39 offset:40960\n" qA "a[192:195],v[80:83],v[144:147],a[192:195]\n" qA "a[196:199],v[84:87],v[144:147],a[196:199]\n" qD "v[180:183],v39 offset:43008\n" \
qA "a[200:203],v[48:51],v[148:151],a[200:203]\n" qA "a[204:207],v[52:55],v[148:151],a[204:207]\n" qD "v[184:187],v39 offset:45056\n" qA "a[208:211],v[56:59],v[148:151],a[208:211]\n" \
qA "a[212:215],v[60:63],v[148:151],a[212:215]\n" qD "v[188:191],v39 offset:47104\n" qA "a[216:219],v[64:67],v[148:151],a[216:219]\n" qA "a[220:223],v[68:71],v[148:151],a[220:223]\n" \
qA "a[224:227],v[72:75],v[148:151],a[224:227]\n" qA "a[228:231],v[76:79],v[148:151],a[228:231]\n" qA "v[20:23],v[80:83],v[28:31],v[20:23]\n" qA "a[232:235],v[80:83],v[148:151],a[232:235]\n" \
qA "a[236:239],v[84:87],v[148:151],a[236:239]\n" qA "a[240:243],v[48:51],v[152:155],a[240:243]\n" qA "a[244:247],v[52:55],v[152:155],a[244:247]\n" qA "a[248:251],v[56:59],v[152:155],a[248:251]\n" \
qA "a[252:255],v[60:63],v[152:155],a[252:255]\n" qA "v[192:195],v[64:67],v[152:155],v[192:195]\n" qA "v[196:199],v[68:71],v[152:155],v[196:199]\n" qA "v[200:203],v[72:75],v[152:155],v[200:203]\n" \
qA "v[204:207],v[76:79],v[152:155],v[204:207]\n" qA "v[208:211],v[80:83],v[152:155],v[208:211]\n" qA "v[212:215],v[84:87],v[152:155],v[212:215]\n" qA "v[216:219],v[48:51],v[156:159],v[216:219]\n" \
qA "v[220:223],v[52:55],v[156:159],v[220:223]\n" qA "v[224:227],v[56:59],v[156:159],v[224:227]\n" qA "v[228:231],v[60:63],v[156:159],v[228:231]\n" qA "v[24:27],v[84:87],v[28:31],v[24:27]\n" \
qA "v[232:235],v[64:67],v[156:159],v[232:235]\n" qA "v[236:239],v[68:71],v[156:159],v[236:239]\n" qA "v[240:243],v[72:75],v[156:159],v[240:243]\n" qA "v[244:247],v[76:79],v[156:159],v[244:247]\n" \
qA "v[248:251],v[80:83],v[156:159],v[248:251]\n" qA "v[252:255],v[84:87],v[156:159],v[252:255]\n" qO "lgkmcnt(0)\n" qA "a[0:3],v[88:91],v[160:163],a[0:3]\n" \
qA "a[4:7],v[92:95],v[160:163],a[4:7]\n" qA "a[8:11],v[96:99],v[160:163],a[8:11]\n" qD "v[48:51],v7 offset:0\n" qA "a[12:15],v[100:103],v[160:163],a[12:15]\n" \
qD "v[52:55],v7 offset:256\n" qA "a[16:19],v[104:107],v[160:163],a[16:19]\n" qD "v[56:59],v7 offset:512\n" qA "a[20:23],v[108:111],v[160:163],a[20:23]\n" \
qD "v[60:63],v7 offset:768\n" qA "a[24:27],v[112:115],v[160:163],a[24:27]\n" qD "v[64:67],v7 offset:1024\n" qA "a[28:31],v[116:119],v[160:163],a[28:31]\n" \
qD "v[68:71],v7 offset:1280\n" qA "a[32:35],v[120:123],v[160:163],a[32:35]\n" qD "v[72:75],v7 offset:1536\n" qA "a[36:39],v[124:127],v[160:163],a[36:39]\n" \
qD "v[76:79],v7 offset:1792\n" qA "v[8:11],v[108:111],v[28:31],v[8:11]\n" qA "a[40:43],v[88:91],v[164:167],a[40:43]\n" qA "a[44:47],v[92:95],v[164:167],a[44:47]\n" \
qA "a[48:51],v[96:99],v[164:167],a[48:51]\n" qA "a[52:55],v[100:103],v[164:167],a[52:55]\n" qA "a[56:59],v[104:107],v[164:167],a[56:59]\n" qA "a[60:63],v[108:111],v[164:167],a[60:63]\n" \
qA "a[64:67],v[112:115],v[164:167],a[64:67]\n" qA "a[68:71],v[116:119],v[164:167],a[68:71]\n" qA "a[72:75],v[120:123],v[164:167],a[72:75]\n" qA "a[76:79],v[124:127],v[164:167],a[76:79]\n" \
qA "a[80:83],v[88:91],v[168:171],a[80:83]\n" qA "a[84:87],v[92:95],v[168:171],a[84:87]\n" qA "a[88:91],v[96:99],v[168:171],a[88:91]\n" qA "a[92:95],v[100:103],v[168:171],a[92:95]\n" \
qA "a[96:99],v[104:107],v[168:171],a[96:99]\n" qA "a[100:103],v[108:111],v[168:171],a[100:103]\n" qA "v[12:15],v[112:115],v[28:31],v[12:15]\n" qA "a[104:107],v[112:115],v[168:171],a[104:107]\n" \
qA "a[108:111],v[116:119],v[168:171],a[108:111]\n" qA "a[112:115],v[120:123],v[168:171],a[112:115]\n" qO "lgkmcnt(0)\n" qA "a[116:119],v[124:127],v[168:171],a[116:119]\n" \
"v_min3_u32 v84,v48,v50,v52\n" qA "a[120:123],v[88:91],v[172:175],a[120:123]\n" "v_max3_u32 v85,v48,v50,v52\n" qA "a[124:127],v[92:95],v[172:175],a[124:127]\n" \
"v_min3_u32 v84,v84,v54,v56\n" qA "a[128:131],v[96:99],v[172:175],a[128:131]\n" "v_max3_u32 v85,v85,v54,v56\n" qA "a[132:135],v[100:103],v[172:175],a[132:135]\n" \
"v_min3_u32 v84,v84,v58,v60\n" qA "a[136:139],v[104:107],v[172:175],a[136:139]\n" "v_max3_u32 v85,v85,v58,v60\n" qA "a[140:143],v[108:111],v[172:175],a[140:143]\n" \
"v_min3_u32 v84,v84,v62,v64\n" qA "a[144:147],v[112:115],v[172:175],a[144:147]\n" "v_max3_u32 v85,v85,v62,v64\n" qA "a[148:151],v[116:119],v[172:175],a[148:151]\n" \
"v_min3_u32 v84,v84,v66,v68\n" qA "a[152:155],v[120:123],v[172:175],a[152:155]\n" "v_max3_u32 v85,v85,v66,v68\n" qA "a[156:159],v[124:127],v[172:175],a[156:159]\n" \
"v_min3_u32 v84,v84,v70,v72\n" qA "a[160:163],v[88:91],v[176:179],a[160:163]\n" "v_max3_u32 v85,v85,v70,v72\n" qA "a[164:167],v[92:95],v[176:179],a[164:167]\n" \
"v_min3_u32 v84,v84,v74,v76\n" qA "v[16:19],v[116:119],v[28:31],v[16:19]\n" "v_max3_u32 v85,v85,v74,v76\n" qA "a[168:171],v[96:99],v[176:179],a[168:171]\n" \
"v_min_u32 v84,v84,v78\n" qA "a[172:175],v[100:103],v[176:179],a[172:175]\n" "v_max_u32 v85,v85,v78\n" qA "a[176:179],v[104:107],v[176:179],a[176:179]\n" \
qA "a[180:183],v[108:111],v[176:179],a[180:183]\n" qA "a[184:187],v[112:115],v[176:179],a[184:187]\n" "v_cmp_ne_u32 s[92:93],s75,v84\n" qA "a[188:191],v[116:119],v[176:179],a[188:191]\n" \
"v_cmp_ne_u32 vcc,s75,v85\n" qA "a[192:195],v[120:123],v[176:179],a[192:195]\n" qA "a[196:199],v[124:127],v[176:179],a[196:199]\n" qA "a[200:203],v[88:91],v[180:183],a[200:203]\n" \
qA "a[204:207],v[92:95],v[180:183],a[204:207]\n" "s_or_b64 s[92:93],s[92:93],vcc\n" qA "a[208:211],v[96:99],v[180:183],a[208:211]\n" qA "a[212:215],v[100:103],v[180:183],a[212:215]\n" \
"v_and_b32 v86,0xf0,v47\n" qA "a[216:219],v[104:107],v[180:183],a[216:219]\n" "v_lshlrev_b32 v86,2,v86\n" qA "a[220:223],v[108:111],v[180:183],a[220:223]\n" \
"v_bfe_u32 v87,v47,8,1\n" qA "a[224:227],v[112:115],v[180:183],a[224:227]\n" "v_bfe_u32 v83,v47,9,1\n" qA "a[228:231],v[116:119],v[180:183],a[228:231]\n" \
"v_lshl_or_b32 v87,v87,1,v83\n" qA "v[20:23],v[120:123],v[28:31],v[20:23]\n" "v_bfe_u32 v83,v47,6,2\n" qA "a[232:235],v[120:123],v[180:183],a[232:235]\n" \
"v_xor_b32 v83,v83,v87\n" qA "a[236:239],v[124:127],v[180:183],a[236:239]\n" "v_lshl_add_u32 v83,v83,4,v86\n" qA "a[240:243],v[88:91],v[184:187],a[240:243]\n" \
"v_add_u32 v3,8192,v83\n" qA "a[244:247],v[92:95],v[184:187],a[244:247]\n" "v_lshl_add_u32 v87,v87,4,v86\n" qA "a[248:251],v[96:99],v[184:187],a[248:251]\n" \
"v_add_u32 v1,40960,v83\n" qA "a[252:255],v[100:103],v[184:187],a[252:255]\n" "v_add_u32 v2,8192,v87\n" qA "v[192:195],v[104:107],v[184:187],v[192:195]\n" \
"v_lshrrev_b32 v83,8,v47\n" qA "v[196:199],v[108:111],v[184:187],v[196:199]\n" "v_lshlrev_b32 v83,4,v83\n" qA "v[200:203],v[112:115],v[184:187],v[200:203]\n" \
"v_add_u32 v5,147456,v83\n" qA "v[204:207],v[116:119],v[184:187],v[204:207]\n" "v_add_u32 v4,320,v5\n" qA "v[208:211],v[120:123],v[184:187],v[208:211]\n" \
qN "s56,s76,4096\n" qA "v[212:215],v[124:127],v[184:187],v[212:215]\n" qA "v[216:219],v[88:91],v[188:191],v[216:219]\n" qA "v[220:223],v[92:95],v[188:191],v[220:223]\n" \
qA "v[224:227],v[96:99],v[188:191],v[224:227]\n" qA "v[228:231],v[100:103],v[188:191],v[228:231]\n" qA "v[24:27],v[124:127],v[28:31],v[24:27]\n" qA "v[232:235],v[104:107],v[188:191],v[232:235]\n" \
qA "v[236:239],v[108:111],v[188:191],v[236:239]\n" qA "v[240:243],v[112:115],v[188:191],v[240:243]\n" qA "v[244:247],v[116:119],v[188:191],v[244:247]\n" qA "v[248:251],v[120:123],v[188:191],v[248:251]\n" \
qA "v[252:255],v[124:127],v[188:191],v[252:255]\n" "s_cmp_eq_u64 s[92:93],0\n" "s_cbranch_scc1 G1OK2_%=\n" "s_mov_b32 s63,0x100000\n" \
"v_subrev_u32 v82,s64,v7\n" "v_add_u32 v82,2048,v82\n" "G1RE2_%=:\n" "s_sleep 1\n" \
qE "v[48:51],v82,s[84:87],0 offen offset:0 sc1\n" qE "v[52:55],v82,s[84:87],0 offen offset:256 sc1\n" qE "v[56:59],v82,s[84:87],0 offen offset:512 sc1\n" qE "v[60:63],v82,s[84:87],0 offen offset:768 sc1\n" \
qE "v[64:67],v82,s[84:87],0 offen offset:1024 sc1\n" qE "v[68:71],v82,s[84:87],0 offen offset:1280 sc1\n" qE "v[72:75],v82,s[84:87],0 offen offset:1536 sc1\n" qE "v[76:79],v82,s[84:87],0 offen offset:1792 sc1\n" \
qO "vmcnt(0)\n" "v_min3_u32 v84,v48,v50,v52\n" "v_max3_u32 v85,v48,v50,v52\n" "v_min3_u32 v84,v84,v54,v56\n" \
"v_max3_u32 v85,v85,v54,v56\n" "v_min3_u32 v84,v84,v58,v60\n" "v_max3_u32 v85,v85,v58,v60\n" "v_min3_u32 v84,v84,v62,v64\n" \
"v_max3_u32 v85,v85,v62,v64\n" "v_min3_u32 v84,v84,v66,v68\n" "v_max3_u32 v85,v85,v66,v68\n" "v_min3_u32 v84,v84,v70,v72\n" \
"v_max3_u32 v85,v85,v70,v72\n" "v_min3_u32 v84,v84,v74,v76\n" "v_max3_u32 v85,v85,v74,v76\n" "v_min_u32 v84,v84,v78\n" \
"v_max_u32 v85,v85,v78\n" "v_cmp_ne_u32 s[92:93],s75,v84\n" "v_cmp_ne_u32 vcc,s75,v85\n" "s_nop 3\n" \
"s_or_b64 s[92:93],s[92:93],vcc\n" "s_sub_u32 s63,s63,1\n" "s_cmp_eq_u32 s63,0\n" "s_cbranch_scc1 G1OK2_%=\n" \
"s_cmp_eq_u64 s[92:93],0\n" "s_cbranch_scc0 G1RE2_%=\n" "G1OK2_%=:\n" "s_nop 7\n" \
"s_mov_b32 exec_lo,0x00010001\n" "s_mov_b32 exec_hi,0x00010001\n" qQ "v4,v[8:11] offset:0\n" qQ "v4,v[12:15] offset:64\n" \
qQ "v4,v[16:19] offset:128\n" qQ "v4,v[20:23] offset:192\n" qQ "v4,v[24:27] offset:256\n" "s_mov_b64 exec,-1\n" \
qO "lgkmcnt(0)\n" "s_barrier\n" qD "v[8:11],v5 offset:0\n" qD "v[12:15],v5 offset:64\n" \
qD "v[16:19],v5 offset:128\n" qD "v[20:23],v5 offset:192\n" qD "v[24:27],v5 offset:256\n" qD "v[28:31],v5 offset:320\n" \
qD "v[32:35],v5 offset:384\n" qD "v[36:39],v5 offset:448\n" qD "v[40:43],v5 offset:512\n" qD "v[44:47],v5 offset:576\n" \
qO "lgkmcnt(0)\n" qG "v80,a32\n" qG "v81,a33\n" qG "v82,a34\n" \
qG "v83,a35\n" qG "v84,a36\n" qG "v85,a37\n" qG "v86,a38\n" \
qG "v87,a39\n" qG "v88,a72\n" qG "v89,a73\n" qG "v90,a74\n" \
qG "v91,a75\n" qG "v92,a76\n" qG "v93,a77\n" qG "v94,a78\n" \
qG "v95,a79\n" qG "v96,a112\n" qG "v97,a113\n" qG "v98,a114\n" \
qG "v99,a115\n" qG "v100,a116\n" qG "v101,a117\n" qG "v102,a118\n" \
qG "v103,a119\n" qG "v104,a152\n" qG "v105,a153\n" qG "v106,a154\n" \
qG "v107,a155\n" qG "v108,a156\n" qG "v109,a157\n" qG "v110,a158\n" \
qG "v111,a159\n" qH "v[80:81],v[48:49],v[40:41],v[80:81]" qR qH "v[82:83],v[48:49],v[42:43],v[82:83]" qR qH "v[84:85],v[48:49],v[44:45],v[84:85]" qR \
qH "v[86:87],v[48:49],v[46:47],v[86:87]" qR qH "v[88:89],v[52:53],v[40:41],v[88:89]" qR qH "v[90:91],v[52:53],v[42:43],v[90:91]" qR qH "v[92:93],v[52:53],v[44:45],v[92:93]" qR \
qH "v[94:95],v[52:53],v[46:47],v[94:95]" qR qH "v[96:97],v[56:57],v[40:41],v[96:97]" qR qH "v[98:99],v[56:57],v[42:43],v[98:99]" qR qH "v[100:101],v[56:57],v[44:45],v[100:101]" qR \
qH "v[102:103],v[56:57],v[46:47],v[102:103]" qR qH "v[104:105],v[60:61],v[40:41],v[104:105]" qR qH "v[106:107],v[60:61],v[42:43],v[106:107]" qR qH "v[108:109],v[60:61],v[44:45],v[108:109]" qR \
qH "v[110:111],v[60:61],v[46:47],v[110:111]" qR qI "v[80:81],v[80:81],v[50:51]" qS qI "v[82:83],v[82:83],v[50:51]" qS qI "v[84:85],v[84:85],v[50:51]" qS \
qI "v[86:87],v[86:87],v[50:51]" qS qI "v[88:89],v[88:89],v[54:55]" qS qI "v[90:91],v[90:91],v[54:55]" qS qI "v[92:93],v[92:93],v[54:55]" qS \
qI "v[94:95],v[94:95],v[54:55]" qS qI "v[96:97],v[96:97],v[58:59]" qS qI "v[98:99],v[98:99],v[58:59]" qS qI "v[100:101],v[100:101],v[58:59]" qS \
qI "v[102:103],v[102:103],v[58:59]" qS qI "v[104:105],v[104:105],v[62:63]" qS qI "v[106:107],v[106:107],v[62:63]" qS qI "v[108:109],v[108:109],v[62:63]" qS \
qI "v[110:111],v[110:111],v[62:63]" qS qJ "v112,v80,v81\n" qJ "v113,v82,v83\n" qJ "v114,v84,v85\n" \
qJ "v115,v86,v87\n" qJ "v116,v88,v89\n" qJ "v117,v90,v91\n" qJ "v118,v92,v93\n" \
qJ "v119,v94,v95\n" qJ "v120,v96,v97\n" qJ "v121,v98,v99\n" qJ "v122,v100,v101\n" \
qJ "v123,v102,v103\n" qJ "v124,v104,v105\n" qJ "v125,v106,v107\n" qJ "v126,v108,v109\n" \
qJ "v127,v110,v111\n" qK "v112,v114\n" qK "v113,v115\n" qK "v116,v118\n" \
qK "v117,v119\n" qK "v120,v122\n" qK "v121,v123\n" qK "v124,v126\n" \
qK "v125,v127\n" qF "v[112:115],v2,s[84:87],s76 offen offset:0 sc1\n" qQ "v1,v[112:115] offset:0\n" qG "v80,a192\n" \
qG "v81,a193\n" qG "v82,a194\n" qG "v83,a195\n" qQ "v1,v[116:119] offset:1024\n" \
qG "v84,a196\n" qF "v[116:119],v2,s[84:87],s76 offen offset:1024 sc1\n" qG "v85,a197\n" qG "v86,a198\n" \
qG "v87,a199\n" qG "v88,a232\n" qG "v89,a233\n" qG "v90,a234\n" \
qG "v91,a235\n" qG "v92,a236\n" qF "v[120:123],v2,s[84:87],s76 offen offset:2048 sc1\n" qG "v93,a237\n" \
qG "v94,a238\n" qG "v95,a239\n" qQ "v1,v[120:123] offset:2048\n" qH "v[80:81],v[64:65],v[40:41],v[80:81]" qR \
qH "v[82:83],v[64:65],v[42:43],v[82:83]" qR qH "v[84:85],v[64:65],v[44:45],v[84:85]" qR qH "v[86:87],v[64:65],v[46:47],v[86:87]" qR qF "v[124:127],v2,s[84:87],s76 offen offset:3072 sc1\n" \
qH "v[88:89],v[68:69],v[40:41],v[88:89]" qR qH "v[90:91],v[68:69],v[42:43],v[90:91]" qR qH "v[92:93],v[68:69],v[44:45],v[92:93]" qR qH "v[94:95],v[68:69],v[46:47],v[94:95]" qR \
qH "v[96:97],v[72:73],v[40:41],v[208:209]" qR qH "v[98:99],v[72:73],v[42:43],v[210:211]" qR qH "v[100:101],v[72:73],v[44:45],v[212:213]" qR qH "v[102:103],v[72:73],v[46:47],v[214:215]" qR \
qH "v[104:105],v[76:77],v[40:41],v[248:249]" qR qH "v[106:107],v[76:77],v[42:43],v[250:251]" qR qH "v[108:109],v[76:77],v[44:45],v[252:253]" qR qH "v[110:111],v[76:77],v[46:47],v[254:255]" qR \
qQ "v1,v[124:127] offset:3072\n" qI "v[80:81],v[80:81],v[66:67]" qS qI "v[82:83],v[82:83],v[66:67]" qS qI "v[84:85],v[84:85],v[66:67]" qS \
qI "v[86:87],v[86:87],v[66:67]" qS qI "v[88:89],v[88:89],v[70:71]" qS qI "v[90:91],v[90:91],v[70:71]" qS qI "v[92:93],v[92:93],v[70:71]" qS \
qI "v[94:95],v[94:95],v[70:71]" qS qI "v[96:97],v[96:97],v[74:75]" qS qI "v[98:99],v[98:99],v[74:75]" qS qI "v[100:101],v[100:101],v[74:75]" qS \
qI "v[102:103],v[102:103],v[74:75]" qS qI "v[104:105],v[104:105],v[78:79]" qS qI "v[106:107],v[106:107],v[78:79]" qS qI "v[108:109],v[108:109],v[78:79]" qS \
qI "v[110:111],v[110:111],v[78:79]" qS qJ "v112,v80,v81\n" qJ "v113,v82,v83\n" qJ "v114,v84,v85\n" \
qJ "v115,v86,v87\n" qJ "v116,v88,v89\n" qJ "v117,v90,v91\n" qJ "v118,v92,v93\n" \
qJ "v119,v94,v95\n" qJ "v120,v96,v97\n" qJ "v121,v98,v99\n" qJ "v122,v100,v101\n" \
qJ "v123,v102,v103\n" qJ "v124,v104,v105\n" qJ "v125,v106,v107\n" qJ "v126,v108,v109\n" \
qJ "v127,v110,v111\n" qK "v112,v114\n" qK "v113,v115\n" qK "v116,v118\n" \
qK "v117,v119\n" qK "v120,v122\n" qK "v121,v123\n" qK "v124,v126\n" \
qK "v125,v127\n" qG "v80,a0\n" qG "v81,a1\n" qG "v82,a2\n" \
qG "v83,a3\n" qG "v88,a8\n" qG "v89,a9\n" qG "v90,a10\n" \
qG "v91,a11\n" qG "v96,a16\n" qG "v97,a17\n" qG "v98,a18\n" \
qG "v99,a19\n" qG "v104,a24\n" qG "v105,a25\n" qG "v106,a26\n" \
qG "v107,a27\n" qH "v[80:81],v[48:49],v[8:9],v[80:81]" qR qH "v[82:83],v[48:49],v[10:11],v[82:83]" qR qH "v[88:89],v[48:49],v[16:17],v[88:89]" qR \
qH "v[90:91],v[48:49],v[18:19],v[90:91]" qR qH "v[96:97],v[48:49],v[24:25],v[96:97]" qR qH "v[98:99],v[48:49],v[26:27],v[98:99]" qR qH "v[104:105],v[48:49],v[32:33],v[104:105]" qR \
qH "v[106:107],v[48:49],v[34:35],v[106:107]" qR "v_mul_f32 v6,v51,v51\n" qG "v84,a4\n" qG "v85,a5\n" \
qG "v86,a6\n" qG "v87,a7\n" qG "v92,a12\n" qG "v93,a13\n" \
qG "v94,a14\n" qG "v95,a15\n" qG "v100,a20\n" qG "v101,a21\n" \
qG "v102,a22\n" qG "v103,a23\n" qG "v108,a28\n" qG "v109,a29\n" \
qG "v110,a30\n" qG "v111,a31\n" qF "v[112:115],v2,s[84:87],s56 offen offset:0 sc1\n" qH "v[84:85],v[48:49],v[12:13],v[84:85]" qR \
qH "v[86:87],v[48:49],v[14:15],v[86:87]" qR qI "v[80:81],v[80:81],v[6:7]" qT qI "v[82:83],v[82:83],v[6:7]" qT qH "v[92:93],v[48:49],v[20:21],v[92:93]" qR \
qH "v[94:95],v[48:49],v[22:23],v[94:95]" qR qI "v[88:89],v[88:89],v[6:7]" qT qI "v[90:91],v[90:91],v[6:7]" qT qF "v[116:119],v2,s[84:87],s56 offen offset:1024 sc1\n" \
qH "v[100:101],v[48:49],v[28:29],v[100:101]" qR qH "v[102:103],v[48:49],v[30:31],v[102:103]" qR qI "v[96:97],v[96:97],v[6:7]" qT qI "v[98:99],v[98:99],v[6:7]" qT \
qH "v[108:109],v[48:49],v[36:37],v[108:109]" qR qH "v[110:111],v[48:49],v[38:39],v[110:111]" qR qI "v[104:105],v[104:105],v[6:7]" qT qI "v[106:107],v[106:107],v[6:7]" qT \
qF "v[120:123],v2,s[84:87],s56 offen offset:2048 sc1\n" qI "v[80:81],v[80:81],v[84:85]\n" qI "v[82:83],v[82:83],v[86:87]\n" qI "v[88:89],v[88:89],v[92:93]\n" \
qI "v[90:91],v[90:91],v[94:95]\n" qI "v[96:97],v[96:97],v[100:101]\n" qI "v[98:99],v[98:99],v[102:103]\n" qI "v[104:105],v[104:105],v[108:109]\n" \
qI "v[106:107],v[106:107],v[110:111]\n" qF "v[124:127],v2,s[84:87],s56 offen offset:3072 sc1\n" qJ "v128,v80,v81\n" qJ "v129,v82,v83\n" \
qJ "v130,v88,v89\n" qJ "v131,v90,v91\n" qJ "v132,v96,v97\n" qJ "v133,v98,v99\n" \
qJ "v134,v104,v105\n" qJ "v135,v106,v107\n" qG "v80,a40\n" qG "v81,a41\n" \
qG "v82,a42\n" qG "v83,a43\n" qG "v88,a48\n" qG "v89,a49\n" \
qG "v90,a50\n" qG "v91,a51\n" qG "v96,a56\n" qG "v97,a57\n" \
qG "v98,a58\n" qG "v99,a59\n" qG "v104,a64\n" qG "v105,a65\n" \
qG "v106,a66\n" qG "v107,a67\n" qH "v[80:81],v[52:53],v[8:9],v[80:81]" qR qH "v[82:83],v[52:53],v[10:11],v[82:83]" qR \
qH "v[88:89],v[52:53],v[16:17],v[88:89]" qR qH "v[90:91],v[52:53],v[18:19],v[90:91]" qR qH "v[96:97],v[52:53],v[24:25],v[96:97]" qR qH "v[98:99],v[52:53],v[26:27],v[98:99]" qR \
qH "v[104:105],v[52:53],v[32:33],v[104:105]" qR qH "v[106:107],v[52:53],v[34:35],v[106:107]" qR "v_mul_f32 v7,v55,v55\n" qG "v84,a44\n" \
qG "v85,a45\n" qG "v86,a46\n" qG "v87,a47\n" qG "v92,a52\n" \
qG "v93,a53\n" qG "v94,a54\n" qG "v95,a55\n" qG "v100,a60\n" \
qG "v101,a61\n" qG "v102,a62\n" qG "v103,a63\n" qG "v108,a68\n" \
qG "v109,a69\n" qG "v110,a70\n" qG "v111,a71\n" qH "v[84:85],v[52:53],v[12:13],v[84:85]" qR \
qH "v[86:87],v[52:53],v[14:15],v[86:87]" qR qI "v[80:81],v[80:81],v[6:7]" qS qI "v[82:83],v[82:83],v[6:7]" qS qH "v[92:93],v[52:53],v[20:21],v[92:93]" qR \
qH "v[94:95],v[52:53],v[22:23],v[94:95]" qR qI "v[88:89],v[88:89],v[6:7]" qS qI "v[90:91],v[90:91],v[6:7]" qS qH "v[100:101],v[52:53],v[28:29],v[100:101]" qR \
qH "v[102:103],v[52:53],v[30:31],v[102:103]" qR qI "v[96:97],v[96:97],v[6:7]" qS qI "v[98:99],v[98:99],v[6:7]" qS qH "v[108:109],v[52:53],v[36:37],v[108:109]" qR \
qH "v[110:111],v[52:53],v[38:39],v[110:111]" qR qI "v[104:105],v[104:105],v[6:7]" qS qI "v[106:107],v[106:107],v[6:7]" qS qI "v[80:81],v[80:81],v[84:85]\n" \
qI "v[82:83],v[82:83],v[86:87]\n" qI "v[88:89],v[88:89],v[92:93]\n" qI "v[90:91],v[90:91],v[94:95]\n" qI "v[96:97],v[96:97],v[100:101]\n" \
qI "v[98:99],v[98:99],v[102:103]\n" qI "v[104:105],v[104:105],v[108:109]\n" qI "v[106:107],v[106:107],v[110:111]\n" qJ "v136,v80,v81\n" \
qJ "v137,v82,v83\n" qJ "v138,v88,v89\n" qJ "v139,v90,v91\n" qJ "v140,v96,v97\n" \
qJ "v141,v98,v99\n" qJ "v142,v104,v105\n" qJ "v143,v106,v107\n" qG "v80,a80\n" \
qG "v81,a81\n" qG "v82,a82\n" qG "v83,a83\n" qG "v88,a88\n" \
qG "v89,a89\n" qG "v90,a90\n" qG "v91,a91\n" qG "v96,a96\n" \
qG "v97,a97\n" qG "v98,a98\n" qG "v99,a99\n" qG "v104,a104\n" \
qG "v105,a105\n" qG "v106,a106\n" qG "v107,a107\n" qH "v[80:81],v[56:57],v[8:9],v[80:81]" qR \
qH "v[82:83],v[56:57],v[10:11],v[82:83]" qR qH "v[88:89],v[56:57],v[16:17],v[88:89]" qR qH "v[90:91],v[56:57],v[18:19],v[90:91]" qR qH "v[96:97],v[56:57],v[24:25],v[96:97]" qR \
qH "v[98:99],v[56:57],v[26:27],v[98:99]" qR qH "v[104:105],v[56:57],v[32:33],v[104:105]" qR qH "v[106:107],v[56:57],v[34:35],v[106:107]" qR "v_mul_f32 v6,v59,v59\n" \
qG "v84,a84\n" qG "v85,a85\n" qG "v86,a86\n" qG "v87,a87\n" \
qG "v92,a92\n" qG "v93,a93\n" qG "v94,a94\n" qG "v95,a95\n" \
qG "v100,a100\n" qG "v101,a101\n" qG "v102,a102\n" qG "v103,a103\n" \
qG "v108,a108\n" qG "v109,a109\n" qG "v110,a110\n" qG "v111,a111\n" \
qH "v[84:85],v[56:57],v[12:13],v[84:85]" qR qH "v[86:87],v[56:57],v[14:15],v[86:87]" qR qI "v[80:81],v[80:81],v[6:7]" qT qI "v[82:83],v[82:83],v[6:7]" qT \
qH "v[92:93],v[56:57],v[20:21],v[92:93]" qR qH "v[94:95],v[56:57],v[22:23],v[94:95]" qR qI "v[88:89],v[88:89],v[6:7]" qT qI "v[90:91],v[90:91],v[6:7]" qT \
qH "v[100:101],v[56:57],v[28:29],v[100:101]" qR qH "v[102:103],v[56:57],v[30:31],v[102:103]" qR qI "v[96:97],v[96:97],v[6:7]" qT qI "v[98:99],v[98:99],v[6:7]" qT \
qH "v[108:109],v[56:57],v[36:37],v[108:109]" qR qH "v[110:111],v[56:57],v[38:39],v[110:111]" qR qI "v[104:105],v[104:105],v[6:7]" qT qI "v[106:107],v[106:107],v[6:7]" qT \
qQ "v1,v[112:115] offset:4096\n" qI "v[80:81],v[80:81],v[84:85]\n" qI "v[82:83],v[82:83],v[86:87]\n" qI "v[88:89],v[88:89],v[92:93]\n" \
qI "v[90:91],v[90:91],v[94:95]\n" qI "v[96:97],v[96:97],v[100:101]\n" qI "v[98:99],v[98:99],v[102:103]\n" qI "v[104:105],v[104:105],v[108:109]\n" \
qI "v[106:107],v[106:107],v[110:111]\n" qQ "v1,v[116:119] offset:5120\n" qJ "v144,v80,v81\n" qJ "v145,v82,v83\n" \
qJ "v146,v88,v89\n" qJ "v147,v90,v91\n" qJ "v148,v96,v97\n" qJ "v149,v98,v99\n" \
qJ "v150,v104,v105\n" qJ "v151,v106,v107\n" qQ "v1,v[120:123] offset:6144\n" qK "v128,v130\n" \
qK "v129,v131\n" qQ "v1,v[124:127] offset:7168\n" qK "v132,v134\n" qK "v133,v135\n" \
qK "v136,v138\n" qK "v137,v139\n" qK "v140,v142\n" qK "v141,v143\n" \
qK "v144,v146\n" qK "v145,v147\n" qK "v148,v150\n" qK "v149,v151\n" \
"v_mbcnt_lo_u32_b32 v82,-1,0\n" "v_mbcnt_hi_u32_b32 v82,-1,v82\n" "v_lshlrev_b32 v82,4,v82\n" "v_and_b32 v1,48,v82\n" \
"v_lshrrev_b32 v1,1,v1\n" "v_mov_b32 v3,s75\n" "v_xor_b32 v2,0x9e3779b9,v3\n" qO "vmcnt(0) lgkmcnt(0)\n" \
"s_mov_b64 exec,1\n" "buffer_store_dwordx2 v[2:3],v1,s[84:87],s77 offen sc1\n" "s_mov_b64 exec,-1\n" qG "v80,a120\n" \
qG "v81,a121\n" qG "v82,a122\n" qG "v83,a123\n" qG "v88,a128\n" \
qG "v89,a129\n" qG "v90,a130\n" qG "v91,a131\n" qG "v96,a136\n" \
qG "v97,a137\n" qG "v98,a138\n" qG "v99,a139\n" qG "v104,a144\n" \
qG "v105,a145\n" qG "v106,a146\n" qG "v107,a147\n" qH "v[80:81],v[60:61],v[8:9],v[80:81]" qR \
qH "v[82:83],v[60:61],v[10:11],v[82:83]" qR qH "v[88:89],v[60:61],v[16:17],v[88:89]" qR qH "v[90:91],v[60:61],v[18:19],v[90:91]" qR qH "v[96:97],v[60:61],v[24:25],v[96:97]" qR \
qH "v[98:99],v[60:61],v[26:27],v[98:99]" qR qH "v[104:105],v[60:61],v[32:33],v[104:105]" qR qH "v[106:107],v[60:61],v[34:35],v[106:107]" qR "v_mul_f32 v7,v63,v63\n" \
qG "v84,a124\n" qG "v85,a125\n" qG "v86,a126\n" qG "v87,a127\n" \
qG "v92,a132\n" qG "v93,a133\n" qG "v94,a134\n" qG "v95,a135\n" \
qG "v100,a140\n" qG "v101,a141\n" qG "v102,a142\n" qG "v103,a143\n" \
qG "v108,a148\n" qG "v109,a149\n" qG "v110,a150\n" qG "v111,a151\n" \
qH "v[84:85],v[60:61],v[12:13],v[84:85]" qR qH "v[86:87],v[60:61],v[14:15],v[86:87]" qR qI "v[80:81],v[80:81],v[6:7]" qS qI "v[82:83],v[82:83],v[6:7]" qS \
qH "v[92:93],v[60:61],v[20:21],v[92:93]" qR qH "v[94:95],v[60:61],v[22:23],v[94:95]" qR qI "v[88:89],v[88:89],v[6:7]" qS qI "v[90:91],v[90:91],v[6:7]" qS \
qH "v[100:101],v[60:61],v[28:29],v[100:101]" qR qH "v[102:103],v[60:61],v[30:31],v[102:103]" qR qI "v[96:97],v[96:97],v[6:7]" qS qI "v[98:99],v[98:99],v[6:7]" qS \
qH "v[108:109],v[60:61],v[36:37],v[108:109]" qR qH "v[110:111],v[60:61],v[38:39],v[110:111]" qR qI "v[104:105],v[104:105],v[6:7]" qS qI "v[106:107],v[106:107],v[6:7]" qS \
qI "v[80:81],v[80:81],v[84:85]\n" qI "v[82:83],v[82:83],v[86:87]\n" qI "v[88:89],v[88:89],v[92:93]\n" qI "v[90:91],v[90:91],v[94:95]\n" \
qI "v[96:97],v[96:97],v[100:101]\n" qI "v[98:99],v[98:99],v[102:103]\n" qI "v[104:105],v[104:105],v[108:109]\n" qI "v[106:107],v[106:107],v[110:111]\n" \
qJ "v152,v80,v81\n" qJ "v153,v82,v83\n" qJ "v154,v88,v89\n" qJ "v155,v90,v91\n" \
qJ "v156,v96,v97\n" qJ "v157,v98,v99\n" qJ "v158,v104,v105\n" qJ "v159,v106,v107\n" \
qG "v80,a160\n" qG "v81,a161\n" qG "v82,a162\n" qG "v83,a163\n" \
qG "v88,a168\n" qG "v89,a169\n" qG "v90,a170\n" qG "v91,a171\n" \
qG "v96,a176\n" qG "v97,a177\n" qG "v98,a178\n" qG "v99,a179\n" \
qG "v104,a184\n" qG "v105,a185\n" qG "v106,a186\n" qG "v107,a187\n" \
qH "v[80:81],v[64:65],v[8:9],v[80:81]" qR qH "v[82:83],v[64:65],v[10:11],v[82:83]" qR qH "v[88:89],v[64:65],v[16:17],v[88:89]" qR qH "v[90:91],v[64:65],v[18:19],v[90:91]" qR \
qH "v[96:97],v[64:65],v[24:25],v[96:97]" qR qH "v[98:99],v[64:65],v[26:27],v[98:99]" qR qH "v[104:105],v[64:65],v[32:33],v[104:105]" qR qH "v[106:107],v[64:65],v[34:35],v[106:107]" qR \
"v_mul_f32 v6,v67,v67\n" qG "v84,a164\n" qG "v85,a165\n" qG "v86,a166\n" \
qG "v87,a167\n" qG "v92,a172\n" qG "v93,a173\n" qG "v94,a174\n" \
qG "v95,a175\n" qG "v100,a180\n" qG "v101,a181\n" qG "v102,a182\n" \
qG "v103,a183\n" qG "v108,a188\n" qG "v109,a189\n" qG "v110,a190\n" \
qG "v111,a191\n" qH "v[84:85],v[64:65],v[12:13],v[84:85]" qR qH "v[86:87],v[64:65],v[14:15],v[86:87]" qR qI "v[80:81],v[80:81],v[6:7]" qT \
qI "v[82:83],v[82:83],v[6:7]" qT qH "v[92:93],v[64:65],v[20:21],v[92:93]" qR qH "v[94:95],v[64:65],v[22:23],v[94:95]" qR qI "v[88:89],v[88:89],v[6:7]" qT \
qI "v[90:91],v[90:91],v[6:7]" qT qH "v[100:101],v[64:65],v[28:29],v[100:101]" qR qH "v[102:103],v[64:65],v[30:31],v[102:103]" qR qI "v[96:97],v[96:97],v[6:7]" qT \
qI "v[98:99],v[98:99],v[6:7]" qT qH "v[108:109],v[64:65],v[36:37],v[108:109]" qR qH "v[110:111],v[64:65],v[38:39],v[110:111]" qR qI "v[104:105],v[104:105],v[6:7]" qT \
qI "v[106:107],v[106:107],v[6:7]" qT qI "v[80:81],v[80:81],v[84:85]\n" qI "v[82:83],v[82:83],v[86:87]\n" qI "v[88:89],v[88:89],v[92:93]\n" \
qI "v[90:91],v[90:91],v[94:95]\n" qI "v[96:97],v[96:97],v[100:101]\n" qI "v[98:99],v[98:99],v[102:103]\n" qI "v[104:105],v[104:105],v[108:109]\n" \
qI "v[106:107],v[106:107],v[110:111]\n" qJ "v160,v80,v81\n" qJ "v161,v82,v83\n" qJ "v162,v88,v89\n" \
qJ "v163,v90,v91\n" qJ "v164,v96,v97\n" qJ "v165,v98,v99\n" qJ "v166,v104,v105\n" \
qJ "v167,v106,v107\n" qG "v80,a200\n" qG "v81,a201\n" qG "v82,a202\n" \
qG "v83,a203\n" qG "v88,a208\n" qG "v89,a209\n" qG "v90,a210\n" \
qG "v91,a211\n" qG "v96,a216\n" qG "v97,a217\n" qG "v98,a218\n" \
qG "v99,a219\n" qG "v104,a224\n" qG "v105,a225\n" qG "v106,a226\n" \
qG "v107,a227\n" qH "v[80:81],v[68:69],v[8:9],v[80:81]" qR qH "v[82:83],v[68:69],v[10:11],v[82:83]" qR qH "v[88:89],v[68:69],v[16:17],v[88:89]" qR \
qH "v[90:91],v[68:69],v[18:19],v[90:91]" qR qH "v[96:97],v[68:69],v[24:25],v[96:97]" qR qH "v[98:99],v[68:69],v[26:27],v[98:99]" qR qH "v[104:105],v[68:69],v[32:33],v[104:105]" qR \
qH "v[106:107],v[68:69],v[34:35],v[106:107]" qR "v_mul_f32 v7,v71,v71\n" qG "v84,a204\n" qG "v85,a205\n" \
qG "v86,a206\n" qG "v87,a207\n" qG "v92,a212\n" qG "v93,a213\n" \
qG "v94,a214\n" qG "v95,a215\n" qG "v100,a220\n" qG "v101,a221\n" \
qG "v102,a222\n" qG "v103,a223\n" qG "v108,a228\n" qG "v109,a229\n" \
qG "v110,a230\n" qG "v111,a231\n" qH "v[84:85],v[68:69],v[12:13],v[84:85]" qR qH "v[86:87],v[68:69],v[14:15],v[86:87]" qR \
qI "v[80:81],v[80:81],v[6:7]" qS qI "v[82:83],v[82:83],v[6:7]" qS qH "v[92:93],v[68:69],v[20:21],v[92:93]" qR qH "v[94:95],v[68:69],v[22:23],v[94:95]" qR \
qI "v[88:89],v[88:89],v[6:7]" qS qI "v[90:91],v[90:91],v[6:7]" qS qH "v[100:101],v[68:69],v[28:29],v[100:101]" qR qH "v[102:103],v[68:69],v[30:31],v[102:103]" qR \
qI "v[96:97],v[96:97],v[6:7]" qS qI "v[98:99],v[98:99],v[6:7]" qS qH "v[108:109],v[68:69],v[36:37],v[108:109]" qR qH "v[110:111],v[68:69],v[38:39],v[110:111]" qR \
qI "v[104:105],v[104:105],v[6:7]" qS qI "v[106:107],v[106:107],v[6:7]" qS qI "v[80:81],v[80:81],v[84:85]\n" qI "v[82:83],v[82:83],v[86:87]\n" \
qI "v[88:89],v[88:89],v[92:93]\n" qI "v[90:91],v[90:91],v[94:95]\n" qI "v[96:97],v[96:97],v[100:101]\n" qI "v[98:99],v[98:99],v[102:103]\n" \
qI "v[104:105],v[104:105],v[108:109]\n" qI "v[106:107],v[106:107],v[110:111]\n" qJ "v168,v80,v81\n" qJ "v169,v82,v83\n" \
qJ "v170,v88,v89\n" qJ "v171,v90,v91\n" qJ "v172,v96,v97\n" qJ "v173,v98,v99\n" \
qJ "v174,v104,v105\n" qJ "v175,v106,v107\n" qK "v152,v154\n" qK "v153,v155\n" \
qK "v156,v158\n" qK "v157,v159\n" qK "v160,v162\n" qK "v161,v163\n" \
qK "v164,v166\n" qK "v165,v167\n" qK "v168,v170\n" qK "v169,v171\n" \
qK "v172,v174\n" qK "v173,v175\n" "s_bitcmp1_b32 s59,9\n" "s_cbranch_scc1 EPP2_%=\n" \
"s_xor_b32 s94,s75,0x9e3779b9\n" "s_mov_b32 s95,s75\n" "s_mov_b32 s58,0x400000\n" "EPQ2_%=:\n" \
"buffer_load_dwordx2 v[2:3],v1,s[84:87],s78 offen sc1\n" qO "vmcnt(0)\n" "v_cmp_ne_u64 vcc,s[94:95],v[2:3]\n" "s_nop 3\n" \
"s_cmp_eq_u64 vcc,0\n" "s_cbranch_scc1 EPP2_%=\n" "s_sleep 1\n" "s_sub_u32 s58,s58,1\n" \
"s_cmp_eq_u32 s58,0\n" "s_cbranch_scc0 EPQ2_%=\n" "EPP2_%=:\n" "v_mbcnt_lo_u32_b32 v82,-1,0\n" \
"v_mbcnt_hi_u32_b32 v82,-1,v82\n" "v_lshlrev_b32 v82,4,v82\n" "v_mov_b32 v47,v82\n" "v_add_u32 v1,2048,v82\n" \
"v_lshrrev_b32 v2,6,v82\n" "v_lshlrev_b32 v2,6,v2\n" "v_bfe_u32 v3,v82,4,2\n" "v_lshrrev_b32 v83,8,v82\n" \
"v_xor_b32 v83,v83,v3\n" "v_lshl_add_u32 v3,v3,4,v2\n" "v_lshl_add_u32 v2,v83,4,v2\n" "v_add_u32 v2,18432,v2\n" \
"v_add_u32 v3,51200,v3\n" "s_bitcmp1_b32 s59,9\n" "s_cbranch_scc1 EPZ2_%=\n" "s_sub_u32 s57,s76,1048576\n" \
qE "a[0:3],v1,s[84:87],s57 offen sc1\n" qN "s57,s57,4096\n" qE "a[4:7],v1,s[84:87],s57 offen sc1\n" qN "s57,s57,4096\n" \
qE "a[8:11],v1,s[84:87],s57 offen sc1\n" qN "s57,s57,4096\n" qE "a[12:15],v1,s[84:87],s57 offen sc1\n" qN "s57,s57,4096\n" \
qE "a[16:19],v1,s[84:87],s57 offen sc1\n" qN "s57,s57,4096\n" qE "a[20:23],v1,s[84:87],s57 offen sc1\n" qN "s57,s57,4096\n" \
qE "a[24:27],v1,s[84:87],s57 offen sc1\n" qN "s57,s57,4096\n" qE "a[28:31],v1,s[84:87],s57 offen sc1\n" "s_branch EPL2_%=\n" \
"EPZ2_%=:\n" "v_accvgpr_write_b32 a0,0\n" "v_accvgpr_write_b32 a1,0\n" "v_accvgpr_write_b32 a2,0\n" \
"v_accvgpr_write_b32 a3,0\n" "v_accvgpr_write_b32 a4,0\n" "v_accvgpr_write_b32 a5,0\n" "v_accvgpr_write_b32 a6,0\n" \
"v_accvgpr_write_b32 a7,0\n" "v_accvgpr_write_b32 a8,0\n" "v_accvgpr_write_b32 a9,0\n" "v_accvgpr_write_b32 a10,0\n" \
"v_accvgpr_write_b32 a11,0\n" "v_accvgpr_write_b32 a12,0\n" "v_accvgpr_write_b32 a13,0\n" "v_accvgpr_write_b32 a14,0\n" \
"v_accvgpr_write_b32 a15,0\n" "v_accvgpr_write_b32 a16,0\n" "v_accvgpr_write_b32 a17,0\n" "v_accvgpr_write_b32 a18,0\n" \
"v_accvgpr_write_b32 a19,0\n" "v_accvgpr_write_b32 a20,0\n" "v_accvgpr_write_b32 a21,0\n" "v_accvgpr_write_b32 a22,0\n" \
"v_accvgpr_write_b32 a23,0\n" "v_accvgpr_write_b32 a24,0\n" "v_accvgpr_write_b32 a25,0\n" "v_accvgpr_write_b32 a26,0\n" \
"v_accvgpr_write_b32 a27,0\n" "v_accvgpr_write_b32 a28,0\n" "v_accvgpr_write_b32 a29,0\n" "v_accvgpr_write_b32 a30,0\n" \
"v_accvgpr_write_b32 a31,0\n" "EPL2_%=:\n" qG "v80,a240\n" qG "v81,a241\n" \
qG "v82,a242\n" qG "v83,a243\n" qG "v88,a248\n" qG "v89,a249\n" \
qG "v90,a250\n" qG "v91,a251\n" qH "v[80:81],v[72:73],v[8:9],v[80:81]" qR qH "v[82:83],v[72:73],v[10:11],v[82:83]" qR \
qH "v[88:89],v[72:73],v[16:17],v[88:89]" qR qH "v[90:91],v[72:73],v[18:19],v[90:91]" qR qH "v[96:97],v[72:73],v[24:25],v[192:193]" qR qH "v[98:99],v[72:73],v[26:27],v[194:195]" qR \
qH "v[104:105],v[72:73],v[32:33],v[200:201]" qR qH "v[106:107],v[72:73],v[34:35],v[202:203]" qR "v_mul_f32 v6,v75,v75\n" qG "v84,a244\n" \
qG "v85,a245\n" qG "v86,a246\n" qG "v87,a247\n" qG "v92,a252\n" \
qG "v93,a253\n" qG "v94,a254\n" qG "v95,a255\n" qH "v[84:85],v[72:73],v[12:13],v[84:85]" qR \
qH "v[86:87],v[72:73],v[14:15],v[86:87]" qR qI "v[80:81],v[80:81],v[6:7]" qT qI "v[82:83],v[82:83],v[6:7]" qT qH "v[92:93],v[72:73],v[20:21],v[92:93]" qR \
qH "v[94:95],v[72:73],v[22:23],v[94:95]" qR qI "v[88:89],v[88:89],v[6:7]" qT qI "v[90:91],v[90:91],v[6:7]" qT qH "v[100:101],v[72:73],v[28:29],v[196:197]" qR \
qH "v[102:103],v[72:73],v[30:31],v[198:199]" qR qI "v[96:97],v[96:97],v[6:7]" qT qI "v[98:99],v[98:99],v[6:7]" qT qH "v[108:109],v[72:73],v[36:37],v[204:205]" qR \
qH "v[110:111],v[72:73],v[38:39],v[206:207]" qR qI "v[104:105],v[104:105],v[6:7]" qT qI "v[106:107],v[106:107],v[6:7]" qT qI "v[80:81],v[80:81],v[84:85]\n" \
qI "v[82:83],v[82:83],v[86:87]\n" qI "v[88:89],v[88:89],v[92:93]\n" qI "v[90:91],v[90:91],v[94:95]\n" qI "v[96:97],v[96:97],v[100:101]\n" \
qI "v[98:99],v[98:99],v[102:103]\n" qI "v[104:105],v[104:105],v[108:109]\n" qI "v[106:107],v[106:107],v[110:111]\n" qJ "v176,v80,v81\n" \
qJ "v177,v82,v83\n" qJ "v178,v88,v89\n" qJ "v179,v90,v91\n" qJ "v180,v96,v97\n" \
qJ "v181,v98,v99\n" qJ "v182,v104,v105\n" qJ "v183,v106,v107\n" qH "v[80:81],v[76:77],v[8:9],v[216:217]" qR \
qH "v[82:83],v[76:77],v[10:11],v[218:219]" qR qH "v[88:89],v[76:77],v[16:17],v[224:225]" qR qH "v[90:91],v[76:77],v[18:19],v[226:227]" qR qH "v[96:97],v[76:77],v[24:25],v[232:233]" qR \
qH "v[98:99],v[76:77],v[26:27],v[234:235]" qR qH "v[104:105],v[76:77],v[32:33],v[240:241]" qR qH "v[106:107],v[76:77],v[34:35],v[242:243]" qR "v_mul_f32 v7,v79,v79\n" \
qH "v[84:85],v[76:77],v[12:13],v[220:221]" qR qH "v[86:87],v[76:77],v[14:15],v[222:223]" qR qI "v[80:81],v[80:81],v[6:7]" qS qI "v[82:83],v[82:83],v[6:7]" qS \
qH "v[92:93],v[76:77],v[20:21],v[228:229]" qR qH "v[94:95],v[76:77],v[22:23],v[230:231]" qR qI "v[88:89],v[88:89],v[6:7]" qS qI "v[90:91],v[90:91],v[6:7]" qS \
qH "v[100:101],v[76:77],v[28:29],v[236:237]" qR qH "v[102:103],v[76:77],v[30:31],v[238:239]" qR qI "v[96:97],v[96:97],v[6:7]" qS qI "v[98:99],v[98:99],v[6:7]" qS \
qH "v[108:109],v[76:77],v[36:37],v[244:245]" qR qH "v[110:111],v[76:77],v[38:39],v[246:247]" qR qI "v[104:105],v[104:105],v[6:7]" qS qI "v[106:107],v[106:107],v[6:7]" qS \
qI "v[80:81],v[80:81],v[84:85]\n" qI "v[82:83],v[82:83],v[86:87]\n" qI "v[88:89],v[88:89],v[92:93]\n" qI "v[90:91],v[90:91],v[94:95]\n" \
qI "v[96:97],v[96:97],v[100:101]\n" qI "v[98:99],v[98:99],v[102:103]\n" qI "v[104:105],v[104:105],v[108:109]\n" qI "v[106:107],v[106:107],v[110:111]\n" \
qJ "v184,v80,v81\n" qJ "v185,v82,v83\n" qJ "v186,v88,v89\n" qJ "v187,v90,v91\n" \
qJ "v188,v96,v97\n" qJ "v189,v98,v99\n" qJ "v190,v104,v105\n" qJ "v191,v106,v107\n" \
qK "v176,v178\n" qK "v177,v179\n" qK "v180,v182\n" qK "v181,v183\n" \
qK "v184,v186\n" qK "v185,v187\n" qK "v188,v190\n" qK "v189,v191\n" \
"v_and_b32 v83,0x1f0,v47\n" "v_lshrrev_b32 v86,9,v47\n" "v_bfe_u32 v87,v47,6,2\n" "v_xor_b32 v87,v87,v86\n" \
"v_lshlrev_b32 v87,4,v87\n" "v_lshl_add_u32 v40,v83,2,v87\n" "v_add_u32 v40,8192,v40\n" "v_xor_b32 v41,32,v40\n" \
"v_bfe_u32 v87,v47,6,2\n" "v_lshl_add_u32 v87,v86,2,v87\n" "v_lshlrev_b32 v87,6,v87\n" "v_bfe_u32 v42,v47,8,1\n" \
"v_lshl_add_u32 v87,v42,5,v87\n" "v_bfe_u32 v42,v47,4,2\n" "v_lshl_add_u32 v42,v42,3,v87\n" "v_add_u32 v42,57344,v42\n" \
"v_lshl_add_u32 v43,v86,15,v83\n" "v_add_u32 v43,s81,v43\n" "v_lshrrev_b32 v44,4,v83\n" "v_lshlrev_b32 v87,2,v86\n" \
"v_sub_u32 v44,v44,v87\n" "v_and_b32 v83,0xf0,v47\n" "v_bfe_u32 v87,v47,8,1\n" "v_lshl_or_b32 v87,v87,1,v86\n" \
"v_lshl_add_u32 v45,v87,15,v83\n" "v_add_u32 v45,s79,v45\n" "v_add_u32 v46,0x20000,v45\n" "v_mov_b32 v47,v46\n" \
qO "vmcnt(0)\n" qQ "v2,a[0:3] offset:0\n" qQ "v2,a[4:7] offset:4096\n" qQ "v2,a[8:11] offset:8192\n" \
qQ "v2,a[12:15] offset:12288\n" qQ "v3,a[16:19] offset:0\n" qQ "v3,a[20:23] offset:4096\n" qQ "v3,a[24:27] offset:8192\n" \
qQ "v3,a[28:31] offset:12288\n" qO "lgkmcnt(0)\n" "s_barrier\n" "s_branch G1END_%=\n" \
"G1W3_%=:\n" qC "v[48:49],v32 offset:4096\n" qC "v[50:51],v32 offset:4352\n" qC "v[52:53],v32 offset:14336\n" \
qC "v[54:55],v32 offset:14592\n" qC "v[56:57],v33 offset:4096\n" qC "v[58:59],v33 offset:4352\n" qC "v[60:61],v33 offset:14336\n" \
qC "v[62:63],v33 offset:14592\n" qC "v[64:65],v34 offset:8192\n" qC "v[66:67],v34 offset:8448\n" qC "v[68:69],v34 offset:9216\n" \
qC "v[70:71],v34 offset:9472\n" qC "v[72:73],v32 offset:6144\n" qC "v[74:75],v32 offset:6400\n" qC "v[76:77],v33 offset:6144\n" \
qC "v[78:79],v33 offset:6400\n" qC "v[80:81],v32 offset:18432\n" qC "v[82:83],v32 offset:18688\n" qC "v[84:85],v33 offset:18432\n" \
qC "v[86:87],v33 offset:18688\n" qD "v[128:131],v38 offset:0\n" qD "v[132:135],v38 offset:2048\n" qD "v[136:139],v38 offset:4096\n" \
qD "v[140:143],v38 offset:6144\n" qD "v[144:147],v38 offset:8192\n" qD "v[148:151],v38 offset:10240\n" qD "v[152:155],v38 offset:12288\n" \
qD "v[156:159],v38 offset:14336\n" "s_mov_b32 s63,7\n" qO "lgkmcnt(0)\n" qA "a[0:3],v[48:51],v[128:131],0\n" \
qO "vmcnt(14)\n" qA "a[4:7],v[52:55],v[128:131],0\n" "s_barrier\n" qA "a[8:11],v[56:59],v[128:131],0\n" \
qC "v[88:89],v32 offset:24576\n" qN "m0,s62,61440\n" qA "a[12:15],v[60:63],v[128:131],0\n" qE "v41,s[44:47],s56" qU \
qA "a[16:19],v[64:67],v[128:131],0\n" qC "v[90:91],v32 offset:24832\n" qA "a[20:23],v[68:71],v[128:131],0\n" qN "m0,s62,63424\n" \
qA "a[24:27],v[72:75],v[128:131],0\n" qC "v[92:93],v32 offset:34816\n" qE "v41,s[44:47],s56 offen offset:64 lds\n" qA "a[28:31],v[76:79],v[128:131],0\n" \
qA "a[32:35],v[80:83],v[128:131],0\n" qC "v[94:95],v32 offset:35072\n" qN "m0,s62,65408\n" qA "a[36:39],v[84:87],v[128:131],0\n" \
qE "v41,s[44:47],s56 offen offset:128 lds\n" qA "v[8:11],v[68:71],v[28:31],0\n" qC "v[96:97],v33 offset:24576\n" qA "a[40:43],v[48:51],v[132:135],0\n" \
qN "m0,s62,67584\n" qA "a[44:47],v[52:55],v[132:135],0\n" qC "v[98:99],v33 offset:24832\n" qE "v42,s[48:51],s57" qU \
qA "a[48:51],v[56:59],v[132:135],0\n" qA "a[52:55],v[60:63],v[132:135],0\n" qC "v[100:101],v33 offset:34816\n" qN "m0,s62,69632\n" \
qA "a[56:59],v[64:67],v[132:135],0\n" qE "v43,s[52:55],s58" qU qA "a[60:63],v[68:71],v[132:135],0\n" qC "v[102:103],v33 offset:35072\n" \
qN "s56,s56,229376\n" qN "s57,s57,196608\n" qN "s58,s58,s60\n" qA "a[64:67],v[72:75],v[132:135],0\n" \
"s_min_u32 s56,s56,7110656\n" "s_min_u32 s57,s57,6094848\n" "s_min_u32 s58,s58,s72\n" qN "m0,s62,0\n" \
qA "a[68:71],v[76:79],v[132:135],0\n" qC "v[104:105],v34 offset:28672\n" qE "v41,s[44:47],s56" qU qA "a[72:75],v[80:83],v[132:135],0\n" \
qA "a[76:79],v[84:87],v[132:135],0\n" qC "v[106:107],v34 offset:28928\n" qN "m0,s62,1984\n" qA "a[80:83],v[48:51],v[136:139],0\n" \
qE "v41,s[44:47],s56 offen offset:64 lds\n" qA "a[84:87],v[52:55],v[136:139],0\n" qC "v[108:109],v34 offset:29696\n" qA "a[88:91],v[56:59],v[136:139],0\n" \
qN "m0,s62,3968\n" qA "a[92:95],v[60:63],v[136:139],0\n" qC "v[110:111],v34 offset:29952\n" qE "v41,s[44:47],s56 offen offset:128 lds\n" \
qA "a[96:99],v[64:67],v[136:139],0\n" qA "a[100:103],v[68:71],v[136:139],0\n" qC "v[112:113],v32 offset:26624\n" qN "m0,s62,6144\n" \
qA "v[12:15],v[72:75],v[28:31],0\n" qE "v42,s[48:51],s57" qU qA "a[104:107],v[72:75],v[136:139],0\n" qC "v[114:115],v32 offset:26880\n" \
qA "a[108:111],v[76:79],v[136:139],0\n" qN "m0,s62,8192\n" qA "a[112:115],v[80:83],v[136:139],0\n" qC "v[116:117],v33 offset:26624\n" \
qE "v43,s[52:55],s58" qU qA "a[116:119],v[84:87],v[136:139],0\n" qA "a[120:123],v[48:51],v[140:143],0\n" qC "v[118:119],v33 offset:26880\n" \
qN "s56,s56,229376\n" qA "a[124:127],v[52:55],v[140:143],0\n" qN "s57,s57,196608\n" qA "a[128:131],v[56:59],v[140:143],0\n" \
qC "v[120:121],v32 offset:38912\n" qN "s58,s58,s60\n" qA "a[132:135],v[60:63],v[140:143],0\n" "s_min_u32 s56,s56,7110656\n" \
qA "a[136:139],v[64:67],v[140:143],0\n" qC "v[122:123],v32 offset:39168\n" "s_min_u32 s57,s57,6094848\n" qA "a[140:143],v[68:71],v[140:143],0\n" \
"s_min_u32 s58,s58,s72\n" qA "a[144:147],v[72:75],v[140:143],0\n" qC "v[124:125],v33 offset:38912\n" qA "a[148:151],v[76:79],v[140:143],0\n" \
qA "a[152:155],v[80:83],v[140:143],0\n" qC "v[126:127],v33 offset:39168\n" qA "a[156:159],v[84:87],v[140:143],0\n" qA "a[160:163],v[48:51],v[144:147],0\n" \
qD "v[160:163],v39 offset:0\n" qA "a[164:167],v[52:55],v[144:147],0\n" qA "v[16:19],v[76:79],v[28:31],0\n" qD "v[164:167],v39 offset:2048\n" \
qA "a[168:171],v[56:59],v[144:147],0\n" qA "a[172:175],v[60:63],v[144:147],0\n" qD "v[168:171],v39 offset:4096\n" qA "a[176:179],v[64:67],v[144:147],0\n" \
qA "a[180:183],v[68:71],v[144:147],0\n" qD "v[172:175],v39 offset:6144\n" qA "a[184:187],v[72:75],v[144:147],0\n" qA "a[188:191],v[76:79],v[144:147],0\n" \
qD "v[176:179],v39 offset:8192\n" qA "a[192:195],v[80:83],v[144:147],0\n" qA "a[196:199],v[84:87],v[144:147],0\n" qD "v[180:183],v39 offset:10240\n" \
qA "a[200:203],v[48:51],v[148:151],0\n" qA "a[204:207],v[52:55],v[148:151],0\n" qD "v[184:187],v39 offset:12288\n" qA "a[208:211],v[56:59],v[148:151],0\n" \
qA "a[212:215],v[60:63],v[148:151],0\n" qD "v[188:191],v39 offset:14336\n" qA "a[216:219],v[64:67],v[148:151],0\n" qA "a[220:223],v[68:71],v[148:151],0\n" \
qA "a[224:227],v[72:75],v[148:151],0\n" qA "a[228:231],v[76:79],v[148:151],0\n" qA "v[20:23],v[80:83],v[28:31],0\n" qA "a[232:235],v[80:83],v[148:151],0\n" \
qA "a[236:239],v[84:87],v[148:151],0\n" qA "a[240:243],v[48:51],v[152:155],0\n" qA "a[244:247],v[52:55],v[152:155],0\n" qA "a[248:251],v[56:59],v[152:155],0\n" \
qA "a[252:255],v[60:63],v[152:155],0\n" qA "v[192:195],v[64:67],v[152:155],0\n" qA "v[196:199],v[68:71],v[152:155],0\n" qA "v[200:203],v[72:75],v[152:155],0\n" \
qA "v[204:207],v[76:79],v[152:155],0\n" qA "v[208:211],v[80:83],v[152:155],0\n" qA "v[212:215],v[84:87],v[152:155],0\n" qA "v[216:219],v[48:51],v[156:159],0\n" \
qA "v[220:223],v[52:55],v[156:159],0\n" qA "v[224:227],v[56:59],v[156:159],0\n" qA "v[228:231],v[60:63],v[156:159],0\n" qA "v[24:27],v[84:87],v[28:31],0\n" \
qA "v[232:235],v[64:67],v[156:159],0\n" qA "v[236:239],v[68:71],v[156:159],0\n" qA "v[240:243],v[72:75],v[156:159],0\n" qA "v[244:247],v[76:79],v[156:159],0\n" \
qA "v[248:251],v[80:83],v[156:159],0\n" qA "v[252:255],v[84:87],v[156:159],0\n" "G1L3_%=:\n" qO "lgkmcnt(0)\n" \
qA "a[0:3],v[88:91],v[160:163],a[0:3]\n" qO "vmcnt(11)\n" qA "a[4:7],v[92:95],v[160:163],a[4:7]\n" "s_barrier\n" \
qA "a[8:11],v[96:99],v[160:163],a[8:11]\n" qC "v[48:49],v35 offset:4096\n" qA "a[12:15],v[100:103],v[160:163],a[12:15]\n" qN "m0,s61,0\n" \
qA "a[16:19],v[104:107],v[160:163],a[16:19]\n" qC "v[50:51],v35 offset:4352\n" qA "a[20:23],v[108:111],v[160:163],a[20:23]\n" qE "v40,s[40:43],0" qU \
qA "a[24:27],v[112:115],v[160:163],a[24:27]\n" qC "v[52:53],v35 offset:14336\n" qA "a[28:31],v[116:119],v[160:163],a[28:31]\n" qN "m0,s61,1024\n" \
qA "a[32:35],v[120:123],v[160:163],a[32:35]\n" qC "v[54:55],v35 offset:14592\n" qA "a[36:39],v[124:127],v[160:163],a[36:39]\n" qE "v40,s[40:43],s65" qU \
qA "v[8:11],v[108:111],v[28:31],v[8:11]\n" qC "v[56:57],v36 offset:4096\n" qA "a[40:43],v[88:91],v[164:167],a[40:43]\n" qN "m0,s61,2048\n" \
qA "a[44:47],v[92:95],v[164:167],a[44:47]\n" qC "v[58:59],v36 offset:4352\n" qA "a[48:51],v[96:99],v[164:167],a[48:51]\n" qE "v40,s[40:43],s66" qU \
qA "a[52:55],v[100:103],v[164:167],a[52:55]\n" qC "v[60:61],v36 offset:14336\n" qA "a[56:59],v[104:107],v[164:167],a[56:59]\n" qN "m0,s61,3072\n" \
qA "a[60:63],v[108:111],v[164:167],a[60:63]\n" qC "v[62:63],v36 offset:14592\n" qA "a[64:67],v[112:115],v[164:167],a[64:67]\n" qE "v40,s[40:43],s67" qU \
qA "a[68:71],v[116:119],v[164:167],a[68:71]\n" qC "v[64:65],v37 offset:8192\n" qA "a[72:75],v[120:123],v[164:167],a[72:75]\n" qN "m0,s61,4096\n" \
qA "a[76:79],v[124:127],v[164:167],a[76:79]\n" qC "v[66:67],v37 offset:8448\n" qA "a[80:83],v[88:91],v[168:171],a[80:83]\n" qE "v40,s[40:43],s68" qU \
qA "a[84:87],v[92:95],v[168:171],a[84:87]\n" qC "v[68:69],v37 offset:9216\n" qA "a[88:91],v[96:99],v[168:171],a[88:91]\n" qN "m0,s61,5120\n" \
qA "a[92:95],v[100:103],v[168:171],a[92:95]\n" qC "v[70:71],v37 offset:9472\n" qA "a[96:99],v[104:107],v[168:171],a[96:99]\n" qE "v40,s[40:43],s69" qU \
qA "a[100:103],v[108:111],v[168:171],a[100:103]\n" qC "v[72:73],v35 offset:6144\n" qA "v[12:15],v[112:115],v[28:31],v[12:15]\n" qN "m0,s61,6144\n" \
qA "a[104:107],v[112:115],v[168:171],a[104:107]\n" qC "v[74:75],v35 offset:6400\n" qA "a[108:111],v[116:119],v[168:171],a[108:111]\n" qE "v40,s[40:43],s70" qU \
qA "a[112:115],v[120:123],v[168:171],a[112:115]\n" qC "v[76:77],v36 offset:6144\n" qA "a[116:119],v[124:127],v[168:171],a[116:119]\n" qN "m0,s61,7168\n" \
qA "a[120:123],v[88:91],v[172:175],a[120:123]\n" qC "v[78:79],v36 offset:6400\n" qA "a[124:127],v[92:95],v[172:175],a[124:127]\n" qE "v40,s[40:43],s71" qU \
qA "a[128:131],v[96:99],v[172:175],a[128:131]\n" qC "v[80:81],v35 offset:18432\n" qA "a[132:135],v[100:103],v[172:175],a[132:135]\n" qN "m0,s62,20480\n" \
qA "a[136:139],v[104:107],v[172:175],a[136:139]\n" qC "v[82:83],v35 offset:18688\n" qA "a[140:143],v[108:111],v[172:175],a[140:143]\n" qE "v41,s[44:47],s56" qU \
qA "a[144:147],v[112:115],v[172:175],a[144:147]\n" qC "v[84:85],v36 offset:18432\n" qA "a[148:151],v[116:119],v[172:175],a[148:151]\n" qN "m0,s62,22464\n" \
qA "a[152:155],v[120:123],v[172:175],a[152:155]\n" qC "v[86:87],v36 offset:18688\n" qA "a[156:159],v[124:127],v[172:175],a[156:159]\n" qE "v41,s[44:47],s56 offen offset:64 lds\n" \
qA "a[160:163],v[88:91],v[176:179],a[160:163]\n" qD "v[128:131],v38 offset:32768\n" qA "a[164:167],v[92:95],v[176:179],a[164:167]\n" qN "m0,s62,24448\n" \
qA "v[16:19],v[116:119],v[28:31],v[16:19]\n" qD "v[132:135],v38 offset:34816\n" qA "a[168:171],v[96:99],v[176:179],a[168:171]\n" qE "v41,s[44:47],s56 offen offset:128 lds\n" \
qA "a[172:175],v[100:103],v[176:179],a[172:175]\n" qD "v[136:139],v38 offset:36864\n" qA "a[176:179],v[104:107],v[176:179],a[176:179]\n" qN "m0,s62,26624\n" \
qA "a[180:183],v[108:111],v[176:179],a[180:183]\n" qD "v[140:143],v38 offset:38912\n" qA "a[184:187],v[112:115],v[176:179],a[184:187]\n" qE "v42,s[48:51],s57" qU \
qA "a[188:191],v[116:119],v[176:179],a[188:191]\n" qD "v[144:147],v38 offset:40960\n" qA "a[192:195],v[120:123],v[176:179],a[192:195]\n" qN "m0,s62,28672\n" \
qA "a[196:199],v[124:127],v[176:179],a[196:199]\n" qD "v[148:151],v38 offset:43008\n" qA "a[200:203],v[88:91],v[180:183],a[200:203]\n" qE "v43,s[52:55],s58" qU \
qA "a[204:207],v[92:95],v[180:183],a[204:207]\n" qD "v[152:155],v38 offset:45056\n" qA "a[208:211],v[96:99],v[180:183],a[208:211]\n" qA "a[212:215],v[100:103],v[180:183],a[212:215]\n" \
qD "v[156:159],v38 offset:47104\n" qA "a[216:219],v[104:107],v[180:183],a[216:219]\n" qA "a[220:223],v[108:111],v[180:183],a[220:223]\n" qA "a[224:227],v[112:115],v[180:183],a[224:227]\n" \
qA "a[228:231],v[116:119],v[180:183],a[228:231]\n" qN "s40,s40,128\n" qA "v[20:23],v[120:123],v[28:31],v[20:23]\n" "s_addc_u32 s41,s41,0\n" \
qA "a[232:235],v[120:123],v[180:183],a[232:235]\n" qN "s56,s56,229376\n" qA "a[236:239],v[124:127],v[180:183],a[236:239]\n" qN "s57,s57,196608\n" \
qA "a[240:243],v[88:91],v[184:187],a[240:243]\n" qN "s58,s58,s60\n" qA "a[244:247],v[92:95],v[184:187],a[244:247]\n" "s_min_u32 s56,s56,7110656\n" \
qA "a[248:251],v[96:99],v[184:187],a[248:251]\n" "s_min_u32 s57,s57,6094848\n" qA "a[252:255],v[100:103],v[184:187],a[252:255]\n" "s_min_u32 s58,s58,s72\n" \
qA "v[192:195],v[104:107],v[184:187],v[192:195]\n" qA "v[196:199],v[108:111],v[184:187],v[196:199]\n" qA "v[200:203],v[112:115],v[184:187],v[200:203]\n" qA "v[204:207],v[116:119],v[184:187],v[204:207]\n" \
qA "v[208:211],v[120:123],v[184:187],v[208:211]\n" qA "v[212:215],v[124:127],v[184:187],v[212:215]\n" qA "v[216:219],v[88:91],v[188:191],v[216:219]\n" qA "v[220:223],v[92:95],v[188:191],v[220:223]\n" \
qA "v[224:227],v[96:99],v[188:191],v[224:227]\n" qA "v[228:231],v[100:103],v[188:191],v[228:231]\n" qA "v[24:27],v[124:127],v[28:31],v[24:27]\n" qA "v[232:235],v[104:107],v[188:191],v[232:235]\n" \
qA "v[236:239],v[108:111],v[188:191],v[236:239]\n" qA "v[240:243],v[112:115],v[188:191],v[240:243]\n" qA "v[244:247],v[116:119],v[188:191],v[244:247]\n" qA "v[248:251],v[120:123],v[188:191],v[248:251]\n" \
qA "v[252:255],v[124:127],v[188:191],v[252:255]\n" qO "lgkmcnt(0)\n" qA "a[0:3],v[48:51],v[128:131],a[0:3]\n" qO "vmcnt(18)\n" \
qA "a[4:7],v[52:55],v[128:131],a[4:7]\n" "s_barrier\n" qA "a[8:11],v[56:59],v[128:131],a[8:11]\n" qC "v[88:89],v35 offset:24576\n" \
qA "a[12:15],v[60:63],v[128:131],a[12:15]\n" qN "m0,s62,40960\n" qA "a[16:19],v[64:67],v[128:131],a[16:19]\n" qC "v[90:91],v35 offset:24832\n" \
qA "a[20:23],v[68:71],v[128:131],a[20:23]\n" qE "v41,s[44:47],s56" qU qA "a[24:27],v[72:75],v[128:131],a[24:27]\n" qC "v[92:93],v35 offset:34816\n" \
qA "a[28:31],v[76:79],v[128:131],a[28:31]\n" qN "m0,s62,42944\n" qA "a[32:35],v[80:83],v[128:131],a[32:35]\n" qC "v[94:95],v35 offset:35072\n" \
qA "a[36:39],v[84:87],v[128:131],a[36:39]\n" qE "v41,s[44:47],s56 offen offset:64 lds\n" qA "v[8:11],v[68:71],v[28:31],v[8:11]\n" qC "v[96:97],v36 offset:24576\n" \
qA "a[40:43],v[48:51],v[132:135],a[40:43]\n" qN "m0,s62,44928\n" qA "a[44:47],v[52:55],v[132:135],a[44:47]\n" qC "v[98:99],v36 offset:24832\n" \
qA "a[48:51],v[56:59],v[132:135],a[48:51]\n" qE "v41,s[44:47],s56 offen offset:128 lds\n" qA "a[52:55],v[60:63],v[132:135],a[52:55]\n" qC "v[100:101],v36 offset:34816\n" \
qA "a[56:59],v[64:67],v[132:135],a[56:59]\n" qN "m0,s62,47104\n" qA "a[60:63],v[68:71],v[132:135],a[60:63]\n" qC "v[102:103],v36 offset:35072\n" \
qA "a[64:67],v[72:75],v[132:135],a[64:67]\n" qE "v42,s[48:51],s57" qU qA "a[68:71],v[76:79],v[132:135],a[68:71]\n" qC "v[104:105],v37 offset:28672\n" \
qA "a[72:75],v[80:83],v[132:135],a[72:75]\n" qN "m0,s62,49152\n" qA "a[76:79],v[84:87],v[132:135],a[76:79]\n" qC "v[106:107],v37 offset:28928\n" \
qA "a[80:83],v[48:51],v[136:139],a[80:83]\n" qE "v43,s[52:55],s58" qU qA "a[84:87],v[52:55],v[136:139],a[84:87]\n" qC "v[108:109],v37 offset:29696\n" \
qA "a[88:91],v[56:59],v[136:139],a[88:91]\n" qA "a[92:95],v[60:63],v[136:139],a[92:95]\n" qC "v[110:111],v37 offset:29952\n" qA "a[96:99],v[64:67],v[136:139],a[96:99]\n" \
qA "a[100:103],v[68:71],v[136:139],a[100:103]\n" qC "v[112:113],v35 offset:26624\n" qA "v[12:15],v[72:75],v[28:31],v[12:15]\n" qA "a[104:107],v[72:75],v[136:139],a[104:107]\n" \
qC "v[114:115],v35 offset:26880\n" qA "a[108:111],v[76:79],v[136:139],a[108:111]\n" qA "a[112:115],v[80:83],v[136:139],a[112:115]\n" qC "v[116:117],v36 offset:26624\n" \
qA "a[116:119],v[84:87],v[136:139],a[116:119]\n" qA "a[120:123],v[48:51],v[140:143],a[120:123]\n" qC "v[118:119],v36 offset:26880\n" qA "a[124:127],v[52:55],v[140:143],a[124:127]\n" \
qA "a[128:131],v[56:59],v[140:143],a[128:131]\n" qC "v[120:121],v35 offset:38912\n" qA "a[132:135],v[60:63],v[140:143],a[132:135]\n" qA "a[136:139],v[64:67],v[140:143],a[136:139]\n" \
qC "v[122:123],v35 offset:39168\n" qA "a[140:143],v[68:71],v[140:143],a[140:143]\n" qA "a[144:147],v[72:75],v[140:143],a[144:147]\n" qC "v[124:125],v36 offset:38912\n" \
qA "a[148:151],v[76:79],v[140:143],a[148:151]\n" qA "a[152:155],v[80:83],v[140:143],a[152:155]\n" qC "v[126:127],v36 offset:39168\n" qA "a[156:159],v[84:87],v[140:143],a[156:159]\n" \
qA "a[160:163],v[48:51],v[144:147],a[160:163]\n" qD "v[160:163],v39 offset:32768\n" qA "a[164:167],v[52:55],v[144:147],a[164:167]\n" qA "v[16:19],v[76:79],v[28:31],v[16:19]\n" \
qD "v[164:167],v39 offset:34816\n" qA "a[168:171],v[56:59],v[144:147],a[168:171]\n" qA "a[172:175],v[60:63],v[144:147],a[172:175]\n" qD "v[168:171],v39 offset:36864\n" \
qA "a[176:179],v[64:67],v[144:147],a[176:179]\n" qA "a[180:183],v[68:71],v[144:147],a[180:183]\n" qD "v[172:175],v39 offset:38912\n" qA "a[184:187],v[72:75],v[144:147],a[184:187]\n" \
qA "a[188:191],v[76:79],v[144:147],a[188:191]\n" qD "v[176:179],v39 offset:40960\n" qA "a[192:195],v[80:83],v[144:147],a[192:195]\n" qA "a[196:199],v[84:87],v[144:147],a[196:199]\n" \
qD "v[180:183],v39 offset:43008\n" qA "a[200:203],v[48:51],v[148:151],a[200:203]\n" qA "a[204:207],v[52:55],v[148:151],a[204:207]\n" qD "v[184:187],v39 offset:45056\n" \
qA "a[208:211],v[56:59],v[148:151],a[208:211]\n" qA "a[212:215],v[60:63],v[148:151],a[212:215]\n" qD "v[188:191],v39 offset:47104\n" qA "a[216:219],v[64:67],v[148:151],a[216:219]\n" \
qA "a[220:223],v[68:71],v[148:151],a[220:223]\n" qA "a[224:227],v[72:75],v[148:151],a[224:227]\n" qA "a[228:231],v[76:79],v[148:151],a[228:231]\n" qN "s56,s56,229376\n" \
qA "v[20:23],v[80:83],v[28:31],v[20:23]\n" qN "s57,s57,196608\n" qA "a[232:235],v[80:83],v[148:151],a[232:235]\n" qN "s58,s58,s60\n" \
qA "a[236:239],v[84:87],v[148:151],a[236:239]\n" "s_min_u32 s56,s56,7110656\n" qA "a[240:243],v[48:51],v[152:155],a[240:243]\n" "s_min_u32 s57,s57,6094848\n" \
qA "a[244:247],v[52:55],v[152:155],a[244:247]\n" "s_min_u32 s58,s58,s72\n" qA "a[248:251],v[56:59],v[152:155],a[248:251]\n" qA "a[252:255],v[60:63],v[152:155],a[252:255]\n" \
qA "v[192:195],v[64:67],v[152:155],v[192:195]\n" qA "v[196:199],v[68:71],v[152:155],v[196:199]\n" qA "v[200:203],v[72:75],v[152:155],v[200:203]\n" qA "v[204:207],v[76:79],v[152:155],v[204:207]\n" \
qA "v[208:211],v[80:83],v[152:155],v[208:211]\n" qA "v[212:215],v[84:87],v[152:155],v[212:215]\n" qA "v[216:219],v[48:51],v[156:159],v[216:219]\n" qA "v[220:223],v[52:55],v[156:159],v[220:223]\n" \
qA "v[224:227],v[56:59],v[156:159],v[224:227]\n" qA "v[228:231],v[60:63],v[156:159],v[228:231]\n" qA "v[24:27],v[84:87],v[28:31],v[24:27]\n" qA "v[232:235],v[64:67],v[156:159],v[232:235]\n" \
qA "v[236:239],v[68:71],v[156:159],v[236:239]\n" qA "v[240:243],v[72:75],v[156:159],v[240:243]\n" qA "v[244:247],v[76:79],v[156:159],v[244:247]\n" qA "v[248:251],v[80:83],v[156:159],v[248:251]\n" \
qA "v[252:255],v[84:87],v[156:159],v[252:255]\n" qO "lgkmcnt(0)\n" qA "a[0:3],v[88:91],v[160:163],a[0:3]\n" qO "vmcnt(10)\n" \
qA "a[4:7],v[92:95],v[160:163],a[4:7]\n" "s_barrier\n" qA "a[8:11],v[96:99],v[160:163],a[8:11]\n" qC "v[48:49],v32 offset:4096\n" \
qA "a[12:15],v[100:103],v[160:163],a[12:15]\n" qN "m0,s61,32768\n" qA "a[16:19],v[104:107],v[160:163],a[16:19]\n" qC "v[50:51],v32 offset:4352\n" \
qA "a[20:23],v[108:111],v[160:163],a[20:23]\n" qE "v40,s[40:43],0" qU qA "a[24:27],v[112:115],v[160:163],a[24:27]\n" qC "v[52:53],v32 offset:14336\n" \
qA "a[28:31],v[116:119],v[160:163],a[28:31]\n" qN "m0,s61,33792\n" qA "a[32:35],v[120:123],v[160:163],a[32:35]\n" qC "v[54:55],v32 offset:14592\n" \
qA "a[36:39],v[124:127],v[160:163],a[36:39]\n" qE "v40,s[40:43],s65" qU qA "v[8:11],v[108:111],v[28:31],v[8:11]\n" qC "v[56:57],v33 offset:4096\n" \
qA "a[40:43],v[88:91],v[164:167],a[40:43]\n" qN "m0,s61,34816\n" qA "a[44:47],v[92:95],v[164:167],a[44:47]\n" qC "v[58:59],v33 offset:4352\n" \
qA "a[48:51],v[96:99],v[164:167],a[48:51]\n" qE "v40,s[40:43],s66" qU qA "a[52:55],v[100:103],v[164:167],a[52:55]\n" qC "v[60:61],v33 offset:14336\n" \
qA "a[56:59],v[104:107],v[164:167],a[56:59]\n" qN "m0,s61,35840\n" qA "a[60:63],v[108:111],v[164:167],a[60:63]\n" qC "v[62:63],v33 offset:14592\n" \
qA "a[64:67],v[112:115],v[164:167],a[64:67]\n" qE "v40,s[40:43],s67" qU qA "a[68:71],v[116:119],v[164:167],a[68:71]\n" qC "v[64:65],v34 offset:8192\n" \
qA "a[72:75],v[120:123],v[164:167],a[72:75]\n" qN "m0,s61,36864\n" qA "a[76:79],v[124:127],v[164:167],a[76:79]\n" qC "v[66:67],v34 offset:8448\n" \
qA "a[80:83],v[88:91],v[168:171],a[80:83]\n" qE "v40,s[40:43],s68" qU qA "a[84:87],v[92:95],v[168:171],a[84:87]\n" qC "v[68:69],v34 offset:9216\n" \
qA "a[88:91],v[96:99],v[168:171],a[88:91]\n" qN "m0,s61,37888\n" qA "a[92:95],v[100:103],v[168:171],a[92:95]\n" qC "v[70:71],v34 offset:9472\n" \
qA "a[96:99],v[104:107],v[168:171],a[96:99]\n" qE "v40,s[40:43],s69" qU qA "a[100:103],v[108:111],v[168:171],a[100:103]\n" qC "v[72:73],v32 offset:6144\n" \
qA "v[12:15],v[112:115],v[28:31],v[12:15]\n" qN "m0,s61,38912\n" qA "a[104:107],v[112:115],v[168:171],a[104:107]\n" qC "v[74:75],v32 offset:6400\n" \
qA "a[108:111],v[116:119],v[168:171],a[108:111]\n" qE "v40,s[40:43],s70" qU qA "a[112:115],v[120:123],v[168:171],a[112:115]\n" qC "v[76:77],v33 offset:6144\n" \
qA "a[116:119],v[124:127],v[168:171],a[116:119]\n" qN "m0,s61,39936\n" qA "a[120:123],v[88:91],v[172:175],a[120:123]\n" qC "v[78:79],v33 offset:6400\n" \
qA "a[124:127],v[92:95],v[172:175],a[124:127]\n" qE "v40,s[40:43],s71" qU qA "a[128:131],v[96:99],v[172:175],a[128:131]\n" qC "v[80:81],v32 offset:18432\n" \
qA "a[132:135],v[100:103],v[172:175],a[132:135]\n" qN "m0,s62,61440\n" qA "a[136:139],v[104:107],v[172:175],a[136:139]\n" qC "v[82:83],v32 offset:18688\n" \
qA "a[140:143],v[108:111],v[172:175],a[140:143]\n" qE "v41,s[44:47],s56" qU qA "a[144:147],v[112:115],v[172:175],a[144:147]\n" qC "v[84:85],v33 offset:18432\n" \
qA "a[148:151],v[116:119],v[172:175],a[148:151]\n" qN "m0,s62,63424\n" qA "a[152:155],v[120:123],v[172:175],a[152:155]\n" qC "v[86:87],v33 offset:18688\n" \
qA "a[156:159],v[124:127],v[172:175],a[156:159]\n" qE "v41,s[44:47],s56 offen offset:64 lds\n" qA "a[160:163],v[88:91],v[176:179],a[160:163]\n" qD "v[128:131],v38 offset:0\n" \
qA "a[164:167],v[92:95],v[176:179],a[164:167]\n" qN "m0,s62,65408\n" qA "v[16:19],v[116:119],v[28:31],v[16:19]\n" qD "v[132:135],v38 offset:2048\n" \
qA "a[168:171],v[96:99],v[176:179],a[168:171]\n" qE "v41,s[44:47],s56 offen offset:128 lds\n" qA "a[172:175],v[100:103],v[176:179],a[172:175]\n" qD "v[136:139],v38 offset:4096\n" \
qA "a[176:179],v[104:107],v[176:179],a[176:179]\n" qN "m0,s62,67584\n" qA "a[180:183],v[108:111],v[176:179],a[180:183]\n" qD "v[140:143],v38 offset:6144\n" \
qA "a[184:187],v[112:115],v[176:179],a[184:187]\n" qE "v42,s[48:51],s57" qU qA "a[188:191],v[116:119],v[176:179],a[188:191]\n" qD "v[144:147],v38 offset:8192\n" \
qA "a[192:195],v[120:123],v[176:179],a[192:195]\n" qN "m0,s62,69632\n" qA "a[196:199],v[124:127],v[176:179],a[196:199]\n" qD "v[148:151],v38 offset:10240\n" \
qA "a[200:203],v[88:91],v[180:183],a[200:203]\n" qE "v43,s[52:55],s58" qU qA "a[204:207],v[92:95],v[180:183],a[204:207]\n" qD "v[152:155],v38 offset:12288\n" \
qA "a[208:211],v[96:99],v[180:183],a[208:211]\n" qA "a[212:215],v[100:103],v[180:183],a[212:215]\n" qD "v[156:159],v38 offset:14336\n" qA "a[216:219],v[104:107],v[180:183],a[216:219]\n" \
qA "a[220:223],v[108:111],v[180:183],a[220:223]\n" qA "a[224:227],v[112:115],v[180:183],a[224:227]\n" qA "a[228:231],v[116:119],v[180:183],a[228:231]\n" qN "s40,s40,128\n" \
qA "v[20:23],v[120:123],v[28:31],v[20:23]\n" "s_addc_u32 s41,s41,0\n" qA "a[232:235],v[120:123],v[180:183],a[232:235]\n" qN "s56,s56,229376\n" \
qA "a[236:239],v[124:127],v[180:183],a[236:239]\n" qN "s57,s57,196608\n" qA "a[240:243],v[88:91],v[184:187],a[240:243]\n" qN "s58,s58,s60\n" \
qA "a[244:247],v[92:95],v[184:187],a[244:247]\n" "s_min_u32 s56,s56,7110656\n" qA "a[248:251],v[96:99],v[184:187],a[248:251]\n" "s_min_u32 s57,s57,6094848\n" \
qA "a[252:255],v[100:103],v[184:187],a[252:255]\n" "s_min_u32 s58,s58,s72\n" qA "v[192:195],v[104:107],v[184:187],v[192:195]\n" qA "v[196:199],v[108:111],v[184:187],v[196:199]\n" \
qA "v[200:203],v[112:115],v[184:187],v[200:203]\n" qA "v[204:207],v[116:119],v[184:187],v[204:207]\n" qA "v[208:211],v[120:123],v[184:187],v[208:211]\n" qA "v[212:215],v[124:127],v[184:187],v[212:215]\n" \
qA "v[216:219],v[88:91],v[188:191],v[216:219]\n" qA "v[220:223],v[92:95],v[188:191],v[220:223]\n" qA "v[224:227],v[96:99],v[188:191],v[224:227]\n" qA "v[228:231],v[100:103],v[188:191],v[228:231]\n" \
qA "v[24:27],v[124:127],v[28:31],v[24:27]\n" qA "v[232:235],v[104:107],v[188:191],v[232:235]\n" qA "v[236:239],v[108:111],v[188:191],v[236:239]\n" qA "v[240:243],v[112:115],v[188:191],v[240:243]\n" \
qA "v[244:247],v[116:119],v[188:191],v[244:247]\n" qA "v[248:251],v[120:123],v[188:191],v[248:251]\n" qA "v[252:255],v[124:127],v[188:191],v[252:255]\n" qO "lgkmcnt(0)\n" \
qA "a[0:3],v[48:51],v[128:131],a[0:3]\n" qO "vmcnt(18)\n" qA "a[4:7],v[52:55],v[128:131],a[4:7]\n" "s_barrier\n" \
qA "a[8:11],v[56:59],v[128:131],a[8:11]\n" qC "v[88:89],v32 offset:24576\n" qA "a[12:15],v[60:63],v[128:131],a[12:15]\n" qN "m0,s62,0\n" \
qA "a[16:19],v[64:67],v[128:131],a[16:19]\n" qC "v[90:91],v32 offset:24832\n" qA "a[20:23],v[68:71],v[128:131],a[20:23]\n" qE "v41,s[44:47],s56" qU \
qA "a[24:27],v[72:75],v[128:131],a[24:27]\n" qC "v[92:93],v32 offset:34816\n" qA "a[28:31],v[76:79],v[128:131],a[28:31]\n" qN "m0,s62,1984\n" \
qA "a[32:35],v[80:83],v[128:131],a[32:35]\n" qC "v[94:95],v32 offset:35072\n" qA "a[36:39],v[84:87],v[128:131],a[36:39]\n" qE "v41,s[44:47],s56 offen offset:64 lds\n" \
qA "v[8:11],v[68:71],v[28:31],v[8:11]\n" qC "v[96:97],v33 offset:24576\n" qA "a[40:43],v[48:51],v[132:135],a[40:43]\n" qN "m0,s62,3968\n" \
qA "a[44:47],v[52:55],v[132:135],a[44:47]\n" qC "v[98:99],v33 offset:24832\n" qA "a[48:51],v[56:59],v[132:135],a[48:51]\n" qE "v41,s[44:47],s56 offen offset:128 lds\n" \
qA "a[52:55],v[60:63],v[132:135],a[52:55]\n" qC "v[100:101],v33 offset:34816\n" qA "a[56:59],v[64:67],v[132:135],a[56:59]\n" qN "m0,s62,6144\n" \
qA "a[60:63],v[68:71],v[132:135],a[60:63]\n" qC "v[102:103],v33 offset:35072\n" qA "a[64:67],v[72:75],v[132:135],a[64:67]\n" qE "v42,s[48:51],s57" qU \
qA "a[68:71],v[76:79],v[132:135],a[68:71]\n" qC "v[104:105],v34 offset:28672\n" qA "a[72:75],v[80:83],v[132:135],a[72:75]\n" qN "m0,s62,8192\n" \
qA "a[76:79],v[84:87],v[132:135],a[76:79]\n" qC "v[106:107],v34 offset:28928\n" qA "a[80:83],v[48:51],v[136:139],a[80:83]\n" qE "v43,s[52:55],s58" qU \
qA "a[84:87],v[52:55],v[136:139],a[84:87]\n" qC "v[108:109],v34 offset:29696\n" qA "a[88:91],v[56:59],v[136:139],a[88:91]\n" qA "a[92:95],v[60:63],v[136:139],a[92:95]\n" \
qC "v[110:111],v34 offset:29952\n" qA "a[96:99],v[64:67],v[136:139],a[96:99]\n" qA "a[100:103],v[68:71],v[136:139],a[100:103]\n" qC "v[112:113],v32 offset:26624\n" \
qA "v[12:15],v[72:75],v[28:31],v[12:15]\n" qA "a[104:107],v[72:75],v[136:139],a[104:107]\n" qC "v[114:115],v32 offset:26880\n" qA "a[108:111],v[76:79],v[136:139],a[108:111]\n" \
qA "a[112:115],v[80:83],v[136:139],a[112:115]\n" qC "v[116:117],v33 offset:26624\n" qA "a[116:119],v[84:87],v[136:139],a[116:119]\n" qA "a[120:123],v[48:51],v[140:143],a[120:123]\n" \
qC "v[118:119],v33 offset:26880\n" qA "a[124:127],v[52:55],v[140:143],a[124:127]\n" qA "a[128:131],v[56:59],v[140:143],a[128:131]\n" qC "v[120:121],v32 offset:38912\n" \
qA "a[132:135],v[60:63],v[140:143],a[132:135]\n" qA "a[136:139],v[64:67],v[140:143],a[136:139]\n" qC "v[122:123],v32 offset:39168\n" qA "a[140:143],v[68:71],v[140:143],a[140:143]\n" \
qA "a[144:147],v[72:75],v[140:143],a[144:147]\n" qC "v[124:125],v33 offset:38912\n" qA "a[148:151],v[76:79],v[140:143],a[148:151]\n" qA "a[152:155],v[80:83],v[140:143],a[152:155]\n" \
qC "v[126:127],v33 offset:39168\n" qA "a[156:159],v[84:87],v[140:143],a[156:159]\n" qA "a[160:163],v[48:51],v[144:147],a[160:163]\n" qD "v[160:163],v39 offset:0\n" \
qA "a[164:167],v[52:55],v[144:147],a[164:167]\n" qA "v[16:19],v[76:79],v[28:31],v[16:19]\n" qD "v[164:167],v39 offset:2048\n" qA "a[168:171],v[56:59],v[144:147],a[168:171]\n" \
qA "a[172:175],v[60:63],v[144:147],a[172:175]\n" qD "v[168:171],v39 offset:4096\n" qA "a[176:179],v[64:67],v[144:147],a[176:179]\n" qA "a[180:183],v[68:71],v[144:147],a[180:183]\n" \
qD "v[172:175],v39 offset:6144\n" qA "a[184:187],v[72:75],v[144:147],a[184:187]\n" qA "a[188:191],v[76:79],v[144:147],a[188:191]\n" qD "v[176:179],v39 offset:8192\n" \
qA "a[192:195],v[80:83],v[144:147],a[192:195]\n" qA "a[196:199],v[84:87],v[144:147],a[196:199]\n" qD "v[180:183],v39 offset:10240\n" qA "a[200:203],v[48:51],v[148:151],a[200:203]\n" \
qA "a[204:207],v[52:55],v[148:151],a[204:207]\n" qD "v[184:187],v39 offset:12288\n" qA "a[208:211],v[56:59],v[148:151],a[208:211]\n" qA "a[212:215],v[60:63],v[148:151],a[212:215]\n" \
qD "v[188:191],v39 offset:14336\n" qA "a[216:219],v[64:67],v[148:151],a[216:219]\n" qA "a[220:223],v[68:71],v[148:151],a[220:223]\n" qA "a[224:227],v[72:75],v[148:151],a[224:227]\n" \
qA "a[228:231],v[76:79],v[148:151],a[228:231]\n" qN "s56,s56,229376\n" qA "v[20:23],v[80:83],v[28:31],v[20:23]\n" qN "s57,s57,196608\n" \
qA "a[232:235],v[80:83],v[148:151],a[232:235]\n" qN "s58,s58,s60\n" qA "a[236:239],v[84:87],v[148:151],a[236:239]\n" "s_min_u32 s56,s56,7110656\n" \
qA "a[240:243],v[48:51],v[152:155],a[240:243]\n" "s_min_u32 s57,s57,6094848\n" qA "a[244:247],v[52:55],v[152:155],a[244:247]\n" "s_min_u32 s58,s58,s72\n" \
qA "a[248:251],v[56:59],v[152:155],a[248:251]\n" qA "a[252:255],v[60:63],v[152:155],a[252:255]\n" qA "v[192:195],v[64:67],v[152:155],v[192:195]\n" qA "v[196:199],v[68:71],v[152:155],v[196:199]\n" \
qA "v[200:203],v[72:75],v[152:155],v[200:203]\n" "s_mov_b32 m0,s64\n" qA "v[204:207],v[76:79],v[152:155],v[204:207]\n" qA "v[208:211],v[80:83],v[152:155],v[208:211]\n" \
qE "v46,s[84:87],0" qV qA "v[212:215],v[84:87],v[152:155],v[212:215]\n" qA "v[216:219],v[48:51],v[156:159],v[216:219]\n" qA "v[220:223],v[52:55],v[156:159],v[220:223]\n" \
qA "v[224:227],v[56:59],v[156:159],v[224:227]\n" qA "v[228:231],v[60:63],v[156:159],v[228:231]\n" qA "v[24:27],v[84:87],v[28:31],v[24:27]\n" qA "v[232:235],v[64:67],v[156:159],v[232:235]\n" \
qA "v[236:239],v[68:71],v[156:159],v[236:239]\n" qA "v[240:243],v[72:75],v[156:159],v[240:243]\n" qA "v[244:247],v[76:79],v[156:159],v[244:247]\n" qA "v[248:251],v[80:83],v[156:159],v[248:251]\n" \
"s_sub_u32 s63,s63,1\n" "s_cmp_lg_u32 s63,0\n" qA "v[252:255],v[84:87],v[156:159],v[252:255]\n" "s_cbranch_scc1 G1L3_%=\n" \
"G1TAIL3_%=:\n" qO "lgkmcnt(0)\n" qA "a[0:3],v[88:91],v[160:163],a[0:3]\n" qO "vmcnt(11)\n" \
qA "a[4:7],v[92:95],v[160:163],a[4:7]\n" "s_barrier\n" qA "a[8:11],v[96:99],v[160:163],a[8:11]\n" qC "v[48:49],v35 offset:4096\n" \
qA "a[12:15],v[100:103],v[160:163],a[12:15]\n" "v_mov_b32 v2,-1\n" qA "a[16:19],v[104:107],v[160:163],a[16:19]\n" qC "v[50:51],v35 offset:4352\n" \
"v_mov_b32 v3,-1\n" qA "a[20:23],v[108:111],v[160:163],a[20:23]\n" "v_mov_b32 v4,-1\n" qA "a[24:27],v[112:115],v[160:163],a[24:27]\n" \
qC "v[52:53],v35 offset:14336\n" "v_mov_b32 v5,-1\n" qA "a[28:31],v[116:119],v[160:163],a[28:31]\n" qA "a[32:35],v[120:123],v[160:163],a[32:35]\n" \
qC "v[54:55],v35 offset:14592\n" qA "a[36:39],v[124:127],v[160:163],a[36:39]\n" qF "v[2:5],v47,s[84:87],s90 offen offset:0 sc1\n" qA "v[8:11],v[108:111],v[28:31],v[8:11]\n" \
qC "v[56:57],v36 offset:4096\n" qA "a[40:43],v[88:91],v[164:167],a[40:43]\n" qA "a[44:47],v[92:95],v[164:167],a[44:47]\n" qC "v[58:59],v36 offset:4352\n" \
qA "a[48:51],v[96:99],v[164:167],a[48:51]\n" qA "a[52:55],v[100:103],v[164:167],a[52:55]\n" qC "v[60:61],v36 offset:14336\n" qA "a[56:59],v[104:107],v[164:167],a[56:59]\n" \
qA "a[60:63],v[108:111],v[164:167],a[60:63]\n" qC "v[62:63],v36 offset:14592\n" qA "a[64:67],v[112:115],v[164:167],a[64:67]\n" qA "a[68:71],v[116:119],v[164:167],a[68:71]\n" \
qC "v[64:65],v37 offset:8192\n" qA "a[72:75],v[120:123],v[164:167],a[72:75]\n" qA "a[76:79],v[124:127],v[164:167],a[76:79]\n" qC "v[66:67],v37 offset:8448\n" \
qA "a[80:83],v[88:91],v[168:171],a[80:83]\n" qF "v[2:5],v47,s[84:87],s90 offen offset:1024 sc1\n" qA "a[84:87],v[92:95],v[168:171],a[84:87]\n" qC "v[68:69],v37 offset:9216\n" \
qA "a[88:91],v[96:99],v[168:171],a[88:91]\n" qA "a[92:95],v[100:103],v[168:171],a[92:95]\n" qC "v[70:71],v37 offset:9472\n" qA "a[96:99],v[104:107],v[168:171],a[96:99]\n" \
qA "a[100:103],v[108:111],v[168:171],a[100:103]\n" qC "v[72:73],v35 offset:6144\n" qA "v[12:15],v[112:115],v[28:31],v[12:15]\n" qA "a[104:107],v[112:115],v[168:171],a[104:107]\n" \
qC "v[74:75],v35 offset:6400\n" qA "a[108:111],v[116:119],v[168:171],a[108:111]\n" qA "a[112:115],v[120:123],v[168:171],a[112:115]\n" qC "v[76:77],v36 offset:6144\n" \
qA "a[116:119],v[124:127],v[168:171],a[116:119]\n" qA "a[120:123],v[88:91],v[172:175],a[120:123]\n" qC "v[78:79],v36 offset:6400\n" qA "a[124:127],v[92:95],v[172:175],a[124:127]\n" \
qF "v[2:5],v47,s[84:87],s90 offen offset:2048 sc1\n" qA "a[128:131],v[96:99],v[172:175],a[128:131]\n" qC "v[80:81],v35 offset:18432\n" qA "a[132:135],v[100:103],v[172:175],a[132:135]\n" \
qA "a[136:139],v[104:107],v[172:175],a[136:139]\n" qC "v[82:83],v35 offset:18688\n" qA "a[140:143],v[108:111],v[172:175],a[140:143]\n" qA "a[144:147],v[112:115],v[172:175],a[144:147]\n" \
qC "v[84:85],v36 offset:18432\n" qA "a[148:151],v[116:119],v[172:175],a[148:151]\n" qA "a[152:155],v[120:123],v[172:175],a[152:155]\n" qC "v[86:87],v36 offset:18688\n" \
qA "a[156:159],v[124:127],v[172:175],a[156:159]\n" qA "a[160:163],v[88:91],v[176:179],a[160:163]\n" qD "v[128:131],v38 offset:32768\n" qA "a[164:167],v[92:95],v[176:179],a[164:167]\n" \
qA "v[16:19],v[116:119],v[28:31],v[16:19]\n" qD "v[132:135],v38 offset:34816\n" qA "a[168:171],v[96:99],v[176:179],a[168:171]\n" qF "v[2:5],v47,s[84:87],s90 offen offset:3072 sc1\n" \
qA "a[172:175],v[100:103],v[176:179],a[172:175]\n" qD "v[136:139],v38 offset:36864\n" qA "a[176:179],v[104:107],v[176:179],a[176:179]\n" qA "a[180:183],v[108:111],v[176:179],a[180:183]\n" \
qD "v[140:143],v38 offset:38912\n" qA "a[184:187],v[112:115],v[176:179],a[184:187]\n" qA "a[188:191],v[116:119],v[176:179],a[188:191]\n" qD "v[144:147],v38 offset:40960\n" \
qA "a[192:195],v[120:123],v[176:179],a[192:195]\n" qA "a[196:199],v[124:127],v[176:179],a[196:199]\n" qD "v[148:151],v38 offset:43008\n" qA "a[200:203],v[88:91],v[180:183],a[200:203]\n" \
qA "a[204:207],v[92:95],v[180:183],a[204:207]\n" qD "v[152:155],v38 offset:45056\n" qA "a[208:211],v[96:99],v[180:183],a[208:211]\n" qA "a[212:215],v[100:103],v[180:183],a[212:215]\n" \
qD "v[156:159],v38 offset:47104\n" qA "a[216:219],v[104:107],v[180:183],a[216:219]\n" qA "a[220:223],v[108:111],v[180:183],a[220:223]\n" "s_mov_b32 m0,s73\n" \
qA "a[224:227],v[112:115],v[180:183],a[224:227]\n" qA "a[228:231],v[116:119],v[180:183],a[228:231]\n" "global_load_lds_dwordx4 v47,s[100:101] offset:0\n" qA "v[20:23],v[120:123],v[28:31],v[20:23]\n" \
qA "a[232:235],v[120:123],v[180:183],a[232:235]\n" "global_load_lds_dwordx4 v47,s[100:101] offset:1024\n" qA "a[236:239],v[124:127],v[180:183],a[236:239]\n" qA "a[240:243],v[88:91],v[184:187],a[240:243]\n" \
"global_load_lds_dwordx4 v47,s[100:101] offset:2048\n" qA "a[244:247],v[92:95],v[184:187],a[244:247]\n" qA "a[248:251],v[96:99],v[184:187],a[248:251]\n" "global_load_lds_dwordx4 v47,s[100:101] offset:3072\n" \
qA "a[252:255],v[100:103],v[184:187],a[252:255]\n" qA "v[192:195],v[104:107],v[184:187],v[192:195]\n" "global_load_lds_dwordx4 v44,s[100:101] offset:0\n" qA "v[196:199],v[108:111],v[184:187],v[196:199]\n" \
qA "v[200:203],v[112:115],v[184:187],v[200:203]\n" "global_load_lds_dwordx4 v44,s[100:101] offset:1024\n" qA "v[204:207],v[116:119],v[184:187],v[204:207]\n" qA "v[208:211],v[120:123],v[184:187],v[208:211]\n" \
"global_load_lds_dwordx4 v44,s[100:101] offset:2048\n" qA "v[212:215],v[124:127],v[184:187],v[212:215]\n" qA "v[216:219],v[88:91],v[188:191],v[216:219]\n" "global_load_lds_dwordx4 v47,s[82:83] offset:0\n" \
qA "v[220:223],v[92:95],v[188:191],v[220:223]\n" qA "v[224:227],v[96:99],v[188:191],v[224:227]\n" "global_load_lds_dwordx4 v47,s[82:83] offset:1024\n" qA "v[228:231],v[100:103],v[188:191],v[228:231]\n" \
qA "v[24:27],v[124:127],v[28:31],v[24:27]\n" qA "v[232:235],v[104:107],v[188:191],v[232:235]\n" qA "v[236:239],v[108:111],v[188:191],v[236:239]\n" qA "v[240:243],v[112:115],v[188:191],v[240:243]\n" \
qA "v[244:247],v[116:119],v[188:191],v[244:247]\n" qA "v[248:251],v[120:123],v[188:191],v[248:251]\n" qA "v[252:255],v[124:127],v[188:191],v[252:255]\n" qO "lgkmcnt(0)\n" \
qA "a[0:3],v[48:51],v[128:131],a[0:3]\n" qO "vmcnt(13)\n" qA "a[4:7],v[52:55],v[128:131],a[4:7]\n" "s_barrier\n" \
qA "a[8:11],v[56:59],v[128:131],a[8:11]\n" qC "v[88:89],v35 offset:24576\n" qA "a[12:15],v[60:63],v[128:131],a[12:15]\n" qA "a[16:19],v[64:67],v[128:131],a[16:19]\n" \
qC "v[90:91],v35 offset:24832\n" qA "a[20:23],v[68:71],v[128:131],a[20:23]\n" qA "a[24:27],v[72:75],v[128:131],a[24:27]\n" qC "v[92:93],v35 offset:34816\n" \
qA "a[28:31],v[76:79],v[128:131],a[28:31]\n" qA "a[32:35],v[80:83],v[128:131],a[32:35]\n" qC "v[94:95],v35 offset:35072\n" qA "a[36:39],v[84:87],v[128:131],a[36:39]\n" \
qA "v[8:11],v[68:71],v[28:31],v[8:11]\n" qC "v[96:97],v36 offset:24576\n" qA "a[40:43],v[48:51],v[132:135],a[40:43]\n" qA "a[44:47],v[52:55],v[132:135],a[44:47]\n" \
qC "v[98:99],v36 offset:24832\n" qA "a[48:51],v[56:59],v[132:135],a[48:51]\n" qA "a[52:55],v[60:63],v[132:135],a[52:55]\n" qC "v[100:101],v36 offset:34816\n" \
qA "a[56:59],v[64:67],v[132:135],a[56:59]\n" qA "a[60:63],v[68:71],v[132:135],a[60:63]\n" qC "v[102:103],v36 offset:35072\n" qA "a[64:67],v[72:75],v[132:135],a[64:67]\n" \
qA "a[68:71],v[76:79],v[132:135],a[68:71]\n" qC "v[104:105],v37 offset:28672\n" qA "a[72:75],v[80:83],v[132:135],a[72:75]\n" qA "a[76:79],v[84:87],v[132:135],a[76:79]\n" \
qC "v[106:107],v37 offset:28928\n" qA "a[80:83],v[48:51],v[136:139],a[80:83]\n" qA "a[84:87],v[52:55],v[136:139],a[84:87]\n" qC "v[108:109],v37 offset:29696\n" \
qA "a[88:91],v[56:59],v[136:139],a[88:91]\n" qA "a[92:95],v[60:63],v[136:139],a[92:95]\n" qC "v[110:111],v37 offset:29952\n" qA "a[96:99],v[64:67],v[136:139],a[96:99]\n" \
qA "a[100:103],v[68:71],v[136:139],a[100:103]\n" qC "v[112:113],v35 offset:26624\n" qA "v[12:15],v[72:75],v[28:31],v[12:15]\n" qA "a[104:107],v[72:75],v[136:139],a[104:107]\n" \
qC "v[114:115],v35 offset:26880\n" qA "a[108:111],v[76:79],v[136:139],a[108:111]\n" qA "a[112:115],v[80:83],v[136:139],a[112:115]\n" qC "v[116:117],v36 offset:26624\n" \
qA "a[116:119],v[84:87],v[136:139],a[116:119]\n" qA "a[120:123],v[48:51],v[140:143],a[120:123]\n" qC "v[118:119],v36 offset:26880\n" qA "a[124:127],v[52:55],v[140:143],a[124:127]\n" \
qA "a[128:131],v[56:59],v[140:143],a[128:131]\n" qC "v[120:121],v35 offset:38912\n" qA "a[132:135],v[60:63],v[140:143],a[132:135]\n" qA "a[136:139],v[64:67],v[140:143],a[136:139]\n" \
qC "v[122:123],v35 offset:39168\n" qA "a[140:143],v[68:71],v[140:143],a[140:143]\n" qA "a[144:147],v[72:75],v[140:143],a[144:147]\n" qC "v[124:125],v36 offset:38912\n" \
qA "a[148:151],v[76:79],v[140:143],a[148:151]\n" qA "a[152:155],v[80:83],v[140:143],a[152:155]\n" qC "v[126:127],v36 offset:39168\n" qA "a[156:159],v[84:87],v[140:143],a[156:159]\n" \
qA "a[160:163],v[48:51],v[144:147],a[160:163]\n" qD "v[160:163],v39 offset:32768\n" qA "a[164:167],v[52:55],v[144:147],a[164:167]\n" qA "v[16:19],v[76:79],v[28:31],v[16:19]\n" \
qD "v[164:167],v39 offset:34816\n" qA "a[168:171],v[56:59],v[144:147],a[168:171]\n" qA "a[172:175],v[60:63],v[144:147],a[172:175]\n" qD "v[168:171],v39 offset:36864\n" \
qA "a[176:179],v[64:67],v[144:147],a[176:179]\n" qA "a[180:183],v[68:71],v[144:147],a[180:183]\n" qD "v[172:175],v39 offset:38912\n" qA "a[184:187],v[72:75],v[144:147],a[184:187]\n" \
qA "a[188:191],v[76:79],v[144:147],a[188:191]\n" qD "v[176:179],v39 offset:40960\n" qA "a[192:195],v[80:83],v[144:147],a[192:195]\n" qA "a[196:199],v[84:87],v[144:147],a[196:199]\n" \
qD "v[180:183],v39 offset:43008\n" qA "a[200:203],v[48:51],v[148:151],a[200:203]\n" qA "a[204:207],v[52:55],v[148:151],a[204:207]\n" qD "v[184:187],v39 offset:45056\n" \
qA "a[208:211],v[56:59],v[148:151],a[208:211]\n" qA "a[212:215],v[60:63],v[148:151],a[212:215]\n" qD "v[188:191],v39 offset:47104\n" qA "a[216:219],v[64:67],v[148:151],a[216:219]\n" \
qA "a[220:223],v[68:71],v[148:151],a[220:223]\n" qA "a[224:227],v[72:75],v[148:151],a[224:227]\n" qA "a[228:231],v[76:79],v[148:151],a[228:231]\n" qA "v[20:23],v[80:83],v[28:31],v[20:23]\n" \
qA "a[232:235],v[80:83],v[148:151],a[232:235]\n" qA "a[236:239],v[84:87],v[148:151],a[236:239]\n" qA "a[240:243],v[48:51],v[152:155],a[240:243]\n" qA "a[244:247],v[52:55],v[152:155],a[244:247]\n" \
qA "a[248:251],v[56:59],v[152:155],a[248:251]\n" qA "a[252:255],v[60:63],v[152:155],a[252:255]\n" qA "v[192:195],v[64:67],v[152:155],v[192:195]\n" qA "v[196:199],v[68:71],v[152:155],v[196:199]\n" \
qA "v[200:203],v[72:75],v[152:155],v[200:203]\n" qA "v[204:207],v[76:79],v[152:155],v[204:207]\n" qA "v[208:211],v[80:83],v[152:155],v[208:211]\n" qA "v[212:215],v[84:87],v[152:155],v[212:215]\n" \
qA "v[216:219],v[48:51],v[156:159],v[216:219]\n" qA "v[220:223],v[52:55],v[156:159],v[220:223]\n" qA "v[224:227],v[56:59],v[156:159],v[224:227]\n" qA "v[228:231],v[60:63],v[156:159],v[228:231]\n" \
qA "v[24:27],v[84:87],v[28:31],v[24:27]\n" qA "v[232:235],v[64:67],v[156:159],v[232:235]\n" qA "v[236:239],v[68:71],v[156:159],v[236:239]\n" qA "v[240:243],v[72:75],v[156:159],v[240:243]\n" \
qA "v[244:247],v[76:79],v[156:159],v[244:247]\n" qA "v[248:251],v[80:83],v[156:159],v[248:251]\n" qA "v[252:255],v[84:87],v[156:159],v[252:255]\n" qO "lgkmcnt(0)\n" \
qA "a[0:3],v[88:91],v[160:163],a[0:3]\n" qA "a[4:7],v[92:95],v[160:163],a[4:7]\n" qA "a[8:11],v[96:99],v[160:163],a[8:11]\n" qD "v[48:51],v7 offset:0\n" \
qA "a[12:15],v[100:103],v[160:163],a[12:15]\n" qD "v[52:55],v7 offset:256\n" qA "a[16:19],v[104:107],v[160:163],a[16:19]\n" qD "v[56:59],v7 offset:512\n" \
qA "a[20:23],v[108:111],v[160:163],a[20:23]\n" qD "v[60:63],v7 offset:768\n" qA "a[24:27],v[112:115],v[160:163],a[24:27]\n" qD "v[64:67],v7 offset:1024\n" \
qA "a[28:31],v[116:119],v[160:163],a[28:31]\n" qD "v[68:71],v7 offset:1280\n" qA "a[32:35],v[120:123],v[160:163],a[32:35]\n" qD "v[72:75],v7 offset:1536\n" \
qA "a[36:39],v[124:127],v[160:163],a[36:39]\n" qD "v[76:79],v7 offset:1792\n" qA "v[8:11],v[108:111],v[28:31],v[8:11]\n" qA "a[40:43],v[88:91],v[164:167],a[40:43]\n" \
qA "a[44:47],v[92:95],v[164:167],a[44:47]\n" qA "a[48:51],v[96:99],v[164:167],a[48:51]\n" qA "a[52:55],v[100:103],v[164:167],a[52:55]\n" qA "a[56:59],v[104:107],v[164:167],a[56:59]\n" \
qA "a[60:63],v[108:111],v[164:167],a[60:63]\n" qA "a[64:67],v[112:115],v[164:167],a[64:67]\n" qA "a[68:71],v[116:119],v[164:167],a[68:71]\n" qA "a[72:75],v[120:123],v[164:167],a[72:75]\n" \
qA "a[76:79],v[124:127],v[164:167],a[76:79]\n" qA "a[80:83],v[88:91],v[168:171],a[80:83]\n" qA "a[84:87],v[92:95],v[168:171],a[84:87]\n" qA "a[88:91],v[96:99],v[168:171],a[88:91]\n" \
qA "a[92:95],v[100:103],v[168:171],a[92:95]\n" qA "a[96:99],v[104:107],v[168:171],a[96:99]\n" qA "a[100:103],v[108:111],v[168:171],a[100:103]\n" qA "v[12:15],v[112:115],v[28:31],v[12:15]\n" \
qA "a[104:107],v[112:115],v[168:171],a[104:107]\n" qA "a[108:111],v[116:119],v[168:171],a[108:111]\n" qA "a[112:115],v[120:123],v[168:171],a[112:115]\n" qO "lgkmcnt(0)\n" \
qA "a[116:119],v[124:127],v[168:171],a[116:119]\n" "v_min3_u32 v84,v48,v50,v52\n" qA "a[120:123],v[88:91],v[172:175],a[120:123]\n" "v_max3_u32 v85,v48,v50,v52\n" \
qA "a[124:127],v[92:95],v[172:175],a[124:127]\n" "v_min3_u32 v84,v84,v54,v56\n" qA "a[128:131],v[96:99],v[172:175],a[128:131]\n" "v_max3_u32 v85,v85,v54,v56\n" \
qA "a[132:135],v[100:103],v[172:175],a[132:135]\n" "v_min3_u32 v84,v84,v58,v60\n" qA "a[136:139],v[104:107],v[172:175],a[136:139]\n" "v_max3_u32 v85,v85,v58,v60\n" \
qA "a[140:143],v[108:111],v[172:175],a[140:143]\n" "v_min3_u32 v84,v84,v62,v64\n" qA "a[144:147],v[112:115],v[172:175],a[144:147]\n" "v_max3_u32 v85,v85,v62,v64\n" \
qA "a[148:151],v[116:119],v[172:175],a[148:151]\n" "v_min3_u32 v84,v84,v66,v68\n" qA "a[152:155],v[120:123],v[172:175],a[152:155]\n" "v_max3_u32 v85,v85,v66,v68\n" \
qA "a[156:159],v[124:127],v[172:175],a[156:159]\n" "v_min3_u32 v84,v84,v70,v72\n" qA "a[160:163],v[88:91],v[176:179],a[160:163]\n" "v_max3_u32 v85,v85,v70,v72\n" \
qA "a[164:167],v[92:95],v[176:179],a[164:167]\n" "v_min3_u32 v84,v84,v74,v76\n" qA "v[16:19],v[116:119],v[28:31],v[16:19]\n" "v_max3_u32 v85,v85,v74,v76\n" \
qA "a[168:171],v[96:99],v[176:179],a[168:171]\n" "v_min_u32 v84,v84,v78\n" qA "a[172:175],v[100:103],v[176:179],a[172:175]\n" "v_max_u32 v85,v85,v78\n" \
qA "a[176:179],v[104:107],v[176:179],a[176:179]\n" qA "a[180:183],v[108:111],v[176:179],a[180:183]\n" qA "a[184:187],v[112:115],v[176:179],a[184:187]\n" "v_cmp_ne_u32 s[92:93],s75,v84\n" \
qA "a[188:191],v[116:119],v[176:179],a[188:191]\n" "v_cmp_ne_u32 vcc,s75,v85\n" qA "a[192:195],v[120:123],v[176:179],a[192:195]\n" qA "a[196:199],v[124:127],v[176:179],a[196:199]\n" \
qA "a[200:203],v[88:91],v[180:183],a[200:203]\n" qA "a[204:207],v[92:95],v[180:183],a[204:207]\n" "s_or_b64 s[92:93],s[92:93],vcc\n" qA "a[208:211],v[96:99],v[180:183],a[208:211]\n" \
qA "a[212:215],v[100:103],v[180:183],a[212:215]\n" "v_and_b32 v86,0xf0,v47\n" qA "a[216:219],v[104:107],v[180:183],a[216:219]\n" "v_lshlrev_b32 v86,2,v86\n" \
qA "a[220:223],v[108:111],v[180:183],a[220:223]\n" "v_bfe_u32 v87,v47,8,1\n" qA "a[224:227],v[112:115],v[180:183],a[224:227]\n" "v_bfe_u32 v83,v47,9,1\n" \
qA "a[228:231],v[116:119],v[180:183],a[228:231]\n" "v_lshl_or_b32 v87,v87,1,v83\n" qA "v[20:23],v[120:123],v[28:31],v[20:23]\n" "v_bfe_u32 v83,v47,6,2\n" \
qA "a[232:235],v[120:123],v[180:183],a[232:235]\n" "v_xor_b32 v83,v83,v87\n" qA "a[236:239],v[124:127],v[180:183],a[236:239]\n" "v_lshl_add_u32 v83,v83,4,v86\n" \
qA "a[240:243],v[88:91],v[184:187],a[240:243]\n" "v_add_u32 v3,8192,v83\n" qA "a[244:247],v[92:95],v[184:187],a[244:247]\n" "v_lshl_add_u32 v87,v87,4,v86\n" \
qA "a[248:251],v[96:99],v[184:187],a[248:251]\n" "v_add_u32 v1,73728,v87\n" qA "a[252:255],v[100:103],v[184:187],a[252:255]\n" "v_add_u32 v2,24576,v87\n" \
qA "v[192:195],v[104:107],v[184:187],v[192:195]\n" "v_lshrrev_b32 v83,8,v47\n" qA "v[196:199],v[108:111],v[184:187],v[196:199]\n" "v_lshlrev_b32 v83,4,v83\n" \
qA "v[200:203],v[112:115],v[184:187],v[200:203]\n" "v_add_u32 v5,148096,v83\n" qA "v[204:207],v[116:119],v[184:187],v[204:207]\n" "v_add_u32 v4,320,v5\n" \
qA "v[208:211],v[120:123],v[184:187],v[208:211]\n" qN "s56,s76,4096\n" qA "v[212:215],v[124:127],v[184:187],v[212:215]\n" qA "v[216:219],v[88:91],v[188:191],v[216:219]\n" \
qA "v[220:223],v[92:95],v[188:191],v[220:223]\n" qA "v[224:227],v[96:99],v[188:191],v[224:227]\n" qA "v[228:231],v[100:103],v[188:191],v[228:231]\n" qA "v[24:27],v[124:127],v[28:31],v[24:27]\n" \
qA "v[232:235],v[104:107],v[188:191],v[232:235]\n" qA "v[236:239],v[108:111],v[188:191],v[236:239]\n" qA "v[240:243],v[112:115],v[188:191],v[240:243]\n" qA "v[244:247],v[116:119],v[188:191],v[244:247]\n" \
qA "v[248:251],v[120:123],v[188:191],v[248:251]\n" qA "v[252:255],v[124:127],v[188:191],v[252:255]\n" "s_cmp_eq_u64 s[92:93],0\n" "s_cbranch_scc1 G1OK3_%=\n" \
"s_mov_b32 s63,0x100000\n" "v_subrev_u32 v82,s64,v7\n" "v_add_u32 v82,3072,v82\n" "G1RE3_%=:\n" \
"s_sleep 1\n" qE "v[48:51],v82,s[84:87],0 offen offset:0 sc1\n" qE "v[52:55],v82,s[84:87],0 offen offset:256 sc1\n" qE "v[56:59],v82,s[84:87],0 offen offset:512 sc1\n" \
qE "v[60:63],v82,s[84:87],0 offen offset:768 sc1\n" qE "v[64:67],v82,s[84:87],0 offen offset:1024 sc1\n" qE "v[68:71],v82,s[84:87],0 offen offset:1280 sc1\n" qE "v[72:75],v82,s[84:87],0 offen offset:1536 sc1\n" \
qE "v[76:79],v82,s[84:87],0 offen offset:1792 sc1\n" qO "vmcnt(0)\n" "v_min3_u32 v84,v48,v50,v52\n" "v_max3_u32 v85,v48,v50,v52\n" \
"v_min3_u32 v84,v84,v54,v56\n" "v_max3_u32 v85,v85,v54,v56\n" "v_min3_u32 v84,v84,v58,v60\n" "v_max3_u32 v85,v85,v58,v60\n" \
"v_min3_u32 v84,v84,v62,v64\n" "v_max3_u32 v85,v85,v62,v64\n" "v_min3_u32 v84,v84,v66,v68\n" "v_max3_u32 v85,v85,v66,v68\n" \
"v_min3_u32 v84,v84,v70,v72\n" "v_max3_u32 v85,v85,v70,v72\n" "v_min3_u32 v84,v84,v74,v76\n" "v_max3_u32 v85,v85,v74,v76\n" \
"v_min_u32 v84,v84,v78\n" "v_max_u32 v85,v85,v78\n" "v_cmp_ne_u32 s[92:93],s75,v84\n" "v_cmp_ne_u32 vcc,s75,v85\n" \
"s_nop 3\n" "s_or_b64 s[92:93],s[92:93],vcc\n" "s_sub_u32 s63,s63,1\n" "s_cmp_eq_u32 s63,0\n" \
"s_cbranch_scc1 G1OK3_%=\n" "s_cmp_eq_u64 s[92:93],0\n" "s_cbranch_scc0 G1RE3_%=\n" "G1OK3_%=:\n" \
"s_nop 7\n" "s_mov_b32 exec_lo,0x00010001\n" "s_mov_b32 exec_hi,0x00010001\n" qQ "v4,v[8:11] offset:0\n" \
qQ "v4,v[12:15] offset:64\n" qQ "v4,v[16:19] offset:128\n" qQ "v4,v[20:23] offset:192\n" qQ "v4,v[24:27] offset:256\n" \
"s_mov_b64 exec,-1\n" qO "lgkmcnt(0)\n" "s_barrier\n" qD "v[8:11],v5 offset:0\n" \
qD "v[12:15],v5 offset:64\n" qD "v[16:19],v5 offset:128\n" qD "v[20:23],v5 offset:192\n" qD "v[24:27],v5 offset:256\n" \
qD "v[28:31],v5 offset:320\n" qD "v[32:35],v5 offset:384\n" qD "v[36:39],v5 offset:448\n" qD "v[40:43],v5 offset:512\n" \
qD "v[44:47],v5 offset:576\n" qO "lgkmcnt(0)\n" qG "v80,a32\n" qG "v81,a33\n" \
qG "v82,a34\n" qG "v83,a35\n" qG "v84,a36\n" qG "v85,a37\n" \
qG "v86,a38\n" qG "v87,a39\n" qG "v88,a72\n" qG "v89,a73\n" \
qG "v90,a74\n" qG "v91,a75\n" qG "v92,a76\n" qG "v93,a77\n" \
qG "v94,a78\n" qG "v95,a79\n" qH "v[80:81],v[48:49],v[40:41],v[80:81]" qR qH "v[82:83],v[48:49],v[42:43],v[82:83]" qR \
qH "v[84:85],v[48:49],v[44:45],v[84:85]" qR qH "v[86:87],v[48:49],v[46:47],v[86:87]" qR qG "v96,a112\n" qG "v97,a113\n" \
qG "v98,a114\n" qG "v99,a115\n" qG "v100,a116\n" qG "v101,a117\n" \
qG "v102,a118\n" qG "v103,a119\n" qH "v[88:89],v[52:53],v[40:41],v[88:89]" qR qH "v[90:91],v[52:53],v[42:43],v[90:91]" qR \
qH "v[92:93],v[52:53],v[44:45],v[92:93]" qR qH "v[94:95],v[52:53],v[46:47],v[94:95]" qR qI "v[80:81],v[80:81],v[50:51]" qS qI "v[82:83],v[82:83],v[50:51]" qS \
qI "v[84:85],v[84:85],v[50:51]" qS qI "v[86:87],v[86:87],v[50:51]" qS qG "v104,a152\n" qG "v105,a153\n" \
qG "v106,a154\n" qG "v107,a155\n" qG "v108,a156\n" qG "v109,a157\n" \
qG "v110,a158\n" qG "v111,a159\n" qH "v[96:97],v[56:57],v[40:41],v[96:97]" qR qH "v[98:99],v[56:57],v[42:43],v[98:99]" qR \
qH "v[100:101],v[56:57],v[44:45],v[100:101]" qR qH "v[102:103],v[56:57],v[46:47],v[102:103]" qR qI "v[88:89],v[88:89],v[54:55]" qS qI "v[90:91],v[90:91],v[54:55]" qS \
qI "v[92:93],v[92:93],v[54:55]" qS qI "v[94:95],v[94:95],v[54:55]" qS qJ "v112,v80,v81\n" qJ "v113,v82,v83\n" \
qJ "v114,v84,v85\n" qJ "v115,v86,v87\n" qH "v[104:105],v[60:61],v[40:41],v[104:105]" qR qH "v[106:107],v[60:61],v[42:43],v[106:107]" qR \
qH "v[108:109],v[60:61],v[44:45],v[108:109]" qR qH "v[110:111],v[60:61],v[46:47],v[110:111]" qR qI "v[96:97],v[96:97],v[58:59]" qS qI "v[98:99],v[98:99],v[58:59]" qS \
qI "v[100:101],v[100:101],v[58:59]" qS qI "v[102:103],v[102:103],v[58:59]" qS qJ "v116,v88,v89\n" qJ "v117,v90,v91\n" \
qJ "v118,v92,v93\n" qJ "v119,v94,v95\n" qK "v112,v114\n" qK "v113,v115\n" \
qI "v[104:105],v[104:105],v[62:63]" qS qI "v[106:107],v[106:107],v[62:63]" qS qI "v[108:109],v[108:109],v[62:63]" qS qI "v[110:111],v[110:111],v[62:63]" qS \
qJ "v120,v96,v97\n" qJ "v121,v98,v99\n" qJ "v122,v100,v101\n" qJ "v123,v102,v103\n" \
qK "v116,v118\n" qK "v117,v119\n" qJ "v124,v104,v105\n" qJ "v125,v106,v107\n" \
qJ "v126,v108,v109\n" qJ "v127,v110,v111\n" qF "v[112:115],v2,s[84:87],s76 offen offset:0 sc1\n" qK "v120,v122\n" \
qK "v121,v123\n" qK "v124,v126\n" qK "v125,v127\n" qG "v80,a192\n" \
qG "v81,a193\n" qF "v[116:119],v2,s[84:87],s76 offen offset:1024 sc1\n" qG "v82,a194\n" qG "v83,a195\n" \
qG "v84,a196\n" qG "v85,a197\n" qG "v86,a198\n" qG "v87,a199\n" \
qG "v88,a232\n" qG "v89,a233\n" qF "v[120:123],v2,s[84:87],s76 offen offset:2048 sc1\n" qG "v90,a234\n" \
qG "v91,a235\n" qG "v92,a236\n" qG "v93,a237\n" qG "v94,a238\n" \
qG "v95,a239\n" qH "v[80:81],v[64:65],v[40:41],v[80:81]" qR qH "v[82:83],v[64:65],v[42:43],v[82:83]" qR qF "v[124:127],v2,s[84:87],s76 offen offset:3072 sc1\n" \
qH "v[84:85],v[64:65],v[44:45],v[84:85]" qR qH "v[86:87],v[64:65],v[46:47],v[86:87]" qR qQ "v1,v[112:115] offset:0\n" qH "v[88:89],v[68:69],v[40:41],v[88:89]" qR \
qH "v[90:91],v[68:69],v[42:43],v[90:91]" qR qH "v[92:93],v[68:69],v[44:45],v[92:93]" qR qH "v[94:95],v[68:69],v[46:47],v[94:95]" qR qI "v[80:81],v[80:81],v[66:67]" qS \
qI "v[82:83],v[82:83],v[66:67]" qS qI "v[84:85],v[84:85],v[66:67]" qS qI "v[86:87],v[86:87],v[66:67]" qS qQ "v1,v[116:119] offset:1024\n" \
qH "v[96:97],v[72:73],v[40:41],v[208:209]" qR qH "v[98:99],v[72:73],v[42:43],v[210:211]" qR qH "v[100:101],v[72:73],v[44:45],v[212:213]" qR qH "v[102:103],v[72:73],v[46:47],v[214:215]" qR \
qI "v[88:89],v[88:89],v[70:71]" qS qI "v[90:91],v[90:91],v[70:71]" qS qI "v[92:93],v[92:93],v[70:71]" qS qI "v[94:95],v[94:95],v[70:71]" qS \
qJ "v112,v80,v81\n" qJ "v113,v82,v83\n" qJ "v114,v84,v85\n" qJ "v115,v86,v87\n" \
qQ "v1,v[120:123] offset:2048\n" qH "v[104:105],v[76:77],v[40:41],v[248:249]" qR qH "v[106:107],v[76:77],v[42:43],v[250:251]" qR qH "v[108:109],v[76:77],v[44:45],v[252:253]" qR \
qH "v[110:111],v[76:77],v[46:47],v[254:255]" qR qI "v[96:97],v[96:97],v[74:75]" qS qI "v[98:99],v[98:99],v[74:75]" qS qI "v[100:101],v[100:101],v[74:75]" qS \
qI "v[102:103],v[102:103],v[74:75]" qS qJ "v116,v88,v89\n" qJ "v117,v90,v91\n" qJ "v118,v92,v93\n" \
qJ "v119,v94,v95\n" qQ "v1,v[124:127] offset:3072\n" qK "v112,v114\n" qK "v113,v115\n" \
qI "v[104:105],v[104:105],v[78:79]" qS qI "v[106:107],v[106:107],v[78:79]" qS qI "v[108:109],v[108:109],v[78:79]" qS qI "v[110:111],v[110:111],v[78:79]" qS \
qJ "v120,v96,v97\n" qJ "v121,v98,v99\n" qJ "v122,v100,v101\n" qJ "v123,v102,v103\n" \
qK "v116,v118\n" qK "v117,v119\n" qJ "v124,v104,v105\n" qJ "v125,v106,v107\n" \
qJ "v126,v108,v109\n" qJ "v127,v110,v111\n" qF "v[112:115],v2,s[84:87],s56 offen offset:0 sc1\n" qK "v120,v122\n" \
qK "v121,v123\n" qK "v124,v126\n" qK "v125,v127\n" qG "v80,a24\n" \
qG "v81,a25\n" qF "v[116:119],v2,s[84:87],s56 offen offset:1024 sc1\n" qG "v82,a26\n" qG "v83,a27\n" \
qG "v84,a28\n" qG "v85,a29\n" qG "v86,a30\n" qG "v87,a31\n" \
qG "v88,a64\n" qG "v89,a65\n" qF "v[120:123],v2,s[84:87],s56 offen offset:2048 sc1\n" qG "v90,a66\n" \
qG "v91,a67\n" qG "v92,a68\n" qG "v93,a69\n" qG "v94,a70\n" \
qG "v95,a71\n" qH "v[80:81],v[48:49],v[32:33],v[80:81]" qR qH "v[82:83],v[48:49],v[34:35],v[82:83]" qR qF "v[124:127],v2,s[84:87],s56 offen offset:3072 sc1\n" \
qH "v[84:85],v[48:49],v[36:37],v[84:85]" qR qH "v[86:87],v[48:49],v[38:39],v[86:87]" qR "v_mul_f32 v4,0x3e8293ee,v51\n" qQ "v1,v[112:115] offset:4096\n" \
qG "v96,a104\n" qG "v97,a105\n" qG "v98,a106\n" qG "v99,a107\n" \
qG "v100,a108\n" qG "v101,a109\n" qG "v102,a110\n" qG "v103,a111\n" \
qH "v[88:89],v[52:53],v[32:33],v[88:89]" qR qH "v[90:91],v[52:53],v[34:35],v[90:91]" qR qH "v[92:93],v[52:53],v[36:37],v[92:93]" qR qH "v[94:95],v[52:53],v[38:39],v[94:95]" qR \
"v_mul_f32 v5,0x3e8293ee,v55\n" qI "v[80:81],v[80:81],v[4:5]" qT qI "v[82:83],v[82:83],v[4:5]" qT qI "v[84:85],v[84:85],v[4:5]" qT \
qI "v[86:87],v[86:87],v[4:5]" qT qQ "v1,v[116:119] offset:5120\n" qG "v104,a144\n" qG "v105,a145\n" \
qG "v106,a146\n" qG "v107,a147\n" qG "v108,a148\n" qG "v109,a149\n" \
qG "v110,a150\n" qG "v111,a151\n" qH "v[96:97],v[56:57],v[32:33],v[96:97]" qR qH "v[98:99],v[56:57],v[34:35],v[98:99]" qR \
qH "v[100:101],v[56:57],v[36:37],v[100:101]" qR qH "v[102:103],v[56:57],v[38:39],v[102:103]" qR "v_mul_f32 v4,0x3e8293ee,v59\n" qI "v[88:89],v[88:89],v[4:5]" qS \
qI "v[90:91],v[90:91],v[4:5]" qS qI "v[92:93],v[92:93],v[4:5]" qS qI "v[94:95],v[94:95],v[4:5]" qS qJ "v112,v80,v81\n" \
qJ "v113,v82,v83\n" qJ "v114,v84,v85\n" qJ "v115,v86,v87\n" qQ "v1,v[120:123] offset:6144\n" \
qH "v[104:105],v[60:61],v[32:33],v[104:105]" qR qH "v[106:107],v[60:61],v[34:35],v[106:107]" qR qH "v[108:109],v[60:61],v[36:37],v[108:109]" qR qH "v[110:111],v[60:61],v[38:39],v[110:111]" qR \
"v_mul_f32 v5,0x3e8293ee,v63\n" qI "v[96:97],v[96:97],v[4:5]" qT qI "v[98:99],v[98:99],v[4:5]" qT qI "v[100:101],v[100:101],v[4:5]" qT \
qI "v[102:103],v[102:103],v[4:5]" qT qJ "v116,v88,v89\n" qJ "v117,v90,v91\n" qJ "v118,v92,v93\n" \
qJ "v119,v94,v95\n" qQ "v1,v[124:127] offset:7168\n" qG "v80,a184\n" qG "v81,a185\n" \
qG "v82,a186\n" qG "v83,a187\n" qG "v84,a188\n" qG "v85,a189\n" \
qG "v86,a190\n" qG "v87,a191\n" qK "v112,v114\n" qK "v113,v115\n" \
qI "v[104:105],v[104:105],v[4:5]" qS qI "v[106:107],v[106:107],v[4:5]" qS qI "v[108:109],v[108:109],v[4:5]" qS qI "v[110:111],v[110:111],v[4:5]" qS \
qJ "v120,v96,v97\n" qJ "v121,v98,v99\n" qJ "v122,v100,v101\n" qJ "v123,v102,v103\n" \
qG "v88,a224\n" qG "v89,a225\n" qG "v90,a226\n" qG "v91,a227\n" \
qG "v92,a228\n" qG "v93,a229\n" qG "v94,a230\n" qG "v95,a231\n" \
qH "v[80:81],v[64:65],v[32:33],v[80:81]" qR qH "v[82:83],v[64:65],v[34:35],v[82:83]" qR qH "v[84:85],v[64:65],v[36:37],v[84:85]" qR qH "v[86:87],v[64:65],v[38:39],v[86:87]" qR \
"v_mul_f32 v4,0x3e8293ee,v67\n" qK "v116,v118\n" qK "v117,v119\n" qJ "v124,v104,v105\n" \
qJ "v125,v106,v107\n" qJ "v126,v108,v109\n" qJ "v127,v110,v111\n" qQ "v3,v[112:115] offset:0\n" \
qH "v[88:89],v[68:69],v[32:33],v[88:89]" qR qH "v[90:91],v[68:69],v[34:35],v[90:91]" qR qH "v[92:93],v[68:69],v[36:37],v[92:93]" qR qH "v[94:95],v[68:69],v[38:39],v[94:95]" qR \
"v_mul_f32 v5,0x3e8293ee,v71\n" qK "v120,v122\n" qK "v121,v123\n" qI "v[80:81],v[80:81],v[4:5]" qT \
qI "v[82:83],v[82:83],v[4:5]" qT qI "v[84:85],v[84:85],v[4:5]" qT qI "v[86:87],v[86:87],v[4:5]" qT qQ "v3,v[116:119] offset:1024\n" \
qH "v[96:97],v[72:73],v[32:33],v[200:201]" qR qH "v[98:99],v[72:73],v[34:35],v[202:203]" qR qH "v[100:101],v[72:73],v[36:37],v[204:205]" qR qH "v[102:103],v[72:73],v[38:39],v[206:207]" qR \
"v_mul_f32 v4,0x3e8293ee,v75\n" qK "v124,v126\n" qK "v125,v127\n" qI "v[88:89],v[88:89],v[4:5]" qS \
qI "v[90:91],v[90:91],v[4:5]" qS qI "v[92:93],v[92:93],v[4:5]" qS qI "v[94:95],v[94:95],v[4:5]" qS qJ "v112,v80,v81\n" \
qJ "v113,v82,v83\n" qJ "v114,v84,v85\n" qJ "v115,v86,v87\n" qQ "v3,v[120:123] offset:2048\n" \
qH "v[104:105],v[76:77],v[32:33],v[240:241]" qR qH "v[106:107],v[76:77],v[34:35],v[242:243]" qR qH "v[108:109],v[76:77],v[36:37],v[244:245]" qR qH "v[110:111],v[76:77],v[38:39],v[246:247]" qR \
"v_mul_f32 v5,0x3e8293ee,v79\n" qI "v[96:97],v[96:97],v[4:5]" qT qI "v[98:99],v[98:99],v[4:5]" qT qI "v[100:101],v[100:101],v[4:5]" qT \
qI "v[102:103],v[102:103],v[4:5]" qT qJ "v116,v88,v89\n" qJ "v117,v90,v91\n" qJ "v118,v92,v93\n" \
qJ "v119,v94,v95\n" qQ "v3,v[124:127] offset:3072\n" qK "v112,v114\n" qK "v113,v115\n" \
qI "v[104:105],v[104:105],v[4:5]" qS qI "v[106:107],v[106:107],v[4:5]" qS qI "v[108:109],v[108:109],v[4:5]" qS qI "v[110:111],v[110:111],v[4:5]" qS \
qJ "v120,v96,v97\n" qJ "v121,v98,v99\n" qJ "v122,v100,v101\n" qJ "v123,v102,v103\n" \
qK "v116,v118\n" qK "v117,v119\n" qJ "v124,v104,v105\n" qJ "v125,v106,v107\n" \
qJ "v126,v108,v109\n" qJ "v127,v110,v111\n" qQ "v3,v[112:115] offset:4096\n" qK "v120,v122\n" \
qK "v121,v123\n" qQ "v3,v[116:119] offset:5120\n" qK "v124,v126\n" qK "v125,v127\n" \
qQ "v3,v[120:123] offset:6144\n" qQ "v3,v[124:127] offset:7168\n" "v_mbcnt_lo_u32_b32 v82,-1,0\n" "v_mbcnt_hi_u32_b32 v82,-1,v82\n" \
"v_lshlrev_b32 v82,4,v82\n" "v_and_b32 v1,48,v82\n" "v_lshrrev_b32 v1,1,v1\n" "v_mov_b32 v3,s75\n" \
"v_xor_b32 v2,0x9e3779b9,v3\n" qO "vmcnt(0) lgkmcnt(0)\n" "s_mov_b64 exec,1\n" "buffer_store_dwordx2 v[2:3],v1,s[84:87],s77 offen sc1\n" \
"s_mov_b64 exec,-1\n" qG "v80,a0\n" qG "v81,a1\n" qG "v82,a2\n" \
qG "v83,a3\n" qG "v88,a8\n" qG "v89,a9\n" qG "v90,a10\n" \
qG "v91,a11\n" qG "v96,a16\n" qG "v97,a17\n" qG "v98,a18\n" \
qG "v99,a19\n" qG "v104,a40\n" qG "v105,a41\n" qG "v106,a42\n" \
qG "v107,a43\n" qH "v[80:81],v[48:49],v[8:9],v[80:81]" qR qH "v[82:83],v[48:49],v[10:11],v[82:83]" qR qH "v[88:89],v[48:49],v[16:17],v[88:89]" qR \
qH "v[90:91],v[48:49],v[18:19],v[90:91]" qR qH "v[96:97],v[48:49],v[24:25],v[96:97]" qR qH "v[98:99],v[48:49],v[26:27],v[98:99]" qR qH "v[104:105],v[52:53],v[8:9],v[104:105]" qR \
qH "v[106:107],v[52:53],v[10:11],v[106:107]" qR "v_mul_f32 v6,v51,v51\n" qG "v84,a4\n" qG "v85,a5\n" \
qG "v86,a6\n" qG "v87,a7\n" qG "v92,a12\n" qG "v93,a13\n" \
qG "v94,a14\n" qG "v95,a15\n" qG "v100,a20\n" qG "v101,a21\n" \
qG "v102,a22\n" qG "v103,a23\n" "v_mul_f32 v7,v55,v55\n" qG "v108,a44\n" \
qG "v109,a45\n" qG "v110,a46\n" qG "v111,a47\n" qH "v[84:85],v[48:49],v[12:13],v[84:85]" qR \
qH "v[86:87],v[48:49],v[14:15],v[86:87]" qR qI "v[80:81],v[80:81],v[6:7]" qT qI "v[82:83],v[82:83],v[6:7]" qT qH "v[92:93],v[48:49],v[20:21],v[92:93]" qR \
qH "v[94:95],v[48:49],v[22:23],v[94:95]" qR qI "v[88:89],v[88:89],v[6:7]" qT qI "v[90:91],v[90:91],v[6:7]" qT qH "v[100:101],v[48:49],v[28:29],v[100:101]" qR \
qH "v[102:103],v[48:49],v[30:31],v[102:103]" qR qI "v[96:97],v[96:97],v[6:7]" qT qI "v[98:99],v[98:99],v[6:7]" qT qH "v[108:109],v[52:53],v[12:13],v[108:109]" qR \
qH "v[110:111],v[52:53],v[14:15],v[110:111]" qR qI "v[104:105],v[104:105],v[6:7]" qS qI "v[106:107],v[106:107],v[6:7]" qS qI "v[80:81],v[80:81],v[84:85]\n" \
qI "v[82:83],v[82:83],v[86:87]\n" qI "v[88:89],v[88:89],v[92:93]\n" qI "v[90:91],v[90:91],v[94:95]\n" qI "v[96:97],v[96:97],v[100:101]\n" \
qI "v[98:99],v[98:99],v[102:103]\n" qI "v[104:105],v[104:105],v[108:109]\n" qI "v[106:107],v[106:107],v[110:111]\n" qJ "v128,v80,v81\n" \
qJ "v129,v82,v83\n" qJ "v130,v88,v89\n" qJ "v131,v90,v91\n" qJ "v136,v104,v105\n" \
qJ "v137,v106,v107\n" qJ "v132,v96,v97\n" qJ "v133,v98,v99\n" qG "v80,a48\n" \
qG "v81,a49\n" qG "v82,a50\n" qG "v83,a51\n" qG "v88,a56\n" \
qG "v89,a57\n" qG "v90,a58\n" qG "v91,a59\n" qG "v96,a80\n" \
qG "v97,a81\n" qG "v98,a82\n" qG "v99,a83\n" qG "v104,a88\n" \
qG "v105,a89\n" qG "v106,a90\n" qG "v107,a91\n" qH "v[80:81],v[52:53],v[16:17],v[80:81]" qR \
qH "v[82:83],v[52:53],v[18:19],v[82:83]" qR qH "v[88:89],v[52:53],v[24:25],v[88:89]" qR qH "v[90:91],v[52:53],v[26:27],v[90:91]" qR qH "v[96:97],v[56:57],v[8:9],v[96:97]" qR \
qH "v[98:99],v[56:57],v[10:11],v[98:99]" qR qH "v[104:105],v[56:57],v[16:17],v[104:105]" qR qH "v[106:107],v[56:57],v[18:19],v[106:107]" qR qG "v84,a52\n" \
qG "v85,a53\n" qG "v86,a54\n" qG "v87,a55\n" qG "v92,a60\n" \
qG "v93,a61\n" qG "v94,a62\n" qG "v95,a63\n" "v_mul_f32 v6,v59,v59\n" \
qG "v100,a84\n" qG "v101,a85\n" qG "v102,a86\n" qG "v103,a87\n" \
qG "v108,a92\n" qG "v109,a93\n" qG "v110,a94\n" qG "v111,a95\n" \
qH "v[84:85],v[52:53],v[20:21],v[84:85]" qR qH "v[86:87],v[52:53],v[22:23],v[86:87]" qR qI "v[80:81],v[80:81],v[6:7]" qS qI "v[82:83],v[82:83],v[6:7]" qS \
qH "v[92:93],v[52:53],v[28:29],v[92:93]" qR qH "v[94:95],v[52:53],v[30:31],v[94:95]" qR qI "v[88:89],v[88:89],v[6:7]" qS qI "v[90:91],v[90:91],v[6:7]" qS \
qH "v[100:101],v[56:57],v[12:13],v[100:101]" qR qH "v[102:103],v[56:57],v[14:15],v[102:103]" qR qI "v[96:97],v[96:97],v[6:7]" qT qI "v[98:99],v[98:99],v[6:7]" qT \
qH "v[108:109],v[56:57],v[20:21],v[108:109]" qR qH "v[110:111],v[56:57],v[22:23],v[110:111]" qR qI "v[104:105],v[104:105],v[6:7]" qT qI "v[106:107],v[106:107],v[6:7]" qT \
qI "v[80:81],v[80:81],v[84:85]\n" qI "v[82:83],v[82:83],v[86:87]\n" qI "v[88:89],v[88:89],v[92:93]\n" qI "v[90:91],v[90:91],v[94:95]\n" \
qI "v[96:97],v[96:97],v[100:101]\n" qI "v[98:99],v[98:99],v[102:103]\n" qI "v[104:105],v[104:105],v[108:109]\n" qI "v[106:107],v[106:107],v[110:111]\n" \
qJ "v138,v80,v81\n" qJ "v139,v82,v83\n" qJ "v134,v88,v89\n" qJ "v135,v90,v91\n" \
qJ "v144,v96,v97\n" qJ "v145,v98,v99\n" qJ "v146,v104,v105\n" qJ "v147,v106,v107\n" \
qG "v80,a96\n" qG "v81,a97\n" qG "v82,a98\n" qG "v83,a99\n" \
qG "v88,a120\n" qG "v89,a121\n" qG "v90,a122\n" qG "v91,a123\n" \
qG "v96,a128\n" qG "v97,a129\n" qG "v98,a130\n" qG "v99,a131\n" \
qG "v104,a136\n" qG "v105,a137\n" qG "v106,a138\n" qG "v107,a139\n" \
qH "v[80:81],v[56:57],v[24:25],v[80:81]" qR qH "v[82:83],v[56:57],v[26:27],v[82:83]" qR qH "v[88:89],v[60:61],v[8:9],v[88:89]" qR qH "v[90:91],v[60:61],v[10:11],v[90:91]" qR \
qH "v[96:97],v[60:61],v[16:17],v[96:97]" qR qH "v[98:99],v[60:61],v[18:19],v[98:99]" qR qH "v[104:105],v[60:61],v[24:25],v[104:105]" qR qH "v[106:107],v[60:61],v[26:27],v[106:107]" qR \
qG "v84,a100\n" qG "v85,a101\n" qG "v86,a102\n" qG "v87,a103\n" \
"v_mul_f32 v7,v63,v63\n" qG "v92,a124\n" qG "v93,a125\n" qG "v94,a126\n" \
qG "v95,a127\n" qG "v100,a132\n" qG "v101,a133\n" qG "v102,a134\n" \
qG "v103,a135\n" qG "v108,a140\n" qG "v109,a141\n" qG "v110,a142\n" \
qG "v111,a143\n" qH "v[84:85],v[56:57],v[28:29],v[84:85]" qR qH "v[86:87],v[56:57],v[30:31],v[86:87]" qR qI "v[80:81],v[80:81],v[6:7]" qT \
qI "v[82:83],v[82:83],v[6:7]" qT qH "v[92:93],v[60:61],v[12:13],v[92:93]" qR qH "v[94:95],v[60:61],v[14:15],v[94:95]" qR qI "v[88:89],v[88:89],v[6:7]" qS \
qI "v[90:91],v[90:91],v[6:7]" qS qH "v[100:101],v[60:61],v[20:21],v[100:101]" qR qH "v[102:103],v[60:61],v[22:23],v[102:103]" qR qI "v[96:97],v[96:97],v[6:7]" qS \
qI "v[98:99],v[98:99],v[6:7]" qS qH "v[108:109],v[60:61],v[28:29],v[108:109]" qR qH "v[110:111],v[60:61],v[30:31],v[110:111]" qR qI "v[104:105],v[104:105],v[6:7]" qS \
qI "v[106:107],v[106:107],v[6:7]" qS qI "v[80:81],v[80:81],v[84:85]\n" qI "v[82:83],v[82:83],v[86:87]\n" qI "v[88:89],v[88:89],v[92:93]\n" \
qI "v[90:91],v[90:91],v[94:95]\n" qI "v[96:97],v[96:97],v[100:101]\n" qI "v[98:99],v[98:99],v[102:103]\n" qI "v[104:105],v[104:105],v[108:109]\n" \
qI "v[106:107],v[106:107],v[110:111]\n" qJ "v152,v88,v89\n" qJ "v153,v90,v91\n" qJ "v154,v96,v97\n" \
qJ "v155,v98,v99\n" qJ "v148,v80,v81\n" qJ "v149,v82,v83\n" qJ "v150,v104,v105\n" \
qJ "v151,v106,v107\n" qK "v128,v130\n" qK "v129,v131\n" qK "v136,v138\n" \
qK "v137,v139\n" qK "v132,v134\n" qK "v133,v135\n" qK "v144,v146\n" \
qK "v145,v147\n" qK "v152,v154\n" qK "v153,v155\n" qK "v148,v150\n" \
qK "v149,v151\n" "s_bitcmp1_b32 s59,9\n" "s_cbranch_scc1 EPP3_%=\n" "s_xor_b32 s94,s75,0x9e3779b9\n" \
"s_mov_b32 s95,s75\n" "s_mov_b32 s58,0x400000\n" "EPQ3_%=:\n" "buffer_load_dwordx2 v[2:3],v1,s[84:87],s78 offen sc1\n" \
qO "vmcnt(0)\n" "v_cmp_ne_u64 vcc,s[94:95],v[2:3]\n" "s_nop 3\n" "s_cmp_eq_u64 vcc,0\n" \
"s_cbranch_scc1 EPP3_%=\n" "s_sleep 1\n" "s_sub_u32 s58,s58,1\n" "s_cmp_eq_u32 s58,0\n" \
"s_cbranch_scc0 EPQ3_%=\n" "EPP3_%=:\n" "v_mbcnt_lo_u32_b32 v82,-1,0\n" "v_mbcnt_hi_u32_b32 v82,-1,v82\n" \
"v_lshlrev_b32 v82,4,v82\n" "v_mov_b32 v47,v82\n" "v_add_u32 v1,3072,v82\n" "v_lshrrev_b32 v2,6,v82\n" \
"v_lshlrev_b32 v2,6,v2\n" "v_bfe_u32 v3,v82,4,2\n" "v_lshrrev_b32 v83,8,v82\n" "v_xor_b32 v83,v83,v3\n" \
"v_lshl_add_u32 v3,v3,4,v2\n" "v_lshl_add_u32 v2,v83,4,v2\n" "v_add_u32 v2,19456,v2\n" "v_add_u32 v3,52224,v3\n" \
"s_bitcmp1_b32 s59,9\n" "s_cbranch_scc1 EPZ3_%=\n" "s_sub_u32 s57,s76,1048576\n" qE "a[0:3],v1,s[84:87],s57 offen sc1\n" \
qN "s57,s57,4096\n" qE "a[4:7],v1,s[84:87],s57 offen sc1\n" qN "s57,s57,4096\n" qE "a[8:11],v1,s[84:87],s57 offen sc1\n" \
qN "s57,s57,4096\n" qE "a[12:15],v1,s[84:87],s57 offen sc1\n" qN "s57,s57,4096\n" qE "a[16:19],v1,s[84:87],s57 offen sc1\n" \
qN "s57,s57,4096\n" qE "a[20:23],v1,s[84:87],s57 offen sc1\n" qN "s57,s57,4096\n" qE "a[24:27],v1,s[84:87],s57 offen sc1\n" \
qN "s57,s57,4096\n" qE "a[28:31],v1,s[84:87],s57 offen sc1\n" "s_branch EPL3_%=\n" "EPZ3_%=:\n" \
"v_accvgpr_write_b32 a0,0\n" "v_accvgpr_write_b32 a1,0\n" "v_accvgpr_write_b32 a2,0\n" "v_accvgpr_write_b32 a3,0\n" \
"v_accvgpr_write_b32 a4,0\n" "v_accvgpr_write_b32 a5,0\n" "v_accvgpr_write_b32 a6,0\n" "v_accvgpr_write_b32 a7,0\n" \
"v_accvgpr_write_b32 a8,0\n" "v_accvgpr_write_b32 a9,0\n" "v_accvgpr_write_b32 a10,0\n" "v_accvgpr_write_b32 a11,0\n" \
"v_accvgpr_write_b32 a12,0\n" "v_accvgpr_write_b32 a13,0\n" "v_accvgpr_write_b32 a14,0\n" "v_accvgpr_write_b32 a15,0\n" \
"v_accvgpr_write_b32 a16,0\n" "v_accvgpr_write_b32 a17,0\n" "v_accvgpr_write_b32 a18,0\n" "v_accvgpr_write_b32 a19,0\n" \
"v_accvgpr_write_b32 a20,0\n" "v_accvgpr_write_b32 a21,0\n" "v_accvgpr_write_b32 a22,0\n" "v_accvgpr_write_b32 a23,0\n" \
"v_accvgpr_write_b32 a24,0\n" "v_accvgpr_write_b32 a25,0\n" "v_accvgpr_write_b32 a26,0\n" "v_accvgpr_write_b32 a27,0\n" \
"v_accvgpr_write_b32 a28,0\n" "v_accvgpr_write_b32 a29,0\n" "v_accvgpr_write_b32 a30,0\n" "v_accvgpr_write_b32 a31,0\n" \
"EPL3_%=:\n" qG "v80,a160\n" qG "v81,a161\n" qG "v82,a162\n" \
qG "v83,a163\n" qG "v88,a168\n" qG "v89,a169\n" qG "v90,a170\n" \
qG "v91,a171\n" qG "v96,a176\n" qG "v97,a177\n" qG "v98,a178\n" \
qG "v99,a179\n" qG "v104,a200\n" qG "v105,a201\n" qG "v106,a202\n" \
qG "v107,a203\n" qH "v[80:81],v[64:65],v[8:9],v[80:81]" qR qH "v[82:83],v[64:65],v[10:11],v[82:83]" qR qH "v[88:89],v[64:65],v[16:17],v[88:89]" qR \
qH "v[90:91],v[64:65],v[18:19],v[90:91]" qR qH "v[96:97],v[64:65],v[24:25],v[96:97]" qR qH "v[98:99],v[64:65],v[26:27],v[98:99]" qR qH "v[104:105],v[68:69],v[8:9],v[104:105]" qR \
qH "v[106:107],v[68:69],v[10:11],v[106:107]" qR "v_mul_f32 v6,v67,v67\n" qG "v84,a164\n" qG "v85,a165\n" \
qG "v86,a166\n" qG "v87,a167\n" qG "v92,a172\n" qG "v93,a173\n" \
qG "v94,a174\n" qG "v95,a175\n" qG "v100,a180\n" qG "v101,a181\n" \
qG "v102,a182\n" qG "v103,a183\n" "v_mul_f32 v7,v71,v71\n" qG "v108,a204\n" \
qG "v109,a205\n" qG "v110,a206\n" qG "v111,a207\n" qH "v[84:85],v[64:65],v[12:13],v[84:85]" qR \
qH "v[86:87],v[64:65],v[14:15],v[86:87]" qR qI "v[80:81],v[80:81],v[6:7]" qT qI "v[82:83],v[82:83],v[6:7]" qT qH "v[92:93],v[64:65],v[20:21],v[92:93]" qR \
qH "v[94:95],v[64:65],v[22:23],v[94:95]" qR qI "v[88:89],v[88:89],v[6:7]" qT qI "v[90:91],v[90:91],v[6:7]" qT qH "v[100:101],v[64:65],v[28:29],v[100:101]" qR \
qH "v[102:103],v[64:65],v[30:31],v[102:103]" qR qI "v[96:97],v[96:97],v[6:7]" qT qI "v[98:99],v[98:99],v[6:7]" qT qH "v[108:109],v[68:69],v[12:13],v[108:109]" qR \
qH "v[110:111],v[68:69],v[14:15],v[110:111]" qR qI "v[104:105],v[104:105],v[6:7]" qS qI "v[106:107],v[106:107],v[6:7]" qS qI "v[80:81],v[80:81],v[84:85]\n" \
qI "v[82:83],v[82:83],v[86:87]\n" qI "v[88:89],v[88:89],v[92:93]\n" qI "v[90:91],v[90:91],v[94:95]\n" qI "v[96:97],v[96:97],v[100:101]\n" \
qI "v[98:99],v[98:99],v[102:103]\n" qI "v[104:105],v[104:105],v[108:109]\n" qI "v[106:107],v[106:107],v[110:111]\n" qJ "v160,v80,v81\n" \
qJ "v161,v82,v83\n" qJ "v162,v88,v89\n" qJ "v163,v90,v91\n" qJ "v168,v104,v105\n" \
qJ "v169,v106,v107\n" qJ "v164,v96,v97\n" qJ "v165,v98,v99\n" qG "v80,a208\n" \
qG "v81,a209\n" qG "v82,a210\n" qG "v83,a211\n" qG "v88,a216\n" \
qG "v89,a217\n" qG "v90,a218\n" qG "v91,a219\n" qG "v96,a240\n" \
qG "v97,a241\n" qG "v98,a242\n" qG "v99,a243\n" qG "v104,a248\n" \
qG "v105,a249\n" qG "v106,a250\n" qG "v107,a251\n" qH "v[80:81],v[68:69],v[16:17],v[80:81]" qR \
qH "v[82:83],v[68:69],v[18:19],v[82:83]" qR qH "v[88:89],v[68:69],v[24:25],v[88:89]" qR qH "v[90:91],v[68:69],v[26:27],v[90:91]" qR qH "v[96:97],v[72:73],v[8:9],v[96:97]" qR \
qH "v[98:99],v[72:73],v[10:11],v[98:99]" qR qH "v[104:105],v[72:73],v[16:17],v[104:105]" qR qH "v[106:107],v[72:73],v[18:19],v[106:107]" qR qG "v84,a212\n" \
qG "v85,a213\n" qG "v86,a214\n" qG "v87,a215\n" qG "v92,a220\n" \
qG "v93,a221\n" qG "v94,a222\n" qG "v95,a223\n" "v_mul_f32 v6,v75,v75\n" \
qG "v100,a244\n" qG "v101,a245\n" qG "v102,a246\n" qG "v103,a247\n" \
qG "v108,a252\n" qG "v109,a253\n" qG "v110,a254\n" qG "v111,a255\n" \
qH "v[84:85],v[68:69],v[20:21],v[84:85]" qR qH "v[86:87],v[68:69],v[22:23],v[86:87]" qR qI "v[80:81],v[80:81],v[6:7]" qS qI "v[82:83],v[82:83],v[6:7]" qS \
qH "v[92:93],v[68:69],v[28:29],v[92:93]" qR qH "v[94:95],v[68:69],v[30:31],v[94:95]" qR qI "v[88:89],v[88:89],v[6:7]" qS qI "v[90:91],v[90:91],v[6:7]" qS \
qH "v[100:101],v[72:73],v[12:13],v[100:101]" qR qH "v[102:103],v[72:73],v[14:15],v[102:103]" qR qI "v[96:97],v[96:97],v[6:7]" qT qI "v[98:99],v[98:99],v[6:7]" qT \
qH "v[108:109],v[72:73],v[20:21],v[108:109]" qR qH "v[110:111],v[72:73],v[22:23],v[110:111]" qR qI "v[104:105],v[104:105],v[6:7]" qT qI "v[106:107],v[106:107],v[6:7]" qT \
qI "v[80:81],v[80:81],v[84:85]\n" qI "v[82:83],v[82:83],v[86:87]\n" qI "v[88:89],v[88:89],v[92:93]\n" qI "v[90:91],v[90:91],v[94:95]\n" \
qI "v[96:97],v[96:97],v[100:101]\n" qI "v[98:99],v[98:99],v[102:103]\n" qI "v[104:105],v[104:105],v[108:109]\n" qI "v[106:107],v[106:107],v[110:111]\n" \
qJ "v170,v80,v81\n" qJ "v171,v82,v83\n" qJ "v166,v88,v89\n" qJ "v167,v90,v91\n" \
qJ "v176,v96,v97\n" qJ "v177,v98,v99\n" qJ "v178,v104,v105\n" qJ "v179,v106,v107\n" \
qH "v[80:81],v[72:73],v[24:25],v[192:193]" qR qH "v[82:83],v[72:73],v[26:27],v[194:195]" qR qH "v[88:89],v[76:77],v[8:9],v[216:217]" qR qH "v[90:91],v[76:77],v[10:11],v[218:219]" qR \
qH "v[96:97],v[76:77],v[16:17],v[224:225]" qR qH "v[98:99],v[76:77],v[18:19],v[226:227]" qR qH "v[104:105],v[76:77],v[24:25],v[232:233]" qR qH "v[106:107],v[76:77],v[26:27],v[234:235]" qR \
"v_mul_f32 v7,v79,v79\n" qH "v[84:85],v[72:73],v[28:29],v[196:197]" qR qH "v[86:87],v[72:73],v[30:31],v[198:199]" qR qI "v[80:81],v[80:81],v[6:7]" qT \
qI "v[82:83],v[82:83],v[6:7]" qT qH "v[92:93],v[76:77],v[12:13],v[220:221]" qR qH "v[94:95],v[76:77],v[14:15],v[222:223]" qR qI "v[88:89],v[88:89],v[6:7]" qS \
qI "v[90:91],v[90:91],v[6:7]" qS qH "v[100:101],v[76:77],v[20:21],v[228:229]" qR qH "v[102:103],v[76:77],v[22:23],v[230:231]" qR qI "v[96:97],v[96:97],v[6:7]" qS \
qI "v[98:99],v[98:99],v[6:7]" qS qH "v[108:109],v[76:77],v[28:29],v[236:237]" qR qH "v[110:111],v[76:77],v[30:31],v[238:239]" qR qI "v[104:105],v[104:105],v[6:7]" qS \
qI "v[106:107],v[106:107],v[6:7]" qS qI "v[80:81],v[80:81],v[84:85]\n" qI "v[82:83],v[82:83],v[86:87]\n" qI "v[88:89],v[88:89],v[92:93]\n" \
qI "v[90:91],v[90:91],v[94:95]\n" qI "v[96:97],v[96:97],v[100:101]\n" qI "v[98:99],v[98:99],v[102:103]\n" qI "v[104:105],v[104:105],v[108:109]\n" \
qI "v[106:107],v[106:107],v[110:111]\n" qJ "v184,v88,v89\n" qJ "v185,v90,v91\n" qJ "v186,v96,v97\n" \
qJ "v187,v98,v99\n" qJ "v180,v80,v81\n" qJ "v181,v82,v83\n" qJ "v182,v104,v105\n" \
qJ "v183,v106,v107\n" qK "v160,v162\n" qK "v161,v163\n" qK "v168,v170\n" \
qK "v169,v171\n" qK "v164,v166\n" qK "v165,v167\n" qK "v176,v178\n" \
qK "v177,v179\n" qK "v184,v186\n" qK "v185,v187\n" qK "v180,v182\n" \
qK "v181,v183\n" "v_and_b32 v83,0x1f0,v47\n" "v_lshrrev_b32 v86,9,v47\n" "v_bfe_u32 v87,v47,6,2\n" \
"v_xor_b32 v87,v87,v86\n" "v_lshlrev_b32 v87,4,v87\n" "v_lshl_add_u32 v40,v83,2,v87\n" "v_add_u32 v40,12288,v40\n" \
"v_xor_b32 v41,32,v40\n" "v_bfe_u32 v87,v47,6,2\n" "v_lshl_add_u32 v87,v86,2,v87\n" "v_lshlrev_b32 v87,6,v87\n" \
"v_bfe_u32 v42,v47,8,1\n" "v_lshl_add_u32 v87,v42,5,v87\n" "v_bfe_u32 v42,v47,4,2\n" "v_lshl_add_u32 v42,v42,3,v87\n" \
"v_add_u32 v42,61440,v42\n" "v_lshl_add_u32 v43,v86,15,v83\n" "v_add_u32 v43,s81,v43\n" "v_lshrrev_b32 v44,4,v83\n" \
"v_lshlrev_b32 v87,2,v86\n" "v_sub_u32 v44,v44,v87\n" "v_and_b32 v83,0xf0,v47\n" "v_bfe_u32 v87,v47,8,1\n" \
"v_lshl_or_b32 v87,v87,1,v86\n" "v_lshl_add_u32 v45,v87,15,v83\n" "v_add_u32 v45,s79,v45\n" "v_bfe_u32 v87,v47,8,1\n" \
"v_lshl_add_u32 v83,v87,8,v83\n" "v_lshl_add_u32 v46,v86,15,v83\n" "v_add_u32 v46,s80,v46\n" "v_mov_b32 v47,0x70000000\n" \
qO "vmcnt(0)\n" qQ "v2,a[0:3] offset:0\n" qQ "v2,a[4:7] offset:4096\n" qQ "v2,a[8:11] offset:8192\n" \
qQ "v2,a[12:15] offset:12288\n" qQ "v3,a[16:19] offset:0\n" qQ "v3,a[20:23] offset:4096\n" qQ "v3,a[24:27] offset:8192\n" \
qQ "v3,a[28:31] offset:12288\n" qO "lgkmcnt(0)\n" "s_barrier\n" "G1END_%=:\n"

#define G1_CLOB "v1", "v2", "v3", "v4", "v5", "v6", "v80", "v81", "v82", "v83", "v84", "v85", "v86", "v87", "v88", "v89", "v90", "v91", "v92", "v93", "v94", "v95", "v96", "v97", "v98", "v99", "v101", "v102", "v103", "v104", "v105", "v106", "v107", "v108", "v109", "v110", "v111", "v112", "v113", "v114", "v115", "v116", "v117", "v118", "v119", "v120", "v121", "v122", "v123", "v124", "v125", "v126", "v127", "v192", "v193", "v194", "v195", "v196", "v197", "v198", "v199", "v200", "v201", "v202", "v203", "v204", "v205", "v206", "v207", "v208", "v209", "v210", "v211", "v212", "v213", "v214", "v215", "v216", "v217", "v218", "v219", "v220", "v221", "v222", "v223", "v224", "v225", "v226", "v227", "v228", "v229", "v230", "v231", "v232", "v233", "v234", "v235", "v236", "v237", "v238", "v239", "v240", "v241", "v242", "v243", "v244", "v245", "v246", "v247", "v248", "v249", "v250", "v251", "v252", "v253", "v254", "v255", "a0", "a1", "a2", "a3", "a4", "a5", "a6", "a7", "a8", "a9", "a10", "a11", "a12", "a13", "a14", "a15", "a16", "a17", "a18", "a19", "a20", "a21", "a22", "a23", "a24", "a25", "a26", "a27", "a28", "a29", "a30", "a31", "a32", "a33", "a34", "a35", "a36", "a37", "a38", "a39", "a40", "a41", "a42", "a43", "a44", "a45", "a46", "a47", "a48", "a49", "a50", "a51", "a52", "a53", "a54", "a55", "a56", "a57", "a58", "a59", "a60", "a61", "a62", "a63", "a64", "a65", "a66", "a67", "a68", "a69", "a70", "a71", "a72", "a73", "a74", "a75", "a76", "a77", "a78", "a79", "a80", "a81", "a82", "a83", "a84", "a85", "a86", "a87", "a88", "a89", "a90", "a91", "a92", "a93", "a94", "a95", "a96", "a97", "a98", "a99", "a100", "a101", "a102", "a103", "a104", "a105", "a106", "a107", "a108", "a109", "a110", "a111", "a112", "a113", "a114", "a115", "a116", "a117", "a118", "a119", "a120", "a121", "a122", "a123", "a124", "a125", "a126", "a127", "a128", "a129", "a130", "a131", "a132", "a133", "a134", "a135", "a136", "a137", "a138", "a139", "a140", "a141", "a142", "a143", "a144", "a145", "a146", "a147", "a148", "a149", "a150", "a151", "a152", "a153", "a154", "a155", "a156", "a157", "a158", "a159", "a160", "a161", "a162", "a163", "a164", "a165", "a166", "a167", "a168", "a169", "a170", "a171", "a172", "a173", "a174", "a175", "a176", "a177", "a178", "a179", "a180", "a181", "a182", "a183", "a184", "a185", "a186", "a187", "a188", "a189", "a190", "a191", "a192", "a193", "a194", "a195", "a196", "a197", "a198", "a199", "a200", "a201", "a202", "a203", "a204", "a205", "a206", "a207", "a208", "a209", "a210", "a211", "a212", "a213", "a214", "a215", "a216", "a217", "a218", "a219", "a220", "a221", "a222", "a223", "a224", "a225", "a226", "a227", "a228", "a229", "a230", "a231", "a232", "a233", "a234", "a235", "a236", "a237", "a238", "a239", "a240", "a241", "a242", "a243", "a244", "a245", "a246", "a247", "a248", "a249", "a250", "a251", "a252", "a253", "a254", "a255", "s63", "s92", "s93", "s94", "s95", "scc", "vcc", "memory"

#define G3_PRO \
qN "m0,s62,0\n" "s_nop 0\n" qE "v43,s[44:47],0" qU qN "m0,s62,1024\n" \
"s_nop 0\n" qE "v44,s[44:47],s65" qU qN "m0,s62,2048\n" "s_nop 0\n" \
qE "v43,s[44:47],s66" qU qN "m0,s62,3072\n" "s_nop 0\n" qE "v44,s[44:47],s67" qU \
qN "s58,s58,1\n" "s_cmp_eq_u32 s58,s59\n" "s_cselect_b32 s50,s48,131072\n" "s_cselect_b32 s51,s49,0\n" \
qN "s44,s44,s50\n" "s_addc_u32 s45,s45,s51\n"

#define G3_ASM \
qN "m0,s61,0\n" "s_nop 0\n" qE "v42,s[40:43],s56" qV qE "v42,s[40:43],s56 offen offset:1024 sc1 lds\n" \
qN "m0,s61,2048\n" "s_nop 0\n" qE "v42,s[40:43],s57" qV qE "v42,s[40:43],s57 offen offset:1024 sc1 lds\n" \
qN "s56,s56,262144\n" qN "s57,s57,262144\n" qN "m0,s62,32768\n" "s_nop 0\n" \
qE "v43,s[44:47],0" qU qN "m0,s62,33792\n" "s_nop 0\n" qE "v44,s[44:47],s65" qU \
qN "m0,s62,34816\n" "s_nop 0\n" qE "v43,s[44:47],s66" qU qN "m0,s62,35840\n" \
"s_nop 0\n" qE "v44,s[44:47],s67" qU qN "s58,s58,1\n" "s_cmp_eq_u32 s58,s59\n" \
"s_cselect_b32 s50,s48,131072\n" "s_cselect_b32 s51,s49,0\n" qN "s44,s44,s50\n" "s_addc_u32 s45,s45,s51\n" \
qN "m0,s61,32768\n" "s_nop 0\n" qE "v42,s[40:43],s56" qV qE "v42,s[40:43],s56 offen offset:1024 sc1 lds\n" \
qN "m0,s61,34816\n" "s_nop 0\n" qE "v42,s[40:43],s57" qV qE "v42,s[40:43],s57 offen offset:1024 sc1 lds\n" \
qN "s56,s56,262144\n" qN "s57,s57,262144\n" qN "m0,s62,65536\n" "s_nop 0\n" \
qE "v43,s[44:47],0" qU qN "m0,s62,66560\n" "s_nop 0\n" qE "v44,s[44:47],s65" qU \
qN "m0,s62,67584\n" "s_nop 0\n" qE "v43,s[44:47],s66" qU qN "m0,s62,68608\n" \
"s_nop 0\n" qE "v44,s[44:47],s67" qU qN "s58,s58,1\n" "s_cmp_eq_u32 s58,s59\n" \
"s_cselect_b32 s50,s48,131072\n" "s_cselect_b32 s51,s49,0\n" qN "s44,s44,s50\n" "s_addc_u32 s45,s45,s51\n" \
qN "m0,s61,65536\n" "s_nop 0\n" qE "v42,s[40:43],s56" qV qE "v42,s[40:43],s56 offen offset:1024 sc1 lds\n" \
qN "m0,s61,67584\n" "s_nop 0\n" qE "v42,s[40:43],s57" qV qE "v42,s[40:43],s57 offen offset:1024 sc1 lds\n" \
qN "s56,s56,262144\n" qN "s57,s57,262144\n" qN "m0,s62,98304\n" "s_nop 0\n" \
qE "v43,s[44:47],0" qU qN "m0,s62,99328\n" "s_nop 0\n" qE "v44,s[44:47],s65" qU \
qN "m0,s62,100352\n" "s_nop 0\n" qE "v43,s[44:47],s66" qU qN "m0,s62,101376\n" \
"s_nop 0\n" qE "v44,s[44:47],s67" qU qN "s58,s58,1\n" "s_cmp_eq_u32 s58,s59\n" \
"s_cselect_b32 s50,s48,131072\n" "s_cselect_b32 s51,s49,0\n" qN "s44,s44,s50\n" "s_addc_u32 s45,s45,s51\n" \
qO "vmcnt(20)\n" "s_barrier\n" qC "v[64:65],v32 offset:0\n" qC "v[66:67],v32 offset:512\n" \
qC "v[68:69],v33 offset:0\n" qC "v[70:71],v33 offset:512\n" qC "v[72:73],v34 offset:0\n" qC "v[74:75],v34 offset:512\n" \
qC "v[76:77],v35 offset:0\n" qC "v[78:79],v35 offset:512\n" qD "v[80:83],v40 offset:0\n" qD "v[84:87],v40 offset:256\n" \
qD "v[88:91],v40 offset:512\n" qD "v[92:95],v40 offset:768\n" "s_mov_b32 s63,7\n" qO "lgkmcnt(3)\n" \
qA "a[0:3],v[64:67],v[80:83],0\n" qC "v[96:97],v32 offset:4096\n" qN "m0,s61,98304\n" qA "a[4:7],v[68:71],v[80:83],0\n" \
qC "v[98:99],v32 offset:4608\n" qE "v42,s[40:43],s56" qV qA "a[8:11],v[72:75],v[80:83],0\n" qC "v[100:101],v33 offset:4096\n" \
qA "a[12:15],v[76:79],v[80:83],0\n" qC "v[102:103],v33 offset:4608\n" qO "lgkmcnt(6)\n" qA "a[16:19],v[64:67],v[84:87],0\n" \
qC "v[104:105],v34 offset:4096\n" qE "v42,s[40:43],s56 offen offset:1024 sc1 lds\n" qA "a[20:23],v[68:71],v[84:87],0\n" qC "v[106:107],v34 offset:4608\n" \
qA "a[24:27],v[72:75],v[84:87],0\n" qC "v[108:109],v35 offset:4096\n" qN "m0,s61,100352\n" qA "a[28:31],v[76:79],v[84:87],0\n" \
qC "v[110:111],v35 offset:4608\n" qE "v42,s[40:43],s57" qV qO "lgkmcnt(9)\n" qA "a[32:35],v[64:67],v[88:91],0\n" \
qD "v[112:115],v40 offset:8192\n" qA "a[36:39],v[68:71],v[88:91],0\n" qD "v[116:119],v40 offset:8448\n" qA "a[40:43],v[72:75],v[88:91],0\n" \
qD "v[120:123],v40 offset:8704\n" qE "v42,s[40:43],s57 offen offset:1024 sc1 lds\n" qA "a[44:47],v[76:79],v[88:91],0\n" qD "v[124:127],v40 offset:8960\n" \
qO "lgkmcnt(12)\n" qA "a[48:51],v[64:67],v[92:95],0\n" qN "s56,s56,262144\n" qN "s57,s57,262144\n" \
qA "a[52:55],v[68:71],v[92:95],0\n" qA "a[56:59],v[72:75],v[92:95],0\n" qA "a[60:63],v[76:79],v[92:95],0\n" qO "lgkmcnt(0)\n" \
qA "a[0:3],v[96:99],v[112:115],a[0:3]\n" qO "vmcnt(16)\n" "s_barrier\n" qC "v[64:65],v32 offset:32768\n" \
qN "m0,s62,0\n" qA "a[4:7],v[100:103],v[112:115],a[4:7]\n" qC "v[66:67],v32 offset:33280\n" qE "v43,s[44:47],0" qU \
qA "a[8:11],v[104:107],v[112:115],a[8:11]\n" qC "v[68:69],v33 offset:32768\n" qA "a[12:15],v[108:111],v[112:115],a[12:15]\n" qC "v[70:71],v33 offset:33280\n" \
qN "m0,s62,1024\n" qA "a[16:19],v[96:99],v[116:119],a[16:19]\n" qC "v[72:73],v34 offset:32768\n" qE "v44,s[44:47],s65" qU \
qA "a[20:23],v[100:103],v[116:119],a[20:23]\n" qC "v[74:75],v34 offset:33280\n" qA "a[24:27],v[104:107],v[116:119],a[24:27]\n" qC "v[76:77],v35 offset:32768\n" \
qN "m0,s62,2048\n" qA "a[28:31],v[108:111],v[116:119],a[28:31]\n" qC "v[78:79],v35 offset:33280\n" qE "v43,s[44:47],s66" qU \
qA "a[32:35],v[96:99],v[120:123],a[32:35]\n" qD "v[80:83],v40 offset:32768\n" qA "a[36:39],v[100:103],v[120:123],a[36:39]\n" qD "v[84:87],v40 offset:33024\n" \
qN "m0,s62,3072\n" qA "a[40:43],v[104:107],v[120:123],a[40:43]\n" qD "v[88:91],v40 offset:33280\n" qE "v44,s[44:47],s67" qU \
qA "a[44:47],v[108:111],v[120:123],a[44:47]\n" qD "v[92:95],v40 offset:33536\n" qA "a[48:51],v[96:99],v[124:127],a[48:51]\n" qN "s58,s58,1\n" \
"s_cmp_eq_u32 s58,s59\n" qA "a[52:55],v[100:103],v[124:127],a[52:55]\n" "s_cselect_b32 s50,s48,131072\n" "s_cselect_b32 s51,s49,0\n" \
qA "a[56:59],v[104:107],v[124:127],a[56:59]\n" qN "s44,s44,s50\n" "s_addc_u32 s45,s45,s51\n" qA "a[60:63],v[108:111],v[124:127],a[60:63]\n" \
qO "lgkmcnt(3)\n" qA "a[0:3],v[64:67],v[80:83],a[0:3]\n" qC "v[96:97],v32 offset:36864\n" qN "m0,s61,0\n" \
qA "a[4:7],v[68:71],v[80:83],a[4:7]\n" qC "v[98:99],v32 offset:37376\n" qE "v42,s[40:43],s56" qV qA "a[8:11],v[72:75],v[80:83],a[8:11]\n" \
qC "v[100:101],v33 offset:36864\n" qA "a[12:15],v[76:79],v[80:83],a[12:15]\n" qC "v[102:103],v33 offset:37376\n" qO "lgkmcnt(6)\n" \
qA "a[16:19],v[64:67],v[84:87],a[16:19]\n" qC "v[104:105],v34 offset:36864\n" qE "v42,s[40:43],s56 offen offset:1024 sc1 lds\n" qA "a[20:23],v[68:71],v[84:87],a[20:23]\n" \
qC "v[106:107],v34 offset:37376\n" qA "a[24:27],v[72:75],v[84:87],a[24:27]\n" qC "v[108:109],v35 offset:36864\n" qN "m0,s61,2048\n" \
qA "a[28:31],v[76:79],v[84:87],a[28:31]\n" qC "v[110:111],v35 offset:37376\n" qE "v42,s[40:43],s57" qV qO "lgkmcnt(9)\n" \
qA "a[32:35],v[64:67],v[88:91],a[32:35]\n" qD "v[112:115],v40 offset:40960\n" qA "a[36:39],v[68:71],v[88:91],a[36:39]\n" qD "v[116:119],v40 offset:41216\n" \
qA "a[40:43],v[72:75],v[88:91],a[40:43]\n" qD "v[120:123],v40 offset:41472\n" qE "v42,s[40:43],s57 offen offset:1024 sc1 lds\n" qA "a[44:47],v[76:79],v[88:91],a[44:47]\n" \
qD "v[124:127],v40 offset:41728\n" qO "lgkmcnt(12)\n" qA "a[48:51],v[64:67],v[92:95],a[48:51]\n" qN "s56,s56,262144\n" \
qN "s57,s57,262144\n" qA "a[52:55],v[68:71],v[92:95],a[52:55]\n" qA "a[56:59],v[72:75],v[92:95],a[56:59]\n" qA "a[60:63],v[76:79],v[92:95],a[60:63]\n" \
qO "lgkmcnt(0)\n" qA "a[0:3],v[96:99],v[112:115],a[0:3]\n" qO "vmcnt(16)\n" "s_barrier\n" \
qC "v[64:65],v36 offset:0\n" qN "m0,s62,32768\n" qA "a[4:7],v[100:103],v[112:115],a[4:7]\n" qC "v[66:67],v36 offset:512\n" \
qE "v43,s[44:47],0" qU qA "a[8:11],v[104:107],v[112:115],a[8:11]\n" qC "v[68:69],v37 offset:0\n" qA "a[12:15],v[108:111],v[112:115],a[12:15]\n" \
qC "v[70:71],v37 offset:512\n" qN "m0,s62,33792\n" qA "a[16:19],v[96:99],v[116:119],a[16:19]\n" qC "v[72:73],v38 offset:0\n" \
qE "v44,s[44:47],s65" qU qA "a[20:23],v[100:103],v[116:119],a[20:23]\n" qC "v[74:75],v38 offset:512\n" qA "a[24:27],v[104:107],v[116:119],a[24:27]\n" \
qC "v[76:77],v39 offset:0\n" qN "m0,s62,34816\n" qA "a[28:31],v[108:111],v[116:119],a[28:31]\n" qC "v[78:79],v39 offset:512\n" \
qE "v43,s[44:47],s66" qU qA "a[32:35],v[96:99],v[120:123],a[32:35]\n" qD "v[80:83],v41 offset:0\n" qA "a[36:39],v[100:103],v[120:123],a[36:39]\n" \
qD "v[84:87],v41 offset:256\n" qN "m0,s62,35840\n" qA "a[40:43],v[104:107],v[120:123],a[40:43]\n" qD "v[88:91],v41 offset:512\n" \
qE "v44,s[44:47],s67" qU qA "a[44:47],v[108:111],v[120:123],a[44:47]\n" qD "v[92:95],v41 offset:768\n" qA "a[48:51],v[96:99],v[124:127],a[48:51]\n" \
qN "s58,s58,1\n" "s_cmp_eq_u32 s58,s59\n" qA "a[52:55],v[100:103],v[124:127],a[52:55]\n" "s_cselect_b32 s50,s48,131072\n" \
"s_cselect_b32 s51,s49,0\n" qA "a[56:59],v[104:107],v[124:127],a[56:59]\n" qN "s44,s44,s50\n" "s_addc_u32 s45,s45,s51\n" \
qA "a[60:63],v[108:111],v[124:127],a[60:63]\n" qO "lgkmcnt(3)\n" qA "a[0:3],v[64:67],v[80:83],a[0:3]\n" qC "v[96:97],v36 offset:4096\n" \
qN "m0,s61,32768\n" qA "a[4:7],v[68:71],v[80:83],a[4:7]\n" qC "v[98:99],v36 offset:4608\n" qE "v42,s[40:43],s56" qV \
qA "a[8:11],v[72:75],v[80:83],a[8:11]\n" qC "v[100:101],v37 offset:4096\n" qA "a[12:15],v[76:79],v[80:83],a[12:15]\n" qC "v[102:103],v37 offset:4608\n" \
qO "lgkmcnt(6)\n" qA "a[16:19],v[64:67],v[84:87],a[16:19]\n" qC "v[104:105],v38 offset:4096\n" qE "v42,s[40:43],s56 offen offset:1024 sc1 lds\n" \
qA "a[20:23],v[68:71],v[84:87],a[20:23]\n" qC "v[106:107],v38 offset:4608\n" qA "a[24:27],v[72:75],v[84:87],a[24:27]\n" qC "v[108:109],v39 offset:4096\n" \
qN "m0,s61,34816\n" qA "a[28:31],v[76:79],v[84:87],a[28:31]\n" qC "v[110:111],v39 offset:4608\n" qE "v42,s[40:43],s57" qV \
qO "lgkmcnt(9)\n" qA "a[32:35],v[64:67],v[88:91],a[32:35]\n" qD "v[112:115],v41 offset:8192\n" qA "a[36:39],v[68:71],v[88:91],a[36:39]\n" \
qD "v[116:119],v41 offset:8448\n" qA "a[40:43],v[72:75],v[88:91],a[40:43]\n" qD "v[120:123],v41 offset:8704\n" qE "v42,s[40:43],s57 offen offset:1024 sc1 lds\n" \
qA "a[44:47],v[76:79],v[88:91],a[44:47]\n" qD "v[124:127],v41 offset:8960\n" qO "lgkmcnt(12)\n" qA "a[48:51],v[64:67],v[92:95],a[48:51]\n" \
qN "s56,s56,262144\n" qN "s57,s57,262144\n" qA "a[52:55],v[68:71],v[92:95],a[52:55]\n" qA "a[56:59],v[72:75],v[92:95],a[56:59]\n" \
qA "a[60:63],v[76:79],v[92:95],a[60:63]\n" qO "lgkmcnt(0)\n" qA "a[0:3],v[96:99],v[112:115],a[0:3]\n" qO "vmcnt(16)\n" \
"s_barrier\n" qC "v[64:65],v36 offset:32768\n" qN "m0,s62,65536\n" qA "a[4:7],v[100:103],v[112:115],a[4:7]\n" \
qC "v[66:67],v36 offset:33280\n" qE "v43,s[44:47],0" qU qA "a[8:11],v[104:107],v[112:115],a[8:11]\n" qC "v[68:69],v37 offset:32768\n" \
qA "a[12:15],v[108:111],v[112:115],a[12:15]\n" qC "v[70:71],v37 offset:33280\n" qN "m0,s62,66560\n" qA "a[16:19],v[96:99],v[116:119],a[16:19]\n" \
qC "v[72:73],v38 offset:32768\n" qE "v44,s[44:47],s65" qU qA "a[20:23],v[100:103],v[116:119],a[20:23]\n" qC "v[74:75],v38 offset:33280\n" \
qA "a[24:27],v[104:107],v[116:119],a[24:27]\n" qC "v[76:77],v39 offset:32768\n" qN "m0,s62,67584\n" qA "a[28:31],v[108:111],v[116:119],a[28:31]\n" \
qC "v[78:79],v39 offset:33280\n" qE "v43,s[44:47],s66" qU qA "a[32:35],v[96:99],v[120:123],a[32:35]\n" qD "v[80:83],v41 offset:32768\n" \
qA "a[36:39],v[100:103],v[120:123],a[36:39]\n" qD "v[84:87],v41 offset:33024\n" qN "m0,s62,68608\n" qA "a[40:43],v[104:107],v[120:123],a[40:43]\n" \
qD "v[88:91],v41 offset:33280\n" qE "v44,s[44:47],s67" qU qA "a[44:47],v[108:111],v[120:123],a[44:47]\n" qD "v[92:95],v41 offset:33536\n" \
qA "a[48:51],v[96:99],v[124:127],a[48:51]\n" qN "s58,s58,1\n" "s_cmp_eq_u32 s58,s59\n" qA "a[52:55],v[100:103],v[124:127],a[52:55]\n" \
"s_cselect_b32 s50,s48,131072\n" "s_cselect_b32 s51,s49,0\n" qA "a[56:59],v[104:107],v[124:127],a[56:59]\n" qN "s44,s44,s50\n" \
"s_addc_u32 s45,s45,s51\n" qA "a[60:63],v[108:111],v[124:127],a[60:63]\n" qO "lgkmcnt(3)\n" qA "a[0:3],v[64:67],v[80:83],a[0:3]\n" \
qC "v[96:97],v36 offset:36864\n" qN "m0,s61,65536\n" qA "a[4:7],v[68:71],v[80:83],a[4:7]\n" qC "v[98:99],v36 offset:37376\n" \
qE "v42,s[40:43],s56" qV qA "a[8:11],v[72:75],v[80:83],a[8:11]\n" qC "v[100:101],v37 offset:36864\n" qA "a[12:15],v[76:79],v[80:83],a[12:15]\n" \
qC "v[102:103],v37 offset:37376\n" qO "lgkmcnt(6)\n" qA "a[16:19],v[64:67],v[84:87],a[16:19]\n" qC "v[104:105],v38 offset:36864\n" \
qE "v42,s[40:43],s56 offen offset:1024 sc1 lds\n" qA "a[20:23],v[68:71],v[84:87],a[20:23]\n" qC "v[106:107],v38 offset:37376\n" qA "a[24:27],v[72:75],v[84:87],a[24:27]\n" \
qC "v[108:109],v39 offset:36864\n" qN "m0,s61,67584\n" qA "a[28:31],v[76:79],v[84:87],a[28:31]\n" qC "v[110:111],v39 offset:37376\n" \
qE "v42,s[40:43],s57" qV qO "lgkmcnt(9)\n" qA "a[32:35],v[64:67],v[88:91],a[32:35]\n" qD "v[112:115],v41 offset:40960\n" \
qA "a[36:39],v[68:71],v[88:91],a[36:39]\n" qD "v[116:119],v41 offset:41216\n" qA "a[40:43],v[72:75],v[88:91],a[40:43]\n" qD "v[120:123],v41 offset:41472\n" \
qE "v42,s[40:43],s57 offen offset:1024 sc1 lds\n" qA "a[44:47],v[76:79],v[88:91],a[44:47]\n" qD "v[124:127],v41 offset:41728\n" qO "lgkmcnt(12)\n" \
qA "a[48:51],v[64:67],v[92:95],a[48:51]\n" qN "s56,s56,262144\n" qN "s57,s57,262144\n" qA "a[52:55],v[68:71],v[92:95],a[52:55]\n" \
qA "a[56:59],v[72:75],v[92:95],a[56:59]\n" qA "a[60:63],v[76:79],v[92:95],a[60:63]\n" qO "lgkmcnt(0)\n" qA "a[0:3],v[96:99],v[112:115],a[0:3]\n" \
qO "vmcnt(16)\n" "s_barrier\n" qC "v[64:65],v32 offset:0\n" qN "m0,s62,98304\n" \
qA "a[4:7],v[100:103],v[112:115],a[4:7]\n" qC "v[66:67],v32 offset:512\n" qE "v43,s[44:47],0" qU qA "a[8:11],v[104:107],v[112:115],a[8:11]\n" \
qC "v[68:69],v33 offset:0\n" qA "a[12:15],v[108:111],v[112:115],a[12:15]\n" qC "v[70:71],v33 offset:512\n" qN "m0,s62,99328\n" \
qA "a[16:19],v[96:99],v[116:119],a[16:19]\n" qC "v[72:73],v34 offset:0\n" qE "v44,s[44:47],s65" qU qA "a[20:23],v[100:103],v[116:119],a[20:23]\n" \
qC "v[74:75],v34 offset:512\n" qA "a[24:27],v[104:107],v[116:119],a[24:27]\n" qC "v[76:77],v35 offset:0\n" qN "m0,s62,100352\n" \
qA "a[28:31],v[108:111],v[116:119],a[28:31]\n" qC "v[78:79],v35 offset:512\n" qE "v43,s[44:47],s66" qU qA "a[32:35],v[96:99],v[120:123],a[32:35]\n" \
qD "v[80:83],v40 offset:0\n" qA "a[36:39],v[100:103],v[120:123],a[36:39]\n" qD "v[84:87],v40 offset:256\n" qN "m0,s62,101376\n" \
qA "a[40:43],v[104:107],v[120:123],a[40:43]\n" qD "v[88:91],v40 offset:512\n" qE "v44,s[44:47],s67" qU qA "a[44:47],v[108:111],v[120:123],a[44:47]\n" \
qD "v[92:95],v40 offset:768\n" qA "a[48:51],v[96:99],v[124:127],a[48:51]\n" qN "s58,s58,1\n" "s_cmp_eq_u32 s58,s59\n" \
qA "a[52:55],v[100:103],v[124:127],a[52:55]\n" "s_cselect_b32 s50,s48,131072\n" "s_cselect_b32 s51,s49,0\n" qA "a[56:59],v[104:107],v[124:127],a[56:59]\n" \
qN "s44,s44,s50\n" "s_addc_u32 s45,s45,s51\n" qA "a[60:63],v[108:111],v[124:127],a[60:63]\n" "G3L_%=:\n" \
qO "lgkmcnt(3)\n" qA "a[0:3],v[64:67],v[80:83],a[0:3]\n" qC "v[96:97],v32 offset:4096\n" qN "m0,s61,98304\n" \
qA "a[4:7],v[68:71],v[80:83],a[4:7]\n" qC "v[98:99],v32 offset:4608\n" qE "v42,s[40:43],s56" qV qA "a[8:11],v[72:75],v[80:83],a[8:11]\n" \
qC "v[100:101],v33 offset:4096\n" qA "a[12:15],v[76:79],v[80:83],a[12:15]\n" qC "v[102:103],v33 offset:4608\n" qO "lgkmcnt(6)\n" \
qA "a[16:19],v[64:67],v[84:87],a[16:19]\n" qC "v[104:105],v34 offset:4096\n" qE "v42,s[40:43],s56 offen offset:1024 sc1 lds\n" qA "a[20:23],v[68:71],v[84:87],a[20:23]\n" \
qC "v[106:107],v34 offset:4608\n" qA "a[24:27],v[72:75],v[84:87],a[24:27]\n" qC "v[108:109],v35 offset:4096\n" qN "m0,s61,100352\n" \
qA "a[28:31],v[76:79],v[84:87],a[28:31]\n" qC "v[110:111],v35 offset:4608\n" qE "v42,s[40:43],s57" qV qO "lgkmcnt(9)\n" \
qA "a[32:35],v[64:67],v[88:91],a[32:35]\n" qD "v[112:115],v40 offset:8192\n" qA "a[36:39],v[68:71],v[88:91],a[36:39]\n" qD "v[116:119],v40 offset:8448\n" \
qA "a[40:43],v[72:75],v[88:91],a[40:43]\n" qD "v[120:123],v40 offset:8704\n" qE "v42,s[40:43],s57 offen offset:1024 sc1 lds\n" qA "a[44:47],v[76:79],v[88:91],a[44:47]\n" \
qD "v[124:127],v40 offset:8960\n" qO "lgkmcnt(12)\n" qA "a[48:51],v[64:67],v[92:95],a[48:51]\n" qN "s56,s56,262144\n" \
qN "s57,s57,262144\n" qA "a[52:55],v[68:71],v[92:95],a[52:55]\n" qA "a[56:59],v[72:75],v[92:95],a[56:59]\n" qA "a[60:63],v[76:79],v[92:95],a[60:63]\n" \
qO "lgkmcnt(0)\n" qA "a[0:3],v[96:99],v[112:115],a[0:3]\n" qO "vmcnt(16)\n" "s_barrier\n" \
qC "v[64:65],v32 offset:32768\n" qN "m0,s62,0\n" qA "a[4:7],v[100:103],v[112:115],a[4:7]\n" qC "v[66:67],v32 offset:33280\n" \
qE "v43,s[44:47],0" qU qA "a[8:11],v[104:107],v[112:115],a[8:11]\n" qC "v[68:69],v33 offset:32768\n" qA "a[12:15],v[108:111],v[112:115],a[12:15]\n" \
qC "v[70:71],v33 offset:33280\n" qN "m0,s62,1024\n" qA "a[16:19],v[96:99],v[116:119],a[16:19]\n" qC "v[72:73],v34 offset:32768\n" \
qE "v44,s[44:47],s65" qU qA "a[20:23],v[100:103],v[116:119],a[20:23]\n" qC "v[74:75],v34 offset:33280\n" qA "a[24:27],v[104:107],v[116:119],a[24:27]\n" \
qC "v[76:77],v35 offset:32768\n" qN "m0,s62,2048\n" qA "a[28:31],v[108:111],v[116:119],a[28:31]\n" qC "v[78:79],v35 offset:33280\n" \
qE "v43,s[44:47],s66" qU qA "a[32:35],v[96:99],v[120:123],a[32:35]\n" qD "v[80:83],v40 offset:32768\n" qA "a[36:39],v[100:103],v[120:123],a[36:39]\n" \
qD "v[84:87],v40 offset:33024\n" qN "m0,s62,3072\n" qA "a[40:43],v[104:107],v[120:123],a[40:43]\n" qD "v[88:91],v40 offset:33280\n" \
qE "v44,s[44:47],s67" qU qA "a[44:47],v[108:111],v[120:123],a[44:47]\n" qD "v[92:95],v40 offset:33536\n" qA "a[48:51],v[96:99],v[124:127],a[48:51]\n" \
qN "s58,s58,1\n" "s_cmp_eq_u32 s58,s59\n" qA "a[52:55],v[100:103],v[124:127],a[52:55]\n" "s_cselect_b32 s50,s48,131072\n" \
"s_cselect_b32 s51,s49,0\n" qA "a[56:59],v[104:107],v[124:127],a[56:59]\n" qN "s44,s44,s50\n" "s_addc_u32 s45,s45,s51\n" \
qA "a[60:63],v[108:111],v[124:127],a[60:63]\n" qO "lgkmcnt(3)\n" qA "a[0:3],v[64:67],v[80:83],a[0:3]\n" qC "v[96:97],v32 offset:36864\n" \
qN "m0,s61,0\n" qA "a[4:7],v[68:71],v[80:83],a[4:7]\n" qC "v[98:99],v32 offset:37376\n" qE "v42,s[40:43],s56" qV \
qA "a[8:11],v[72:75],v[80:83],a[8:11]\n" qC "v[100:101],v33 offset:36864\n" qA "a[12:15],v[76:79],v[80:83],a[12:15]\n" qC "v[102:103],v33 offset:37376\n" \
qO "lgkmcnt(6)\n" qA "a[16:19],v[64:67],v[84:87],a[16:19]\n" qC "v[104:105],v34 offset:36864\n" qE "v42,s[40:43],s56 offen offset:1024 sc1 lds\n" \
qA "a[20:23],v[68:71],v[84:87],a[20:23]\n" qC "v[106:107],v34 offset:37376\n" qA "a[24:27],v[72:75],v[84:87],a[24:27]\n" qC "v[108:109],v35 offset:36864\n" \
qN "m0,s61,2048\n" qA "a[28:31],v[76:79],v[84:87],a[28:31]\n" qC "v[110:111],v35 offset:37376\n" qE "v42,s[40:43],s57" qV \
qO "lgkmcnt(9)\n" qA "a[32:35],v[64:67],v[88:91],a[32:35]\n" qD "v[112:115],v40 offset:40960\n" qA "a[36:39],v[68:71],v[88:91],a[36:39]\n" \
qD "v[116:119],v40 offset:41216\n" qA "a[40:43],v[72:75],v[88:91],a[40:43]\n" qD "v[120:123],v40 offset:41472\n" qE "v42,s[40:43],s57 offen offset:1024 sc1 lds\n" \
qA "a[44:47],v[76:79],v[88:91],a[44:47]\n" qD "v[124:127],v40 offset:41728\n" qO "lgkmcnt(12)\n" qA "a[48:51],v[64:67],v[92:95],a[48:51]\n" \
qN "s56,s56,262144\n" qN "s57,s57,262144\n" qA "a[52:55],v[68:71],v[92:95],a[52:55]\n" qA "a[56:59],v[72:75],v[92:95],a[56:59]\n" \
qA "a[60:63],v[76:79],v[92:95],a[60:63]\n" qO "lgkmcnt(0)\n" qA "a[0:3],v[96:99],v[112:115],a[0:3]\n" qO "vmcnt(16)\n" \
"s_barrier\n" qC "v[64:65],v36 offset:0\n" qN "m0,s62,32768\n" qA "a[4:7],v[100:103],v[112:115],a[4:7]\n" \
qC "v[66:67],v36 offset:512\n" qE "v43,s[44:47],0" qU qA "a[8:11],v[104:107],v[112:115],a[8:11]\n" qC "v[68:69],v37 offset:0\n" \
qA "a[12:15],v[108:111],v[112:115],a[12:15]\n" qC "v[70:71],v37 offset:512\n" qN "m0,s62,33792\n" qA "a[16:19],v[96:99],v[116:119],a[16:19]\n" \
qC "v[72:73],v38 offset:0\n" qE "v44,s[44:47],s65" qU qA "a[20:23],v[100:103],v[116:119],a[20:23]\n" qC "v[74:75],v38 offset:512\n" \
qA "a[24:27],v[104:107],v[116:119],a[24:27]\n" qC "v[76:77],v39 offset:0\n" qN "m0,s62,34816\n" qA "a[28:31],v[108:111],v[116:119],a[28:31]\n" \
qC "v[78:79],v39 offset:512\n" qE "v43,s[44:47],s66" qU qA "a[32:35],v[96:99],v[120:123],a[32:35]\n" qD "v[80:83],v41 offset:0\n" \
qA "a[36:39],v[100:103],v[120:123],a[36:39]\n" qD "v[84:87],v41 offset:256\n" qN "m0,s62,35840\n" qA "a[40:43],v[104:107],v[120:123],a[40:43]\n" \
qD "v[88:91],v41 offset:512\n" qE "v44,s[44:47],s67" qU qA "a[44:47],v[108:111],v[120:123],a[44:47]\n" qD "v[92:95],v41 offset:768\n" \
qA "a[48:51],v[96:99],v[124:127],a[48:51]\n" qN "s58,s58,1\n" "s_cmp_eq_u32 s58,s59\n" qA "a[52:55],v[100:103],v[124:127],a[52:55]\n" \
"s_cselect_b32 s50,s48,131072\n" "s_cselect_b32 s51,s49,0\n" qA "a[56:59],v[104:107],v[124:127],a[56:59]\n" qN "s44,s44,s50\n" \
"s_addc_u32 s45,s45,s51\n" qA "a[60:63],v[108:111],v[124:127],a[60:63]\n" qO "lgkmcnt(3)\n" qA "a[0:3],v[64:67],v[80:83],a[0:3]\n" \
qC "v[96:97],v36 offset:4096\n" qN "m0,s61,32768\n" qA "a[4:7],v[68:71],v[80:83],a[4:7]\n" qC "v[98:99],v36 offset:4608\n" \
qE "v42,s[40:43],s56" qV qA "a[8:11],v[72:75],v[80:83],a[8:11]\n" qC "v[100:101],v37 offset:4096\n" qA "a[12:15],v[76:79],v[80:83],a[12:15]\n" \
qC "v[102:103],v37 offset:4608\n" qO "lgkmcnt(6)\n" qA "a[16:19],v[64:67],v[84:87],a[16:19]\n" qC "v[104:105],v38 offset:4096\n" \
qE "v42,s[40:43],s56 offen offset:1024 sc1 lds\n" qA "a[20:23],v[68:71],v[84:87],a[20:23]\n" qC "v[106:107],v38 offset:4608\n" qA "a[24:27],v[72:75],v[84:87],a[24:27]\n" \
qC "v[108:109],v39 offset:4096\n" qN "m0,s61,34816\n" qA "a[28:31],v[76:79],v[84:87],a[28:31]\n" qC "v[110:111],v39 offset:4608\n" \
qE "v42,s[40:43],s57" qV qO "lgkmcnt(9)\n" qA "a[32:35],v[64:67],v[88:91],a[32:35]\n" qD "v[112:115],v41 offset:8192\n" \
qA "a[36:39],v[68:71],v[88:91],a[36:39]\n" qD "v[116:119],v41 offset:8448\n" qA "a[40:43],v[72:75],v[88:91],a[40:43]\n" qD "v[120:123],v41 offset:8704\n" \
qE "v42,s[40:43],s57 offen offset:1024 sc1 lds\n" qA "a[44:47],v[76:79],v[88:91],a[44:47]\n" qD "v[124:127],v41 offset:8960\n" qO "lgkmcnt(12)\n" \
qA "a[48:51],v[64:67],v[92:95],a[48:51]\n" qN "s56,s56,262144\n" qN "s57,s57,262144\n" qA "a[52:55],v[68:71],v[92:95],a[52:55]\n" \
qA "a[56:59],v[72:75],v[92:95],a[56:59]\n" qA "a[60:63],v[76:79],v[92:95],a[60:63]\n" qO "lgkmcnt(0)\n" qA "a[0:3],v[96:99],v[112:115],a[0:3]\n" \
qO "vmcnt(16)\n" "s_barrier\n" qC "v[64:65],v36 offset:32768\n" qN "m0,s62,65536\n" \
qA "a[4:7],v[100:103],v[112:115],a[4:7]\n" qC "v[66:67],v36 offset:33280\n" qE "v43,s[44:47],0" qU qA "a[8:11],v[104:107],v[112:115],a[8:11]\n" \
qC "v[68:69],v37 offset:32768\n" qA "a[12:15],v[108:111],v[112:115],a[12:15]\n" qC "v[70:71],v37 offset:33280\n" qN "m0,s62,66560\n" \
qA "a[16:19],v[96:99],v[116:119],a[16:19]\n" qC "v[72:73],v38 offset:32768\n" qE "v44,s[44:47],s65" qU qA "a[20:23],v[100:103],v[116:119],a[20:23]\n" \
qC "v[74:75],v38 offset:33280\n" qA "a[24:27],v[104:107],v[116:119],a[24:27]\n" qC "v[76:77],v39 offset:32768\n" qN "m0,s62,67584\n" \
qA "a[28:31],v[108:111],v[116:119],a[28:31]\n" qC "v[78:79],v39 offset:33280\n" qE "v43,s[44:47],s66" qU qA "a[32:35],v[96:99],v[120:123],a[32:35]\n" \
qD "v[80:83],v41 offset:32768\n" qA "a[36:39],v[100:103],v[120:123],a[36:39]\n" qD "v[84:87],v41 offset:33024\n" qN "m0,s62,68608\n" \
qA "a[40:43],v[104:107],v[120:123],a[40:43]\n" qD "v[88:91],v41 offset:33280\n" qE "v44,s[44:47],s67" qU qA "a[44:47],v[108:111],v[120:123],a[44:47]\n" \
qD "v[92:95],v41 offset:33536\n" qA "a[48:51],v[96:99],v[124:127],a[48:51]\n" qN "s58,s58,1\n" "s_cmp_eq_u32 s58,s59\n" \
qA "a[52:55],v[100:103],v[124:127],a[52:55]\n" "s_cselect_b32 s50,s48,131072\n" "s_cselect_b32 s51,s49,0\n" qA "a[56:59],v[104:107],v[124:127],a[56:59]\n" \
qN "s44,s44,s50\n" "s_addc_u32 s45,s45,s51\n" qA "a[60:63],v[108:111],v[124:127],a[60:63]\n" qO "lgkmcnt(3)\n" \
qA "a[0:3],v[64:67],v[80:83],a[0:3]\n" qC "v[96:97],v36 offset:36864\n" qN "m0,s61,65536\n" qA "a[4:7],v[68:71],v[80:83],a[4:7]\n" \
qC "v[98:99],v36 offset:37376\n" qE "v42,s[40:43],s56" qV qA "a[8:11],v[72:75],v[80:83],a[8:11]\n" qC "v[100:101],v37 offset:36864\n" \
qA "a[12:15],v[76:79],v[80:83],a[12:15]\n" qC "v[102:103],v37 offset:37376\n" qO "lgkmcnt(6)\n" qA "a[16:19],v[64:67],v[84:87],a[16:19]\n" \
qC "v[104:105],v38 offset:36864\n" qE "v42,s[40:43],s56 offen offset:1024 sc1 lds\n" qA "a[20:23],v[68:71],v[84:87],a[20:23]\n" qC "v[106:107],v38 offset:37376\n" \
qA "a[24:27],v[72:75],v[84:87],a[24:27]\n" qC "v[108:109],v39 offset:36864\n" qN "m0,s61,67584\n" qA "a[28:31],v[76:79],v[84:87],a[28:31]\n" \
qC "v[110:111],v39 offset:37376\n" qE "v42,s[40:43],s57" qV qO "lgkmcnt(9)\n" qA "a[32:35],v[64:67],v[88:91],a[32:35]\n" \
qD "v[112:115],v41 offset:40960\n" qA "a[36:39],v[68:71],v[88:91],a[36:39]\n" qD "v[116:119],v41 offset:41216\n" qA "a[40:43],v[72:75],v[88:91],a[40:43]\n" \
qD "v[120:123],v41 offset:41472\n" qE "v42,s[40:43],s57 offen offset:1024 sc1 lds\n" qA "a[44:47],v[76:79],v[88:91],a[44:47]\n" qD "v[124:127],v41 offset:41728\n" \
qO "lgkmcnt(12)\n" qA "a[48:51],v[64:67],v[92:95],a[48:51]\n" qN "s56,s56,262144\n" qN "s57,s57,262144\n" \
qA "a[52:55],v[68:71],v[92:95],a[52:55]\n" qA "a[56:59],v[72:75],v[92:95],a[56:59]\n" qA "a[60:63],v[76:79],v[92:95],a[60:63]\n" qO "lgkmcnt(0)\n" \
qA "a[0:3],v[96:99],v[112:115],a[0:3]\n" qO "vmcnt(16)\n" "s_barrier\n" qC "v[64:65],v32 offset:0\n" \
qN "m0,s62,98304\n" qA "a[4:7],v[100:103],v[112:115],a[4:7]\n" qC "v[66:67],v32 offset:512\n" qE "v43,s[44:47],0" qU \
qA "a[8:11],v[104:107],v[112:115],a[8:11]\n" qC "v[68:69],v33 offset:0\n" qA "a[12:15],v[108:111],v[112:115],a[12:15]\n" qC "v[70:71],v33 offset:512\n" \
qN "m0,s62,99328\n" qA "a[16:19],v[96:99],v[116:119],a[16:19]\n" qC "v[72:73],v34 offset:0\n" qE "v44,s[44:47],s65" qU \
qA "a[20:23],v[100:103],v[116:119],a[20:23]\n" qC "v[74:75],v34 offset:512\n" qA "a[24:27],v[104:107],v[116:119],a[24:27]\n" qC "v[76:77],v35 offset:0\n" \
qN "m0,s62,100352\n" qA "a[28:31],v[108:111],v[116:119],a[28:31]\n" qC "v[78:79],v35 offset:512\n" qE "v43,s[44:47],s66" qU \
qA "a[32:35],v[96:99],v[120:123],a[32:35]\n" qD "v[80:83],v40 offset:0\n" qA "a[36:39],v[100:103],v[120:123],a[36:39]\n" qD "v[84:87],v40 offset:256\n" \
qN "m0,s62,101376\n" qA "a[40:43],v[104:107],v[120:123],a[40:43]\n" qD "v[88:91],v40 offset:512\n" qE "v44,s[44:47],s67" qU \
qA "a[44:47],v[108:111],v[120:123],a[44:47]\n" qD "v[92:95],v40 offset:768\n" qA "a[48:51],v[96:99],v[124:127],a[48:51]\n" qN "s58,s58,1\n" \
"s_cmp_eq_u32 s58,s59\n" qA "a[52:55],v[100:103],v[124:127],a[52:55]\n" "s_cselect_b32 s50,s48,131072\n" "s_cselect_b32 s51,s49,0\n" \
qA "a[56:59],v[104:107],v[124:127],a[56:59]\n" qN "s44,s44,s50\n" "s_addc_u32 s45,s45,s51\n" qA "a[60:63],v[108:111],v[124:127],a[60:63]\n" \
"s_sub_u32 s63,s63,1\n" "s_cmp_lg_u32 s63,0\n" "s_cbranch_scc1 G3L_%=\n" qO "lgkmcnt(3)\n" \
qA "a[0:3],v[64:67],v[80:83],a[0:3]\n" qC "v[96:97],v32 offset:4096\n" qN "m0,s61,98304\n" qA "a[4:7],v[68:71],v[80:83],a[4:7]\n" \
qC "v[98:99],v32 offset:4608\n" qE "v42,s[40:43],s56" qV qA "a[8:11],v[72:75],v[80:83],a[8:11]\n" qC "v[100:101],v33 offset:4096\n" \
qA "a[12:15],v[76:79],v[80:83],a[12:15]\n" qC "v[102:103],v33 offset:4608\n" qO "lgkmcnt(6)\n" qA "a[16:19],v[64:67],v[84:87],a[16:19]\n" \
qC "v[104:105],v34 offset:4096\n" qE "v42,s[40:43],s56 offen offset:1024 sc1 lds\n" qA "a[20:23],v[68:71],v[84:87],a[20:23]\n" qC "v[106:107],v34 offset:4608\n" \
qA "a[24:27],v[72:75],v[84:87],a[24:27]\n" qC "v[108:109],v35 offset:4096\n" qN "m0,s61,100352\n" qA "a[28:31],v[76:79],v[84:87],a[28:31]\n" \
qC "v[110:111],v35 offset:4608\n" qE "v42,s[40:43],s57" qV qO "lgkmcnt(9)\n" qA "a[32:35],v[64:67],v[88:91],a[32:35]\n" \
qD "v[112:115],v40 offset:8192\n" qA "a[36:39],v[68:71],v[88:91],a[36:39]\n" qD "v[116:119],v40 offset:8448\n" qA "a[40:43],v[72:75],v[88:91],a[40:43]\n" \
qD "v[120:123],v40 offset:8704\n" qE "v42,s[40:43],s57 offen offset:1024 sc1 lds\n" qA "a[44:47],v[76:79],v[88:91],a[44:47]\n" qD "v[124:127],v40 offset:8960\n" \
qO "lgkmcnt(12)\n" qA "a[48:51],v[64:67],v[92:95],a[48:51]\n" qN "s56,s56,262144\n" qN "s57,s57,262144\n" \
qA "a[52:55],v[68:71],v[92:95],a[52:55]\n" qA "a[56:59],v[72:75],v[92:95],a[56:59]\n" qA "a[60:63],v[76:79],v[92:95],a[60:63]\n" qO "lgkmcnt(0)\n" \
qA "a[0:3],v[96:99],v[112:115],a[0:3]\n" qO "vmcnt(16)\n" "s_barrier\n" qC "v[64:65],v32 offset:32768\n" \
qA "a[4:7],v[100:103],v[112:115],a[4:7]\n" qC "v[66:67],v32 offset:33280\n" qA "a[8:11],v[104:107],v[112:115],a[8:11]\n" qC "v[68:69],v33 offset:32768\n" \
qA "a[12:15],v[108:111],v[112:115],a[12:15]\n" qC "v[70:71],v33 offset:33280\n" qA "a[16:19],v[96:99],v[116:119],a[16:19]\n" qC "v[72:73],v34 offset:32768\n" \
qA "a[20:23],v[100:103],v[116:119],a[20:23]\n" qC "v[74:75],v34 offset:33280\n" qA "a[24:27],v[104:107],v[116:119],a[24:27]\n" qC "v[76:77],v35 offset:32768\n" \
qA "a[28:31],v[108:111],v[116:119],a[28:31]\n" qC "v[78:79],v35 offset:33280\n" qA "a[32:35],v[96:99],v[120:123],a[32:35]\n" qD "v[80:83],v40 offset:32768\n" \
qA "a[36:39],v[100:103],v[120:123],a[36:39]\n" qD "v[84:87],v40 offset:33024\n" qA "a[40:43],v[104:107],v[120:123],a[40:43]\n" qD "v[88:91],v40 offset:33280\n" \
qA "a[44:47],v[108:111],v[120:123],a[44:47]\n" qD "v[92:95],v40 offset:33536\n" qA "a[48:51],v[96:99],v[124:127],a[48:51]\n" qA "a[52:55],v[100:103],v[124:127],a[52:55]\n" \
qA "a[56:59],v[104:107],v[124:127],a[56:59]\n" qA "a[60:63],v[108:111],v[124:127],a[60:63]\n" qO "lgkmcnt(3)\n" qA "a[0:3],v[64:67],v[80:83],a[0:3]\n" \
qC "v[96:97],v32 offset:36864\n" qA "a[4:7],v[68:71],v[80:83],a[4:7]\n" qC "v[98:99],v32 offset:37376\n" qA "a[8:11],v[72:75],v[80:83],a[8:11]\n" \
qC "v[100:101],v33 offset:36864\n" qA "a[12:15],v[76:79],v[80:83],a[12:15]\n" qC "v[102:103],v33 offset:37376\n" qO "lgkmcnt(6)\n" \
qA "a[16:19],v[64:67],v[84:87],a[16:19]\n" qC "v[104:105],v34 offset:36864\n" qA "a[20:23],v[68:71],v[84:87],a[20:23]\n" qC "v[106:107],v34 offset:37376\n" \
qA "a[24:27],v[72:75],v[84:87],a[24:27]\n" qC "v[108:109],v35 offset:36864\n" qA "a[28:31],v[76:79],v[84:87],a[28:31]\n" qC "v[110:111],v35 offset:37376\n" \
qO "lgkmcnt(9)\n" qA "a[32:35],v[64:67],v[88:91],a[32:35]\n" qD "v[112:115],v40 offset:40960\n" qA "a[36:39],v[68:71],v[88:91],a[36:39]\n" \
qD "v[116:119],v40 offset:41216\n" qA "a[40:43],v[72:75],v[88:91],a[40:43]\n" qD "v[120:123],v40 offset:41472\n" qA "a[44:47],v[76:79],v[88:91],a[44:47]\n" \
qD "v[124:127],v40 offset:41728\n" qO "lgkmcnt(12)\n" qA "a[48:51],v[64:67],v[92:95],a[48:51]\n" qA "a[52:55],v[68:71],v[92:95],a[52:55]\n" \
qA "a[56:59],v[72:75],v[92:95],a[56:59]\n" qA "a[60:63],v[76:79],v[92:95],a[60:63]\n" qO "lgkmcnt(0)\n" qA "a[0:3],v[96:99],v[112:115],a[0:3]\n" \
qO "vmcnt(8)\n" "s_barrier\n" qC "v[64:65],v36 offset:0\n" qA "a[4:7],v[100:103],v[112:115],a[4:7]\n" \
qC "v[66:67],v36 offset:512\n" qA "a[8:11],v[104:107],v[112:115],a[8:11]\n" qC "v[68:69],v37 offset:0\n" qA "a[12:15],v[108:111],v[112:115],a[12:15]\n" \
qC "v[70:71],v37 offset:512\n" qA "a[16:19],v[96:99],v[116:119],a[16:19]\n" qC "v[72:73],v38 offset:0\n" qA "a[20:23],v[100:103],v[116:119],a[20:23]\n" \
qC "v[74:75],v38 offset:512\n" qA "a[24:27],v[104:107],v[116:119],a[24:27]\n" qC "v[76:77],v39 offset:0\n" qA "a[28:31],v[108:111],v[116:119],a[28:31]\n" \
qC "v[78:79],v39 offset:512\n" qA "a[32:35],v[96:99],v[120:123],a[32:35]\n" qD "v[80:83],v41 offset:0\n" qA "a[36:39],v[100:103],v[120:123],a[36:39]\n" \
qD "v[84:87],v41 offset:256\n" qA "a[40:43],v[104:107],v[120:123],a[40:43]\n" qD "v[88:91],v41 offset:512\n" qA "a[44:47],v[108:111],v[120:123],a[44:47]\n" \
qD "v[92:95],v41 offset:768\n" qA "a[48:51],v[96:99],v[124:127],a[48:51]\n" qA "a[52:55],v[100:103],v[124:127],a[52:55]\n" qA "a[56:59],v[104:107],v[124:127],a[56:59]\n" \
qA "a[60:63],v[108:111],v[124:127],a[60:63]\n" qO "lgkmcnt(3)\n" qA "a[0:3],v[64:67],v[80:83],a[0:3]\n" qC "v[96:97],v36 offset:4096\n" \
qA "a[4:7],v[68:71],v[80:83],a[4:7]\n" qC "v[98:99],v36 offset:4608\n" qA "a[8:11],v[72:75],v[80:83],a[8:11]\n" qC "v[100:101],v37 offset:4096\n" \
qA "a[12:15],v[76:79],v[80:83],a[12:15]\n" qC "v[102:103],v37 offset:4608\n" qO "lgkmcnt(6)\n" qA "a[16:19],v[64:67],v[84:87],a[16:19]\n" \
qC "v[104:105],v38 offset:4096\n" qA "a[20:23],v[68:71],v[84:87],a[20:23]\n" qC "v[106:107],v38 offset:4608\n" qA "a[24:27],v[72:75],v[84:87],a[24:27]\n" \
qC "v[108:109],v39 offset:4096\n" qA "a[28:31],v[76:79],v[84:87],a[28:31]\n" qC "v[110:111],v39 offset:4608\n" qO "lgkmcnt(9)\n" \
qA "a[32:35],v[64:67],v[88:91],a[32:35]\n" qD "v[112:115],v41 offset:8192\n" qA "a[36:39],v[68:71],v[88:91],a[36:39]\n" qD "v[116:119],v41 offset:8448\n" \
qA "a[40:43],v[72:75],v[88:91],a[40:43]\n" qD "v[120:123],v41 offset:8704\n" qA "a[44:47],v[76:79],v[88:91],a[44:47]\n" qD "v[124:127],v41 offset:8960\n" \
qO "lgkmcnt(12)\n" qA "a[48:51],v[64:67],v[92:95],a[48:51]\n" qA "a[52:55],v[68:71],v[92:95],a[52:55]\n" qA "a[56:59],v[72:75],v[92:95],a[56:59]\n" \
qA "a[60:63],v[76:79],v[92:95],a[60:63]\n" qO "lgkmcnt(0)\n" qA "a[0:3],v[96:99],v[112:115],a[0:3]\n" qO "vmcnt(0)\n" \
"s_barrier\n" qC "v[64:65],v36 offset:32768\n" qA "a[4:7],v[100:103],v[112:115],a[4:7]\n" qC "v[66:67],v36 offset:33280\n" \
qA "a[8:11],v[104:107],v[112:115],a[8:11]\n" qC "v[68:69],v37 offset:32768\n" qA "a[12:15],v[108:111],v[112:115],a[12:15]\n" qC "v[70:71],v37 offset:33280\n" \
qA "a[16:19],v[96:99],v[116:119],a[16:19]\n" qC "v[72:73],v38 offset:32768\n" qA "a[20:23],v[100:103],v[116:119],a[20:23]\n" qC "v[74:75],v38 offset:33280\n" \
qA "a[24:27],v[104:107],v[116:119],a[24:27]\n" qC "v[76:77],v39 offset:32768\n" qA "a[28:31],v[108:111],v[116:119],a[28:31]\n" qC "v[78:79],v39 offset:33280\n" \
qA "a[32:35],v[96:99],v[120:123],a[32:35]\n" qD "v[80:83],v41 offset:32768\n" qA "a[36:39],v[100:103],v[120:123],a[36:39]\n" qD "v[84:87],v41 offset:33024\n" \
qA "a[40:43],v[104:107],v[120:123],a[40:43]\n" qD "v[88:91],v41 offset:33280\n" qA "a[44:47],v[108:111],v[120:123],a[44:47]\n" qD "v[92:95],v41 offset:33536\n" \
qA "a[48:51],v[96:99],v[124:127],a[48:51]\n" qA "a[52:55],v[100:103],v[124:127],a[52:55]\n" qA "a[56:59],v[104:107],v[124:127],a[56:59]\n" qA "a[60:63],v[108:111],v[124:127],a[60:63]\n" \
qO "lgkmcnt(3)\n" qA "a[0:3],v[64:67],v[80:83],a[0:3]\n" qC "v[96:97],v36 offset:36864\n" qA "a[4:7],v[68:71],v[80:83],a[4:7]\n" \
qC "v[98:99],v36 offset:37376\n" qA "a[8:11],v[72:75],v[80:83],a[8:11]\n" qC "v[100:101],v37 offset:36864\n" qA "a[12:15],v[76:79],v[80:83],a[12:15]\n" \
qC "v[102:103],v37 offset:37376\n" qO "lgkmcnt(6)\n" qA "a[16:19],v[64:67],v[84:87],a[16:19]\n" qC "v[104:105],v38 offset:36864\n" \
qA "a[20:23],v[68:71],v[84:87],a[20:23]\n" qC "v[106:107],v38 offset:37376\n" qA "a[24:27],v[72:75],v[84:87],a[24:27]\n" qC "v[108:109],v39 offset:36864\n" \
qA "a[28:31],v[76:79],v[84:87],a[28:31]\n" qC "v[110:111],v39 offset:37376\n" qO "lgkmcnt(9)\n" qA "a[32:35],v[64:67],v[88:91],a[32:35]\n" \
qD "v[112:115],v41 offset:40960\n" qA "a[36:39],v[68:71],v[88:91],a[36:39]\n" qD "v[116:119],v41 offset:41216\n" qA "a[40:43],v[72:75],v[88:91],a[40:43]\n" \
qD "v[120:123],v41 offset:41472\n" qA "a[44:47],v[76:79],v[88:91],a[44:47]\n" qD "v[124:127],v41 offset:41728\n" qO "lgkmcnt(12)\n" \
qA "a[48:51],v[64:67],v[92:95],a[48:51]\n" qA "a[52:55],v[68:71],v[92:95],a[52:55]\n" qA "a[56:59],v[72:75],v[92:95],a[56:59]\n" qA "a[60:63],v[76:79],v[92:95],a[60:63]\n" \
qO "lgkmcnt(0)\n" qA "a[0:3],v[96:99],v[112:115],a[0:3]\n" qA "a[4:7],v[100:103],v[112:115],a[4:7]\n" qA "a[8:11],v[104:107],v[112:115],a[8:11]\n" \
qA "a[12:15],v[108:111],v[112:115],a[12:15]\n" qA "a[16:19],v[96:99],v[116:119],a[16:19]\n" qA "a[20:23],v[100:103],v[116:119],a[20:23]\n" qA "a[24:27],v[104:107],v[116:119],a[24:27]\n" \
qA "a[28:31],v[108:111],v[116:119],a[28:31]\n" qA "a[32:35],v[96:99],v[120:123],a[32:35]\n" qA "a[36:39],v[100:103],v[120:123],a[36:39]\n" qA "a[40:43],v[104:107],v[120:123],a[40:43]\n" \
qA "a[44:47],v[108:111],v[120:123],a[44:47]\n" qA "a[48:51],v[96:99],v[124:127],a[48:51]\n" qA "a[52:55],v[100:103],v[124:127],a[52:55]\n" qA "a[56:59],v[104:107],v[124:127],a[56:59]\n" \
qA "a[60:63],v[108:111],v[124:127],a[60:63]\n" "s_nop 7\n"

#define G3_CLOB "v64", "v65", "v66", "v67", "v68", "v69", "v70", "v71", "v72", "v73", "v74", "v75", "v76", "v77", "v78", "v79", "v80", "v81", "v82", "v83", "v84", "v85", "v86", "v87", "v88", "v89", "v90", "v91", "v92", "v93", "v94", "v95", "v96", "v97", "v98", "v99", "v100", "v101", "v102", "v103", "v104", "v105", "v106", "v107", "v108", "v109", "v110", "v111", "v112", "v113", "v114", "v115", "v116", "v117", "v118", "v119", "v120", "v121", "v122", "v123", "v124", "v125", "v126", "v127", "s50", "s51", "s63", "scc", "memory"

#define ATTN_MAIN \
qF "v[128:131],v45,s[84:87],0 offen offset:0 sc1\n" qD "a[160:163],v40 offset:0\n" qD "a[164:167],v41 offset:0\n" qD "a[64:67],v40 offset:32768\n" \
qD "a[68:71],v41 offset:32768\n" qD "a[0:3],v40 offset:16384\n" qD "a[4:7],v41 offset:16384\n" qD "a[168:171],v40 offset:2048\n" \
qD "a[172:175],v41 offset:2048\n" qD "a[8:11],v40 offset:18432\n" qD "a[12:15],v41 offset:18432\n" qD "a[16:19],v40 offset:20480\n" \
qD "a[20:23],v41 offset:20480\n" qD "a[24:27],v40 offset:22528\n" "v_cmp_le_i32 s[40:41],0,v44\n" "v_cmp_le_i32 s[42:43],1,v44\n" \
"v_cmp_le_i32 s[44:45],2,v44\n" "v_cmp_le_i32 s[64:65],3,v44\n" "v_mov_b32 v14,0xffff\n" "v_mov_b32 v15,0xffff0000\n" \
qD "a[28:31],v41 offset:22528\n" qP "v240,0,v14,s[40:41]\n" qP "v241,0,v15,s[42:43]\n" qP "v242,0,v14,s[44:45]\n" \
qP "v243,0,v15,s[64:65]\n" "v_cmp_le_i32 s[66:67],8,v44\n" "v_cmp_le_i32 s[70:71],9,v44\n" "v_cmp_le_i32 s[72:73],10,v44\n" \
"v_cmp_le_i32 s[76:77],11,v44\n" "v_or_b32 v24,v240,v241\n" "v_or_b32 v25,v242,v243\n" qP "v240,0,v14,s[66:67]\n" \
qO "lgkmcnt(11)\n" qB "v[48:63],a[64:67],a[160:163],0\n" qP "v241,0,v15,s[70:71]\n" qP "v242,0,v14,s[72:73]\n" \
qP "v243,0,v15,s[76:77]\n" "v_cmp_le_i32 s[78:79],16,v44\n" "v_cmp_le_i32 s[80:81],17,v44\n" qO "lgkmcnt(9)\n" \
qB "v[64:79],a[0:3],a[160:163],0\n" "v_cmp_le_i32 s[82:83],18,v44\n" "v_cmp_le_i32 s[88:89],19,v44\n" "v_or_b32 v26,v240,v241\n" \
"v_or_b32 v27,v242,v243\n" qP "v240,0,v14,s[78:79]\n" qO "lgkmcnt(5)\n" qB "v[80:95],a[8:11],a[160:163],0\n" \
qP "v241,0,v15,s[80:81]\n" qP "v242,0,v14,s[82:83]\n" qP "v243,0,v15,s[88:89]\n" qD "a[32:35],v40 offset:24576\n" \
"v_cmp_le_i32 s[90:91],24,v44\n" "v_cmp_le_i32 s[94:95],26,v44\n" qB "v[48:63],a[68:71],a[164:167],v[48:63]\n" qD "a[36:39],v41 offset:24576\n" \
"v_cmp_le_i32 s[92:93],25,v44\n" "v_cmp_le_i32 s[98:99],27,v44\n" "v_or_b32 v28,v240,v241\n" "v_or_b32 v29,v242,v243\n" \
qD "a[40:43],v40 offset:26624\n" qB "v[64:79],a[4:7],a[164:167],v[64:79]\n" qP "v240,0,v14,s[90:91]\n" qP "v242,0,v14,s[94:95]\n" \
qF "v[132:135],v46,s[84:87],0 offen offset:0 sc1\n" qD "a[44:47],v41 offset:26624\n" qD "a[48:51],v40 offset:28672\n" qO "lgkmcnt(9)\n" \
qB "v[80:95],a[12:15],a[164:167],v[80:95]\n" qD "a[52:55],v41 offset:28672\n" qD "a[56:59],v40 offset:30720\n" qD "a[60:63],v41 offset:30720\n" \
qD "a[72:75],v40 offset:34816\n" qD "a[76:79],v41 offset:34816\n" qP "v112,v64,v48,s[40:41]\n" qO "lgkmcnt(13)\n" \
qB "v[96:111],a[16:19],a[160:163],0\n" qP "v113,v65,v49,s[42:43]\n" qP "v114,v66,v50,s[44:45]\n" qP "v115,v67,v51,s[64:65]\n" \
qP "v116,v68,v52,s[66:67]\n" qP "v117,v69,v53,s[70:71]\n" qO "lgkmcnt(12)\n" qB "v[96:111],a[20:23],a[164:167],v[96:111]\n" \
qP "v118,v70,v54,s[72:73]\n" qP "v119,v71,v55,s[76:77]\n" qP "v120,v72,v56,s[78:79]\n" qP "v121,v73,v57,s[80:81]\n" \
qP "v122,v74,v58,s[82:83]\n" qP "v123,v75,v59,s[88:89]\n" qP "v124,v76,v60,s[90:91]\n" qP "v125,v77,v61,s[92:93]\n" \
qP "v126,v78,v62,s[94:95]\n" qP "v127,v79,v63,s[98:99]\n" qO "lgkmcnt(11)\n" qB "v[48:63],a[24:27],a[160:163],0\n" \
qM "v12,v112,v113,v114\n" qM "v14,v80,v81,v82\n" qM "v12,v12,v115,v116\n" qM "v14,v14,v83,v84\n" \
qM "v12,v12,v117,v118\n" qO "lgkmcnt(10)\n" qB "v[48:63],a[28:31],a[164:167],v[48:63]\n" qM "v14,v14,v85,v86\n" \
qM "v12,v12,v119,v120\n" qM "v14,v14,v87,v88\n" qM "v12,v12,v121,v122\n" qM "v14,v14,v89,v90\n" \
qO "lgkmcnt(9)\n" qB "v[64:79],a[32:35],a[160:163],0\n" qM "v12,v12,v123,v124\n" qM "v14,v14,v91,v92\n" \
qM "v12,v12,v125,v126\n" qM "v14,v14,v93,v94\n" "v_max_f32 v12,v12,v127\n" qO "lgkmcnt(8)\n" \
qB "v[64:79],a[36:39],a[164:167],v[64:79]\n" "v_max_f32 v14,v14,v95\n" qM "v12,v12,v96,v97\n" qM "v14,v14,v48,v49\n" \
qM "v12,v12,v98,v99\n" qM "v14,v14,v50,v51\n" qO "lgkmcnt(7)\n" qB "v[80:95],a[40:43],a[160:163],0\n" \
qM "v12,v12,v100,v101\n" qM "v14,v14,v52,v53\n" qM "v12,v12,v102,v103\n" qM "v14,v14,v54,v55\n" \
qM "v12,v12,v104,v105\n" qO "lgkmcnt(6)\n" qB "v[80:95],a[44:47],a[164:167],v[80:95]\n" qF "v[136:139],v45,s[84:87],0 offen offset:256 sc1\n" \
qM "v14,v14,v56,v57\n" qM "v12,v12,v106,v107\n" qM "v14,v14,v58,v59\n" qM "v12,v12,v108,v109\n" \
qM "v14,v14,v60,v61\n" qM "v12,v12,v110,v111\n" qO "lgkmcnt(5)\n" qB "v[96:111],a[48:51],a[160:163],0\n" \
qM "v14,v14,v62,v63\n" qM "v12,v12,v64,v65\n" "s_nop 0\n" qM "v14,v14,v80,v81\n" \
qM "v12,v12,v66,v67\n" qM "v14,v14,v82,v83\n" qO "lgkmcnt(3)\n" qB "v[48:63],a[56:59],a[160:163],0\n" \
qM "v12,v12,v68,v69\n" qM "v14,v14,v84,v85\n" qM "v12,v12,v70,v71\n" qM "v14,v14,v86,v87\n" \
qM "v12,v12,v72,v73\n" qB "v[96:111],a[52:55],a[164:167],v[96:111]\n" qM "v14,v14,v88,v89\n" qM "v12,v12,v74,v75\n" \
qM "v14,v14,v90,v91\n" qM "v12,v12,v76,v77\n" qM "v14,v14,v92,v93\n" qO "lgkmcnt(2)\n" \
qB "v[48:63],a[60:63],a[164:167],v[48:63]\n" qM "v12,v12,v78,v79\n" qM "v14,v14,v94,v95\n" qC "a[144:145],v42 offset:16384\n" \
qC "a[146:147],v42 offset:16896\n" qC "a[148:149],v42 offset:17408\n" qM "v12,v12,v96,v97\n" qC "a[150:151],v42 offset:17920\n" \
qM "v12,v12,v98,v99\n" qC "a[80:81],v42 offset:0\n" qM "v12,v12,v100,v101\n" qP "v241,0,v15,s[92:93]\n" \
qM "v12,v12,v102,v103\n" qM "v14,v14,v48,v49\n" qM "v12,v12,v104,v105\n" qM "v14,v14,v50,v51\n" \
qM "v12,v12,v106,v107\n" qM "v14,v14,v52,v53\n" qM "v12,v12,v108,v109\n" qM "v14,v14,v54,v55\n" \
qM "v12,v12,v110,v111\n" qM "v14,v14,v56,v57\n" qP "v243,0,v15,s[98:99]\n" qM "v14,v14,v58,v59\n" \
qC "a[82:83],v42 offset:512\n" qM "v14,v14,v60,v61\n" "v_or_b32 v30,v240,v241\n" qM "v14,v14,v62,v63\n" \
"v_or_b32 v31,v242,v243\n" "v_max_f32 v12,v12,v14\n" qC "a[84:85],v42 offset:1024\n" "v_mov_b32 v14,v12\n" \
qC "a[86:87],v42 offset:1536\n" qC "a[88:89],v42 offset:2048\n" qC "a[90:91],v42 offset:2560\n" qF "v[140:143],v47,s[84:87],0 offen offset:256 sc1\n" \
"v_permlane32_swap_b32 v12,v14\n" qC "a[92:93],v42 offset:3072\n" "v_max_f32 v12,v12,v14\n" "v_mov_b32 v8,0x3f803f80\n" \
"v_mul_f32 v208,-1.0,v12\n" "v_mul_f32 v209,-1.0,v12\n" "v_add_f32 v112,v112,v208\n" "v_add_f32 v113,v113,v208\n" \
"v_add_f32 v114,v114,v208\n" "v_add_f32 v115,v115,v208\n" "v_add_f32 v116,v116,v208\n" "v_add_f32 v117,v117,v208\n" \
"v_add_f32 v118,v118,v208\n" "v_add_f32 v119,v119,v208\n" qL "v112,v112\n" qL "v113,v113\n" \
qL "v114,v114\n" qL "v115,v115\n" qL "v116,v116\n" qL "v117,v117\n" \
qL "v118,v118\n" qL "v119,v119\n" qJ "v240,v112,v113\n" qJ "v241,v114,v115\n" \
qJ "v242,v116,v117\n" qJ "v243,v118,v119\n" "v_add_f32 v120,v120,v208\n" "v_add_f32 v121,v121,v208\n" \
"v_add_f32 v122,v122,v208\n" "v_add_f32 v123,v123,v208\n" "v_add_f32 v124,v124,v208\n" "v_add_f32 v125,v125,v208\n" \
"v_add_f32 v126,v126,v208\n" "v_add_f32 v127,v127,v208\n" "v_and_b32 v248,v240,v24\n" "v_and_b32 v249,v241,v25\n" \
"v_and_b32 v250,v242,v26\n" "v_and_b32 v251,v243,v27\n" qL "v120,v120\n" qL "v121,v121\n" \
qO "lgkmcnt(9)\n" qB "a[208:223],a[144:147],v[248:251],0\n" qL "v122,v122\n" qL "v123,v123\n" \
qL "v124,v124\n" qL "v125,v125\n" qL "v126,v126\n" qL "v127,v127\n" \
"v_mul_f32 v210,-1.0,v12\n" qF "v[144:147],v45,s[84:87],0 offen offset:512 sc1\n" "v_mul_f32 v211,-1.0,v12\n" "v_mul_f32 v212,-1.0,v12\n" \
"v_mul_f32 v213,-1.0,v12\n" "v_mul_f32 v214,-1.0,v12\n" "v_mul_f32 v215,-1.0,v12\n" "v_mul_f32 v216,-1.0,v12\n" \
"v_mul_f32 v217,-1.0,v12\n" "v_mul_f32 v218,-1.0,v12\n" "v_mul_f32 v219,-1.0,v12\n" "v_mul_f32 v220,-1.0,v12\n" \
"v_mul_f32 v221,-1.0,v12\n" "v_mul_f32 v222,-1.0,v12\n" "v_mul_f32 v223,-1.0,v12\n" qJ "v244,v120,v121\n" \
qJ "v245,v122,v123\n" qB "v[64:79],a[8:11],a[160:163],v[208:223]\n" qJ "v246,v124,v125\n" qJ "v247,v126,v127\n" \
"v_and_b32 v252,v244,v28\n" "v_and_b32 v253,v245,v29\n" "v_and_b32 v254,v246,v30\n" qB "v[80:95],a[16:19],a[160:163],v[208:223]\n" \
"v_and_b32 v255,v247,v31\n" "v_mov_b32 v9,0x3f803f80\n" "v_mov_b32 v10,0x3f803f80\n" "v_mov_b32 v11,0x3f803f80\n" \
"v_bfi_b32 v32,v24,0,v240\n" qO "lgkmcnt(7)\n" qB "a[208:223],a[148:151],v[252:255],a[208:223]\n" "v_bfi_b32 v33,v25,0,v241\n" \
"v_bfi_b32 v34,v26,0,v242\n" "v_bfi_b32 v35,v27,0,v243\n" qC "a[94:95],v42 offset:3584\n" qC "a[96:97],v42 offset:4096\n" \
"v_bfi_b32 v36,v28,0,v244\n" qB "v[64:79],a[12:15],a[164:167],v[64:79]\n" "v_bfi_b32 v37,v29,0,v245\n" "v_bfi_b32 v38,v30,0,v246\n" \
"v_bfi_b32 v39,v31,0,v247\n" qC "a[98:99],v42 offset:4608\n" qC "a[100:101],v42 offset:5120\n" qC "a[102:103],v42 offset:5632\n" \
qB "v[80:95],a[20:23],a[164:167],v[80:95]\n" qC "a[104:105],v42 offset:6144\n" qC "a[106:107],v42 offset:6656\n" qC "a[108:109],v42 offset:7168\n" \
qC "a[110:111],v42 offset:7680\n" qC "a[112:113],v42 offset:8192\n" qL "v64,v64\n" qB "a[176:191],v[8:11],v[240:243],0\n" \
qL "v65,v65\n" qL "v66,v66\n" qL "v67,v67\n" qL "v68,v68\n" \
qO "lgkmcnt(15)\n" qB "a[208:223],a[80:83],v[32:35],a[208:223]\n" qL "v69,v69\n" qF "v[148:151],v46,s[84:87],0 offen offset:512 sc1\n" \
qL "v70,v70\n" qL "v71,v71\n" qB "a[176:191],v[8:11],v[244:247],a[176:191]\n" qL "v72,v72\n" \
qL "v73,v73\n" qL "v74,v74\n" qL "v75,v75\n" qO "lgkmcnt(13)\n" \
qB "a[208:223],a[84:87],v[36:39],a[208:223]\n" qL "v76,v76\n" qL "v77,v77\n" qL "v78,v78\n" \
qL "v79,v79\n" qB "v[96:111],a[24:27],a[160:163],v[208:223]\n" qL "v80,v80\n" qL "v81,v81\n" \
qL "v82,v82\n" qL "v83,v83\n" qB "v[48:63],a[32:35],a[160:163],v[208:223]\n" qL "v84,v84\n" \
qL "v85,v85\n" qL "v86,v86\n" qL "v87,v87\n" qB "v[96:111],a[28:31],a[164:167],v[96:111]\n" \
qL "v88,v88\n" qL "v89,v89\n" qL "v90,v90\n" qL "v91,v91\n" \
qB "v[48:63],a[36:39],a[164:167],v[48:63]\n" qL "v92,v92\n" qL "v93,v93\n" qL "v94,v94\n" \
qL "v95,v95\n" qJ "v16,v64,v65\n" qJ "v17,v66,v67\n" qJ "v18,v68,v69\n" \
qJ "v19,v70,v71\n" qJ "v20,v72,v73\n" qJ "v21,v74,v75\n" qB "a[176:191],v[8:11],v[16:19],a[176:191]\n" \
qJ "v22,v76,v77\n" qF "v[152:155],v45,s[84:87],0 offen offset:768 sc1\n" qJ "v23,v78,v79\n" qJ "v240,v80,v81\n" \
qJ "v241,v82,v83\n" qB "v[64:79],a[40:43],a[160:163],v[208:223]\n" qJ "v242,v84,v85\n" qJ "v243,v86,v87\n" \
qJ "v244,v88,v89\n" qJ "v245,v90,v91\n" qJ "v246,v92,v93\n" qO "lgkmcnt(11)\n" \
qB "a[208:223],a[88:91],v[16:19],a[208:223]\n" qJ "v247,v94,v95\n" qL "v96,v96\n" qL "v97,v97\n" \
qL "v98,v98\n" qB "v[80:95],a[48:51],a[160:163],v[208:223]\n" qL "v99,v99\n" qL "v100,v100\n" \
qL "v101,v101\n" qL "v102,v102\n" qB "v[64:79],a[44:47],a[164:167],v[64:79]\n" qL "v103,v103\n" \
qL "v104,v104\n" qL "v105,v105\n" qL "v106,v106\n" qB "a[176:191],v[8:11],v[20:23],a[176:191]\n" \
qL "v107,v107\n" qL "v108,v108\n" qL "v109,v109\n" qL "v110,v110\n" \
qO "lgkmcnt(9)\n" qB "a[208:223],a[92:95],v[20:23],a[208:223]\n" qL "v111,v111\n" qL "v48,v48\n" \
qL "v49,v49\n" qL "v50,v50\n" qB "v[80:95],a[52:55],a[164:167],v[80:95]\n" qL "v51,v51\n" \
qL "v52,v52\n" qL "v53,v53\n" qL "v54,v54\n" qB "a[176:191],v[8:11],v[240:243],a[176:191]\n" \
qL "v55,v55\n" qF "v[156:159],v47,s[84:87],0 offen offset:768 sc1\n" qL "v56,v56\n" qL "v57,v57\n" \
qO "lgkmcnt(7)\n" qB "a[208:223],a[96:99],v[240:243],a[208:223]\n" qL "v58,v58\n" qL "v59,v59\n" \
qL "v60,v60\n" qL "v61,v61\n" qB "a[176:191],v[8:11],v[244:247],a[176:191]\n" qL "v62,v62\n" \
qL "v63,v63\n" qL "v64,v64\n" qL "v65,v65\n" qO "lgkmcnt(5)\n" \
qB "a[208:223],a[100:103],v[244:247],a[208:223]\n" qL "v66,v66\n" qL "v67,v67\n" qL "v68,v68\n" \
qL "v69,v69\n" qL "v70,v70\n" qL "v71,v71\n" qL "v72,v72\n" \
qL "v73,v73\n" qL "v74,v74\n" qL "v75,v75\n" qL "v76,v76\n" \
qL "v77,v77\n" qL "v78,v78\n" qL "v79,v79\n" qJ "v248,v96,v97\n" \
qJ "v249,v98,v99\n" qJ "v250,v100,v101\n" qJ "v251,v102,v103\n" qJ "v252,v104,v105\n" \
qJ "v253,v106,v107\n" qB "a[176:191],v[8:11],v[248:251],a[176:191]\n" qJ "v254,v108,v109\n" qJ "v255,v110,v111\n" \
qL "v80,v80\n" qL "v81,v81\n" qB "v[96:111],a[56:59],a[160:163],v[208:223]\n" qL "v82,v82\n" \
qL "v83,v83\n" qF "v[160:163],v45,s[84:87],0 offen offset:1024 sc1\n" qL "v84,v84\n" qB "v[96:111],a[60:63],a[164:167],v[96:111]\n" \
qL "v85,v85\n" qL "v86,v86\n" qL "v87,v87\n" qL "v88,v88\n" \
qO "lgkmcnt(3)\n" qB "a[208:223],a[104:107],v[248:251],a[208:223]\n" qL "v89,v89\n" qL "v90,v90\n" \
qL "v91,v91\n" qL "v92,v92\n" qB "a[176:191],v[8:11],v[252:255],a[176:191]\n" qL "v93,v93\n" \
qL "v94,v94\n" qL "v95,v95\n" qJ "v32,v48,v49\n" qO "lgkmcnt(1)\n" \
qB "a[208:223],a[108:111],v[252:255],a[208:223]\n" qJ "v33,v50,v51\n" qJ "v34,v52,v53\n" qJ "v35,v54,v55\n" \
qJ "v36,v56,v57\n" qJ "v37,v58,v59\n" qB "a[176:191],v[8:11],v[32:35],a[176:191]\n" qJ "v38,v60,v61\n" \
qJ "v39,v62,v63\n" qJ "v16,v64,v65\n" qJ "v17,v66,v67\n" qJ "v18,v68,v69\n" \
qB "v[48:63],a[72:75],a[168:171],0\n" qJ "v19,v70,v71\n" qJ "v20,v72,v73\n" qJ "v21,v74,v75\n" \
qJ "v22,v76,v77\n" qJ "v23,v78,v79\n" qB "v[64:79],a[8:11],a[168:171],0\n" qJ "v240,v80,v81\n" \
qJ "v241,v82,v83\n" qJ "v242,v84,v85\n" qJ "v243,v86,v87\n" qJ "v244,v88,v89\n" \
qB "v[48:63],a[76:79],a[172:175],v[48:63]\n" qJ "v245,v90,v91\n" qJ "v246,v92,v93\n" qJ "v247,v94,v95\n" \
qL "v96,v96\n" qL "v97,v97\n" qB "v[80:95],a[16:19],a[168:171],0\n" qF "v[164:167],v46,s[84:87],0 offen offset:1024 sc1\n" \
qL "v98,v98\n" qL "v99,v99\n" qL "v100,v100\n" qB "v[64:79],a[12:15],a[172:175],v[64:79]\n" \
qL "v101,v101\n" qL "v102,v102\n" qL "v103,v103\n" qL "v104,v104\n" \
qB "v[80:95],a[20:23],a[172:175],v[80:95]\n" qL "v105,v105\n" qL "v106,v106\n" qL "v107,v107\n" \
qL "v108,v108\n" qB "a[176:191],v[8:11],v[36:39],a[176:191]\n" qL "v109,v109\n" qL "v110,v110\n" \
qL "v111,v111\n" qP "v192,v64,v48,s[40:41]\n" qB "a[176:191],v[8:11],v[16:19],a[176:191]\n" qP "v193,v65,v49,s[42:43]\n" \
qP "v194,v66,v50,s[44:45]\n" qJ "v248,v96,v97\n" qJ "v249,v98,v99\n" qJ "v250,v100,v101\n" \
qB "a[176:191],v[8:11],v[20:23],a[176:191]\n" qJ "v251,v102,v103\n" qJ "v252,v104,v105\n" qJ "v253,v106,v107\n" \
qJ "v254,v108,v109\n" qJ "v255,v110,v111\n" qB "v[96:111],a[24:27],a[168:171],0\n" qP "v195,v67,v51,s[64:65]\n" \
qP "v196,v68,v52,s[66:67]\n" qP "v197,v69,v53,s[70:71]\n" qP "v198,v70,v54,s[72:73]\n" qP "v199,v71,v55,s[76:77]\n" \
qB "v[96:111],a[28:31],a[172:175],v[96:111]\n" qP "v200,v72,v56,s[78:79]\n" qP "v201,v73,v57,s[80:81]\n" qP "v202,v74,v58,s[82:83]\n" \
qP "v203,v75,v59,s[88:89]\n" qP "v204,v76,v60,s[90:91]\n" qB "a[176:191],v[8:11],v[240:243],a[176:191]\n" qP "v205,v77,v61,s[92:93]\n" \
qP "v206,v78,v62,s[94:95]\n" qP "v207,v79,v63,s[98:99]\n" qM "v13,v192,v193,v194\n" qF "v[168:171],v45,s[84:87],0 offen offset:1280 sc1\n" \
qB "v[48:63],a[32:35],a[168:171],0\n" qM "v15,v80,v81,v82\n" qM "v13,v13,v195,v196\n" qM "v15,v15,v83,v84\n" \
qM "v13,v13,v197,v198\n" qM "v15,v15,v85,v86\n" qB "v[48:63],a[36:39],a[172:175],v[48:63]\n" qM "v13,v13,v199,v200\n" \
qM "v15,v15,v87,v88\n" qM "v13,v13,v201,v202\n" qM "v15,v15,v89,v90\n" qM "v13,v13,v203,v204\n" \
qB "v[64:79],a[40:43],a[168:171],0\n" qM "v15,v15,v91,v92\n" qM "v13,v13,v205,v206\n" qM "v15,v15,v93,v94\n" \
qC "a[114:115],v42 offset:8704\n" "v_max_f32 v13,v13,v207\n" "v_max_f32 v15,v15,v95\n" qB "v[80:95],a[48:51],a[168:171],0\n" \
qM "v13,v13,v96,v97\n" qM "v15,v15,v48,v49\n" qM "v13,v13,v98,v99\n" qM "v15,v15,v50,v51\n" \
qC "a[116:117],v42 offset:9216\n" qC "a[118:119],v42 offset:9728\n" qB "v[64:79],a[44:47],a[172:175],v[64:79]\n" qM "v13,v13,v100,v101\n" \
qM "v15,v15,v52,v53\n" qM "v13,v13,v102,v103\n" qM "v15,v15,v54,v55\n" qC "a[120:121],v42 offset:10240\n" \
qM "v13,v13,v104,v105\n" qB "v[80:95],a[52:55],a[172:175],v[80:95]\n" qM "v15,v15,v56,v57\n" qC "a[122:123],v42 offset:10752\n" \
qM "v13,v13,v106,v107\n" qM "v15,v15,v58,v59\n" qM "v13,v13,v108,v109\n" qM "v15,v15,v60,v61\n" \
qB "a[176:191],v[8:11],v[244:247],a[176:191]\n" qC "a[124:125],v42 offset:11264\n" qM "v13,v13,v110,v111\n" qM "v15,v15,v62,v63\n" \
qC "a[126:127],v42 offset:11776\n" qM "v13,v13,v64,v65\n" qM "v15,v15,v80,v81\n" qB "v[96:111],a[56:59],a[168:171],0\n" \
qM "v13,v13,v66,v67\n" qM "v15,v15,v82,v83\n" qC "a[128:129],v42 offset:12288\n" qM "v13,v13,v68,v69\n" \
qM "v15,v15,v84,v85\n" qC "a[130:131],v42 offset:12800\n" qB "v[48:63],a[64:67],a[168:171],0\n" qM "v13,v13,v70,v71\n" \
qM "v15,v15,v86,v87\n" qF "v[172:175],v47,s[84:87],0 offen offset:1280 sc1\n" qM "v13,v13,v72,v73\n" qM "v15,v15,v88,v89\n" \
qO "lgkmcnt(8)\n" qB "a[208:223],a[112:115],v[32:35],a[208:223]\n" qC "a[132:133],v42 offset:13312\n" qC "a[134:135],v42 offset:13824\n" \
qM "v13,v13,v74,v75\n" qM "v15,v15,v90,v91\n" qM "v13,v13,v76,v77\n" qM "v15,v15,v92,v93\n" \
qB "v[96:111],a[60:63],a[172:175],v[96:111]\n" qM "v13,v13,v78,v79\n" qM "v15,v15,v94,v95\n" qC "a[136:137],v42 offset:14336\n" \
qC "a[138:139],v42 offset:14848\n" qC "a[140:141],v42 offset:15360\n" qC "a[142:143],v42 offset:15872\n" qB "v[48:63],a[68:71],a[172:175],v[48:63]\n" \
qC "a[152:153],v42 offset:18432\n" qC "a[154:155],v42 offset:18944\n" qC "a[156:157],v42 offset:19456\n" qC "a[158:159],v42 offset:19968\n" \
"s_nop 0\n" qM "v13,v13,v96,v97\n" qO "lgkmcnt(15)\n" qB "a[208:223],a[116:119],v[36:39],a[208:223]\n" \
qM "v13,v13,v98,v99\n" qM "v13,v13,v100,v101\n" qM "v13,v13,v102,v103\n" "s_nop 0\n" \
qM "v15,v15,v48,v49\n" qO "lgkmcnt(14)\n" qB "a[208:223],a[120:123],v[16:19],a[208:223]\n" qM "v15,v15,v50,v51\n" \
qM "v13,v13,v104,v105\n" qM "v15,v15,v52,v53\n" qM "v13,v13,v106,v107\n" qM "v15,v15,v54,v55\n" \
qO "lgkmcnt(12)\n" qB "a[208:223],a[124:127],v[20:23],a[208:223]\n" qM "v15,v15,v56,v57\n" qM "v13,v13,v108,v109\n" \
qM "v15,v15,v58,v59\n" qM "v13,v13,v110,v111\n" qM "v15,v15,v60,v61\n" qO "lgkmcnt(10)\n" \
qB "a[208:223],a[128:131],v[240:243],a[208:223]\n" qM "v15,v15,v62,v63\n" "v_max_f32 v13,v13,v15\n" "v_mov_b32 v15,v13\n" \
qO "lgkmcnt(8)\n" qB "a[208:223],a[132:135],v[244:247],a[208:223]\n" "v_permlane32_swap_b32 v13,v15\n" "v_max_f32 v13,v13,v15\n" \
"v_mul_f32 v224,-1.0,v13\n" qB "a[176:191],v[8:11],v[248:251],a[176:191]\n" "v_add_f32 v192,v192,v224\n" "v_add_f32 v193,v193,v224\n" \
qF "v[176:179],v45,s[84:87],0 offen offset:1536 sc1\n" "v_add_f32 v194,v194,v224\n" "v_add_f32 v195,v195,v224\n" qO "lgkmcnt(6)\n" \
qB "a[208:223],a[136:139],v[248:251],a[208:223]\n" "v_add_f32 v196,v196,v224\n" "v_add_f32 v197,v197,v224\n" "v_add_f32 v198,v198,v224\n" \
"v_add_f32 v199,v199,v224\n" qL "v192,v192\n" qB "a[176:191],v[8:11],v[252:255],a[176:191]\n" qL "v193,v193\n" \
qL "v194,v194\n" qL "v195,v195\n" qL "v196,v196\n" qO "lgkmcnt(4)\n" \
qB "a[208:223],a[140:143],v[252:255],a[208:223]\n" qL "v197,v197\n" qL "v198,v198\n" qL "v199,v199\n" \
"s_nop 2\n" qG "v2,a176\n" "v_mul_f32 v225,-1.0,v13\n" "v_rcp_f32 v2,v2\n" \
"v_mul_f32 v226,-1.0,v13\n" "v_mul_f32 v227,-1.0,v13\n" "v_mul_f32 v228,-1.0,v13\n" "v_mul_f32 v229,-1.0,v13\n" \
"v_mul_f32 v230,-1.0,v13\n" "v_mul_f32 v231,-1.0,v13\n" "v_mul_f32 v232,-1.0,v13\n" "v_mul_f32 v233,-1.0,v13\n" \
"v_mul_f32 v234,-1.0,v13\n" "v_mul_f32 v235,-1.0,v13\n" "v_mul_f32 v236,-1.0,v13\n" "v_mul_f32 v237,-1.0,v13\n" \
"v_mul_f32 v238,-1.0,v13\n" "v_mul_f32 v239,-1.0,v13\n" qJ "v240,v192,v193\n" qJ "v241,v194,v195\n" \
qB "v[64:79],a[16:19],a[168:171],v[224:239]\n" qJ "v242,v196,v197\n" qJ "v243,v198,v199\n" "v_add_f32 v200,v200,v224\n" \
"v_add_f32 v201,v201,v224\n" "v_add_f32 v202,v202,v224\n" qB "v[64:79],a[20:23],a[172:175],v[64:79]\n" "v_add_f32 v203,v203,v224\n" \
"v_add_f32 v204,v204,v224\n" "v_add_f32 v205,v205,v224\n" "v_add_f32 v206,v206,v224\n" "v_add_f32 v207,v207,v224\n" \
qB "a[192:207],v[8:11],v[240:243],0\n" qG "v112,a208\n" qG "v113,a209\n" qF "v[180:183],v46,s[84:87],0 offen offset:1536 sc1\n" \
qG "v114,a210\n" qG "v115,a211\n" qB "v[80:95],a[24:27],a[168:171],v[224:239]\n" qG "v116,a212\n" \
qG "v117,a213\n" qG "v118,a214\n" qG "v119,a215\n" "v_and_b32 v248,v240,v24\n" \
qB "v[80:95],a[28:31],a[172:175],v[80:95]\n" "v_and_b32 v249,v241,v25\n" "v_and_b32 v250,v242,v26\n" "v_and_b32 v251,v243,v27\n" \
qL "v200,v200\n" qL "v201,v201\n" qO "lgkmcnt(2)\n" qB "a[224:239],a[152:155],v[248:251],0\n" \
qL "v202,v202\n" qL "v203,v203\n" qL "v204,v204\n" qL "v205,v205\n" \
qB "v[96:111],a[32:35],a[168:171],v[224:239]\n" qL "v206,v206\n" qL "v207,v207\n" "v_mul_f32 v112,v112,v2\n" \
"v_mul_f32 v113,v113,v2\n" qB "v[96:111],a[36:39],a[172:175],v[96:111]\n" "v_mul_f32 v114,v114,v2\n" "v_mul_f32 v115,v115,v2\n" \
"v_mul_f32 v116,v116,v2\n" "v_mul_f32 v117,v117,v2\n" "v_mul_f32 v118,v118,v2\n" qB "v[48:63],a[40:43],a[168:171],v[224:239]\n" \
"v_mul_f32 v119,v119,v2\n" qJ "v32,v112,v113\n" qJ "v33,v114,v115\n" qJ "v34,v116,v117\n" \
qJ "v35,v118,v119\n" qB "v[48:63],a[44:47],a[172:175],v[48:63]\n" qJ "v244,v200,v201\n" qJ "v245,v202,v203\n" \
qJ "v246,v204,v205\n" qJ "v247,v206,v207\n" "v_permlane32_swap_b32 v32,v34\n" "v_permlane32_swap_b32 v33,v35\n" \
qB "a[192:207],v[8:11],v[244:247],a[192:207]\n" "v_and_b32 v252,v244,v28\n" "v_and_b32 v253,v245,v29\n" "v_and_b32 v254,v246,v30\n" \
"v_and_b32 v255,v247,v31\n" qG "v120,a216\n" qG "v121,a217\n" qF "v[32:35],v43,s[84:87],0 offen offset:0 sc1\n" \
qO "lgkmcnt(0)\n" qB "a[224:239],a[156:159],v[252:255],a[224:239]\n" qG "v122,a218\n" qG "v123,a219\n" \
qG "v124,a220\n" qG "v125,a221\n" qG "v126,a222\n" qG "v127,a223\n" \
"v_mul_f32 v120,v120,v2\n" "v_mul_f32 v121,v121,v2\n" "v_mul_f32 v122,v122,v2\n" "v_mul_f32 v123,v123,v2\n" \
"v_mul_f32 v124,v124,v2\n" "v_mul_f32 v125,v125,v2\n" "v_mul_f32 v126,v126,v2\n" "v_mul_f32 v127,v127,v2\n" \
qJ "v16,v120,v121\n" qJ "v17,v122,v123\n" qJ "v18,v124,v125\n" qJ "v19,v126,v127\n" \
"v_bfi_b32 v32,v24,0,v240\n" "v_bfi_b32 v33,v25,0,v241\n" "v_bfi_b32 v34,v26,0,v242\n" qF "v[184:187],v45,s[84:87],0 offen offset:1792 sc1\n" \
"v_bfi_b32 v35,v27,0,v243\n" "v_permlane32_swap_b32 v16,v18\n" "v_permlane32_swap_b32 v17,v19\n" qB "a[224:239],a[88:91],v[32:35],a[224:239]\n" \
qL "v64,v64\n" qL "v65,v65\n" qL "v66,v66\n" qL "v67,v67\n" \
qL "v68,v68\n" qL "v69,v69\n" qL "v70,v70\n" qL "v71,v71\n" \
"v_bfi_b32 v36,v28,0,v244\n" "v_bfi_b32 v37,v29,0,v245\n" "v_bfi_b32 v38,v30,0,v246\n" "v_bfi_b32 v39,v31,0,v247\n" \
qL "v72,v72\n" qL "v73,v73\n" qB "a[224:239],a[92:95],v[36:39],a[224:239]\n" qL "v74,v74\n" \
qL "v75,v75\n" qL "v76,v76\n" qL "v77,v77\n" qL "v78,v78\n" \
qL "v79,v79\n" qJ "v20,v72,v73\n" qJ "v21,v74,v75\n" qJ "v22,v76,v77\n" \
qJ "v23,v78,v79\n" qL "v80,v80\n" qL "v81,v81\n" qL "v82,v82\n" \
qL "v83,v83\n" qL "v84,v84\n" qL "v85,v85\n" qL "v86,v86\n" \
qL "v87,v87\n" qJ "v240,v80,v81\n" qJ "v241,v82,v83\n" qJ "v242,v84,v85\n" \
qJ "v243,v86,v87\n" qL "v88,v88\n" qF "v[16:19],v43,s[84:87],s68 offen offset:0 sc1\n" qL "v89,v89\n" \
"s_nop 0\n" qJ "v16,v64,v65\n" qJ "v17,v66,v67\n" qJ "v18,v68,v69\n" \
qJ "v19,v70,v71\n" qB "v[64:79],a[48:51],a[168:171],v[224:239]\n" qL "v90,v90\n" qL "v91,v91\n" \
qL "v92,v92\n" qL "v93,v93\n" qB "a[192:207],v[8:11],v[16:19],a[192:207]\n" qL "v94,v94\n" \
qL "v95,v95\n" qJ "v244,v88,v89\n" qJ "v245,v90,v91\n" qB "a[224:239],a[96:99],v[16:19],a[224:239]\n" \
qJ "v246,v92,v93\n" qF "v[188:191],v47,s[84:87],0 offen offset:1792 sc1\n" qJ "v247,v94,v95\n" qL "v96,v96\n" \
qB "a[192:207],v[8:11],v[20:23],a[192:207]\n" qL "v97,v97\n" qL "v98,v98\n" qL "v99,v99\n" \
qL "v100,v100\n" qB "a[224:239],a[100:103],v[20:23],a[224:239]\n" qL "v101,v101\n" qL "v102,v102\n" \
qL "v103,v103\n" qJ "v248,v96,v97\n" qB "a[192:207],v[8:11],v[240:243],a[192:207]\n" qJ "v249,v98,v99\n" \
qJ "v250,v100,v101\n" qJ "v251,v102,v103\n" qL "v104,v104\n" qL "v105,v105\n" \
qB "a[224:239],a[104:107],v[240:243],a[224:239]\n" qL "v106,v106\n" qL "v107,v107\n" qL "v108,v108\n" \
qL "v109,v109\n" qB "a[192:207],v[8:11],v[244:247],a[192:207]\n" qL "v110,v110\n" qL "v111,v111\n" \
qJ "v252,v104,v105\n" qJ "v253,v106,v107\n" qB "a[224:239],a[108:111],v[244:247],a[224:239]\n" qJ "v254,v108,v109\n" \
qJ "v255,v110,v111\n" qL "v48,v48\n" qL "v49,v49\n" qB "a[192:207],v[8:11],v[248:251],a[192:207]\n" \
qL "v50,v50\n" qL "v51,v51\n" qL "v52,v52\n" qL "v53,v53\n" \
qB "a[224:239],a[112:115],v[248:251],a[224:239]\n" qL "v54,v54\n" qL "v55,v55\n" qJ "v32,v48,v49\n" \
qJ "v33,v50,v51\n" qB "a[192:207],v[8:11],v[252:255],a[192:207]\n" qJ "v34,v52,v53\n" qJ "v35,v54,v55\n" \
qL "v56,v56\n" qL "v57,v57\n" qB "v[64:79],a[52:55],a[172:175],v[64:79]\n" qL "v58,v58\n" \
qL "v59,v59\n" qL "v60,v60\n" qL "v61,v61\n" qB "a[224:239],a[116:119],v[252:255],a[224:239]\n" \
qL "v62,v62\n" qL "v63,v63\n" qJ "v36,v56,v57\n" qJ "v37,v58,v59\n" \
qB "a[192:207],v[8:11],v[32:35],a[192:207]\n" qJ "v38,v60,v61\n" qJ "v39,v62,v63\n" qL "v64,v64\n" \
qL "v65,v65\n" qB "v[80:95],a[56:59],a[168:171],v[224:239]\n" qL "v66,v66\n" qL "v67,v67\n" \
qL "v68,v68\n" qL "v69,v69\n" qB "a[224:239],a[120:123],v[32:35],a[224:239]\n" qL "v70,v70\n" \
qL "v71,v71\n" qJ "v16,v64,v65\n" qJ "v17,v66,v67\n" qB "a[192:207],v[8:11],v[36:39],a[192:207]\n" \
qJ "v18,v68,v69\n" qJ "v19,v70,v71\n" qL "v72,v72\n" qL "v73,v73\n" \
qB "v[80:95],a[60:63],a[172:175],v[80:95]\n" qL "v74,v74\n" qL "v75,v75\n" qL "v76,v76\n" \
qL "v77,v77\n" qB "a[224:239],a[124:127],v[36:39],a[224:239]\n" qL "v78,v78\n" qL "v79,v79\n" \
qJ "v20,v72,v73\n" qJ "v21,v74,v75\n" qB "a[192:207],v[8:11],v[16:19],a[192:207]\n" qJ "v22,v76,v77\n" \
qJ "v23,v78,v79\n" qL "v80,v80\n" qL "v81,v81\n" qB "v[96:111],a[64:67],a[168:171],v[224:239]\n" \
qL "v82,v82\n" qL "v83,v83\n" qL "v84,v84\n" qL "v85,v85\n" \
qB "a[224:239],a[128:131],v[16:19],a[224:239]\n" qL "v86,v86\n" qL "v87,v87\n" qJ "v240,v80,v81\n" \
qJ "v241,v82,v83\n" qB "a[192:207],v[8:11],v[20:23],a[192:207]\n" qJ "v242,v84,v85\n" qJ "v243,v86,v87\n" \
qL "v88,v88\n" qL "v89,v89\n" qB "v[96:111],a[68:71],a[172:175],v[96:111]\n" qL "v90,v90\n" \
qL "v91,v91\n" qL "v92,v92\n" qL "v93,v93\n" qB "a[224:239],a[132:135],v[20:23],a[224:239]\n" \
qL "v94,v94\n" qL "v95,v95\n" qJ "v244,v88,v89\n" qJ "v245,v90,v91\n" \
qB "a[192:207],v[8:11],v[240:243],a[192:207]\n" qJ "v246,v92,v93\n" qJ "v247,v94,v95\n" qL "v96,v96\n" \
qL "v97,v97\n" qB "a[224:239],a[136:139],v[240:243],a[224:239]\n" qL "v98,v98\n" qL "v99,v99\n" \
qL "v100,v100\n" qL "v101,v101\n" qB "a[192:207],v[8:11],v[244:247],a[192:207]\n" qL "v102,v102\n" \
qL "v103,v103\n" qJ "v248,v96,v97\n" qJ "v249,v98,v99\n" qB "a[224:239],a[140:143],v[244:247],a[224:239]\n" \
qJ "v250,v100,v101\n" qJ "v251,v102,v103\n" qL "v104,v104\n" qL "v105,v105\n" \
qB "a[192:207],v[8:11],v[248:251],a[192:207]\n" qL "v106,v106\n" qL "v107,v107\n" qL "v108,v108\n" \
qL "v109,v109\n" qB "a[224:239],a[144:147],v[248:251],a[224:239]\n" qL "v110,v110\n" qL "v111,v111\n" \
qJ "v252,v104,v105\n" qJ "v253,v106,v107\n" qJ "v254,v108,v109\n" qJ "v255,v110,v111\n" \
"s_nop 1\n" qB "a[192:207],v[8:11],v[252:255],a[192:207]\n" qB "a[224:239],a[148:151],v[252:255],a[224:239]\n" "s_nop 7\n" \
"s_nop 2\n" qG "v2,a192\n" "v_rcp_f32 v2,v2\n" qG "v192,a224\n" \
qG "v193,a225\n" qG "v194,a226\n" qG "v195,a227\n" qG "v196,a228\n" \
qG "v197,a229\n" qG "v198,a230\n" qG "v199,a231\n" qG "v200,a232\n" \
qG "v201,a233\n" qG "v202,a234\n" qG "v203,a235\n" qG "v204,a236\n" \
qG "v205,a237\n" qG "v206,a238\n" qG "v207,a239\n" "v_mul_f32 v192,v192,v2\n" \
"v_mul_f32 v193,v193,v2\n" "v_mul_f32 v194,v194,v2\n" "v_mul_f32 v195,v195,v2\n" "v_mul_f32 v196,v196,v2\n" \
"v_mul_f32 v197,v197,v2\n" "v_mul_f32 v198,v198,v2\n" "v_mul_f32 v199,v199,v2\n" "v_mul_f32 v200,v200,v2\n" \
"v_mul_f32 v201,v201,v2\n" "v_mul_f32 v202,v202,v2\n" "v_mul_f32 v203,v203,v2\n" "v_mul_f32 v204,v204,v2\n" \
"v_mul_f32 v205,v205,v2\n" "v_mul_f32 v206,v206,v2\n" "v_mul_f32 v207,v207,v2\n" qJ "v32,v192,v193\n" \
qJ "v33,v194,v195\n" qJ "v34,v196,v197\n" qJ "v35,v198,v199\n" qJ "v16,v200,v201\n" \
qJ "v17,v202,v203\n" qJ "v18,v204,v205\n" qJ "v19,v206,v207\n" "v_permlane32_swap_b32 v32,v34\n" \
"v_permlane32_swap_b32 v33,v35\n" "v_permlane32_swap_b32 v16,v18\n" qF "v[32:35],v43,s[84:87],0 offen offset:512 sc1\n" "v_permlane32_swap_b32 v17,v19\n" \
qF "v[16:19],v43,s[84:87],s68 offen offset:512 sc1\n"

#define ATTN_FIRST \
qF "v[128:131],v45,s[84:87],0 offen offset:0 sc1\n" qD "a[160:163],v40 offset:0\n" qD "a[164:167],v41 offset:0\n" qD "a[64:67],v40 offset:32768\n" \
qD "a[68:71],v41 offset:32768\n" qD "a[0:3],v40 offset:16384\n" qD "a[4:7],v41 offset:16384\n" qD "a[168:171],v40 offset:2048\n" \
qD "a[172:175],v41 offset:2048\n" qD "a[8:11],v40 offset:18432\n" qD "a[12:15],v41 offset:18432\n" qD "a[16:19],v40 offset:20480\n" \
"v_cmp_le_i32 s[40:41],0,v44\n" "v_cmp_le_i32 s[42:43],1,v44\n" "v_cmp_le_i32 s[44:45],2,v44\n" "v_cmp_le_i32 s[64:65],3,v44\n" \
"v_mov_b32 v14,0xffff\n" "v_mov_b32 v15,0xffff0000\n" qP "v240,0,v14,s[40:41]\n" qP "v241,0,v15,s[42:43]\n" \
qP "v242,0,v14,s[44:45]\n" qP "v243,0,v15,s[64:65]\n" "v_cmp_le_i32 s[66:67],8,v44\n" "v_cmp_le_i32 s[70:71],9,v44\n" \
"v_cmp_le_i32 s[72:73],10,v44\n" "v_cmp_le_i32 s[76:77],11,v44\n" "v_or_b32 v24,v240,v241\n" "v_or_b32 v25,v242,v243\n" \
qD "a[20:23],v41 offset:20480\n" qP "v240,0,v14,s[66:67]\n" qP "v241,0,v15,s[70:71]\n" qO "lgkmcnt(9)\n" \
qB "v[48:63],a[64:67],a[160:163],0\n" qP "v242,0,v14,s[72:73]\n" qP "v243,0,v15,s[76:77]\n" "v_cmp_le_i32 s[78:79],16,v44\n" \
"v_cmp_le_i32 s[80:81],17,v44\n" "v_cmp_le_i32 s[82:83],18,v44\n" qO "lgkmcnt(3)\n" qB "v[64:79],a[8:11],a[160:163],0\n" \
"v_cmp_le_i32 s[88:89],19,v44\n" "v_or_b32 v26,v240,v241\n" "v_or_b32 v27,v242,v243\n" qP "v240,0,v14,s[78:79]\n" \
qP "v241,0,v15,s[80:81]\n" qB "v[48:63],a[68:71],a[164:167],v[48:63]\n" qP "v242,0,v14,s[82:83]\n" qP "v243,0,v15,s[88:89]\n" \
"v_cmp_le_i32 s[90:91],24,v44\n" "v_cmp_le_i32 s[94:95],26,v44\n" "v_or_b32 v28,v240,v241\n" qO "lgkmcnt(2)\n" \
qB "v[64:79],a[12:15],a[164:167],v[64:79]\n" "v_or_b32 v29,v242,v243\n" "v_mov_b32 v1,0xff800000\n" qP "v240,0,v14,s[90:91]\n" \
qP "v242,0,v14,s[94:95]\n" qD "a[24:27],v40 offset:22528\n" qP "v112,v1,v48,s[40:41]\n" qO "lgkmcnt(2)\n" \
qB "v[80:95],a[16:19],a[160:163],0\n" qP "v113,v1,v49,s[42:43]\n" qP "v114,v1,v50,s[44:45]\n" qP "v115,v1,v51,s[64:65]\n" \
qF "v[132:135],v46,s[84:87],0 offen offset:0 sc1\n" qM "v14,v64,v65,v66\n" qO "lgkmcnt(1)\n" qB "v[80:95],a[20:23],a[164:167],v[80:95]\n" \
qP "v116,v1,v52,s[66:67]\n" qM "v12,v112,v113,v114\n" qM "v14,v14,v67,v68\n" qP "v117,v1,v53,s[70:71]\n" \
qP "v118,v1,v54,s[72:73]\n" qM "v12,v12,v115,v116\n" qM "v14,v14,v69,v70\n" qP "v119,v1,v55,s[76:77]\n" \
qP "v120,v1,v56,s[78:79]\n" qM "v12,v12,v117,v118\n" qM "v14,v14,v71,v72\n" qP "v121,v1,v57,s[80:81]\n" \
qP "v122,v1,v58,s[82:83]\n" qM "v12,v12,v119,v120\n" qM "v14,v14,v73,v74\n" "v_cmp_le_i32 s[92:93],25,v44\n" \
qO "lgkmcnt(0)\n" qB "v[96:111],a[24:27],a[160:163],0\n" qP "v123,v1,v59,s[88:89]\n" qP "v124,v1,v60,s[90:91]\n" \
qM "v12,v12,v121,v122\n" qM "v14,v14,v75,v76\n" qD "a[28:31],v41 offset:22528\n" "v_cmp_le_i32 s[98:99],27,v44\n" \
qP "v125,v1,v61,s[92:93]\n" qP "v126,v1,v62,s[94:95]\n" qM "v12,v12,v123,v124\n" qM "v14,v14,v77,v78\n" \
qP "v127,v1,v63,s[98:99]\n" qM "v12,v12,v125,v126\n" "v_max_f32 v14,v14,v79\n" "v_max_f32 v12,v12,v127\n" \
"v_add_f32 v14,s46,v14\n" qD "a[32:35],v40 offset:24576\n" "v_max_f32 v12,v12,v14\n" qM "v14,v80,v81,v82\n" \
qD "a[36:39],v41 offset:24576\n" qM "v14,v14,v83,v84\n" qD "a[40:43],v40 offset:26624\n" qM "v14,v14,v85,v86\n" \
qD "a[44:47],v41 offset:26624\n" qM "v14,v14,v87,v88\n" qD "a[48:51],v40 offset:28672\n" qM "v14,v14,v89,v90\n" \
qD "a[52:55],v41 offset:28672\n" qM "v14,v14,v91,v92\n" qD "a[56:59],v40 offset:30720\n" qM "v14,v14,v93,v94\n" \
qD "a[60:63],v41 offset:30720\n" "v_max_f32 v14,v14,v95\n" qO "lgkmcnt(8)\n" qB "v[96:111],a[28:31],a[164:167],v[96:111]\n" \
"v_add_f32 v14,s47,v14\n" qD "a[72:75],v40 offset:34816\n" "v_max_f32 v12,v12,v14\n" qD "a[76:79],v41 offset:34816\n" \
qC "a[144:145],v42 offset:16384\n" qC "a[146:147],v42 offset:16896\n" qC "a[148:149],v42 offset:17408\n" qC "a[150:151],v42 offset:17920\n" \
qC "a[80:81],v42 offset:0\n" qF "v[136:139],v45,s[84:87],0 offen offset:256 sc1\n" qO "lgkmcnt(14)\n" qB "v[48:63],a[32:35],a[160:163],0\n" \
qM "v14,v96,v97,v98\n" qC "a[82:83],v42 offset:512\n" qM "v14,v14,v99,v100\n" qC "a[84:85],v42 offset:1024\n" \
qM "v14,v14,v101,v102\n" qC "a[86:87],v42 offset:1536\n" qO "lgkmcnt(15)\n" qB "v[48:63],a[36:39],a[164:167],v[48:63]\n" \
qM "v14,v14,v103,v104\n" qC "a[88:89],v42 offset:2048\n" qM "v14,v14,v105,v106\n" qP "v241,0,v15,s[92:93]\n" \
qM "v14,v14,v107,v108\n" qP "v243,0,v15,s[98:99]\n" qB "v[64:79],a[40:43],a[160:163],0\n" qM "v14,v14,v109,v110\n" \
qC "a[90:91],v42 offset:2560\n" "v_max_f32 v14,v14,v111\n" "v_or_b32 v30,v240,v241\n" "v_add_f32 v14,s48,v14\n" \
"v_or_b32 v31,v242,v243\n" qO "lgkmcnt(15)\n" qB "v[64:79],a[44:47],a[164:167],v[64:79]\n" "v_max_f32 v12,v12,v14\n" \
qM "v14,v48,v49,v50\n" qC "a[92:93],v42 offset:3072\n" qM "v14,v14,v51,v52\n" "v_mov_b32 v8,0x3f803f80\n" \
qM "v14,v14,v53,v54\n" qB "v[80:95],a[48:51],a[160:163],0\n" qM "v14,v14,v55,v56\n" "v_mov_b32 v9,0x3f803f80\n" \
qM "v14,v14,v57,v58\n" "v_mov_b32 v10,0x3f803f80\n" qM "v14,v14,v59,v60\n" qO "lgkmcnt(15)\n" \
qB "v[80:95],a[52:55],a[164:167],v[80:95]\n" qM "v14,v14,v61,v62\n" "v_mov_b32 v11,0x3f803f80\n" "v_max_f32 v14,v14,v63\n" \
qC "a[94:95],v42 offset:3584\n" "v_add_f32 v14,s49,v14\n" qC "a[96:97],v42 offset:4096\n" qO "lgkmcnt(15)\n" \
qB "v[96:111],a[56:59],a[160:163],0\n" "v_max_f32 v12,v12,v14\n" qM "v14,v64,v65,v66\n" qC "a[98:99],v42 offset:4608\n" \
qM "v14,v14,v67,v68\n" qC "a[100:101],v42 offset:5120\n" qM "v14,v14,v69,v70\n" qB "v[96:111],a[60:63],a[164:167],v[96:111]\n" \
qM "v14,v14,v71,v72\n" qC "a[102:103],v42 offset:5632\n" qM "v14,v14,v73,v74\n" qC "a[104:105],v42 offset:6144\n" \
qM "v14,v14,v75,v76\n" qC "a[106:107],v42 offset:6656\n" qM "v14,v14,v77,v78\n" qC "a[108:109],v42 offset:7168\n" \
"v_max_f32 v14,v14,v79\n" qF "v[140:143],v47,s[84:87],0 offen offset:256 sc1\n" "v_add_f32 v14,s50,v14\n" qC "a[110:111],v42 offset:7680\n" \
"v_max_f32 v12,v12,v14\n" qM "v14,v80,v81,v82\n" qC "a[112:113],v42 offset:8192\n" qM "v14,v14,v83,v84\n" \
qC "a[114:115],v42 offset:8704\n" qM "v14,v14,v85,v86\n" qC "a[116:117],v42 offset:9216\n" qM "v14,v14,v87,v88\n" \
qC "a[118:119],v42 offset:9728\n" qM "v14,v14,v89,v90\n" qC "a[120:121],v42 offset:10240\n" qM "v14,v14,v91,v92\n" \
qC "a[122:123],v42 offset:10752\n" qM "v14,v14,v93,v94\n" qC "a[124:125],v42 offset:11264\n" "v_max_f32 v14,v14,v95\n" \
qC "a[126:127],v42 offset:11776\n" "v_add_f32 v14,s51,v14\n" qC "a[128:129],v42 offset:12288\n" "v_max_f32 v12,v12,v14\n" \
qM "v14,v96,v97,v98\n" qC "a[130:131],v42 offset:12800\n" qM "v14,v14,v99,v100\n" qC "a[132:133],v42 offset:13312\n" \
qM "v14,v14,v101,v102\n" qC "a[134:135],v42 offset:13824\n" qM "v14,v14,v103,v104\n" qC "a[136:137],v42 offset:14336\n" \
qM "v14,v14,v105,v106\n" qC "a[138:139],v42 offset:14848\n" qM "v14,v14,v107,v108\n" qC "a[140:141],v42 offset:15360\n" \
qM "v14,v14,v109,v110\n" qC "a[142:143],v42 offset:15872\n" "v_max_f32 v14,v14,v111\n" qC "a[152:153],v42 offset:18432\n" \
"v_add_f32 v14,s52,v14\n" qC "a[154:155],v42 offset:18944\n" "v_max_f32 v12,v12,v14\n" qC "a[156:157],v42 offset:19456\n" \
"v_mov_b32 v14,v12\n" qC "a[158:159],v42 offset:19968\n" "s_nop 0\n" "v_permlane32_swap_b32 v12,v14\n" \
"v_max_f32 v12,v12,v14\n" "v_mul_f32 v208,-1.0,v12\n" "v_mul_f32 v209,-1.0,v12\n" "v_mul_f32 v210,-1.0,v12\n" \
"v_mul_f32 v211,-1.0,v12\n" "v_mul_f32 v212,-1.0,v12\n" "v_mul_f32 v213,-1.0,v12\n" "v_mul_f32 v214,-1.0,v12\n" \
"v_mul_f32 v215,-1.0,v12\n" "v_mul_f32 v216,-1.0,v12\n" "v_mul_f32 v217,-1.0,v12\n" "v_mul_f32 v218,-1.0,v12\n" \
"v_mul_f32 v219,-1.0,v12\n" "v_mul_f32 v220,-1.0,v12\n" "v_mul_f32 v221,-1.0,v12\n" qF "v[144:147],v45,s[84:87],0 offen offset:512 sc1\n" \
"v_mul_f32 v222,-1.0,v12\n" "v_mul_f32 v223,-1.0,v12\n" "v_add_f32 v112,v112,v208\n" "v_add_f32 v113,v113,v208\n" \
qB "v[48:63],a[8:11],a[160:163],v[208:223]\n" "v_add_f32 v114,v114,v208\n" "v_add_f32 v115,v115,v208\n" "v_add_f32 v116,v116,v208\n" \
"v_add_f32 v117,v117,v208\n" "v_add_f32 v118,v118,v208\n" qB "v[48:63],a[12:15],a[164:167],v[48:63]\n" "v_add_f32 v119,v119,v208\n" \
qL "v112,v112\n" qL "v113,v113\n" qL "v114,v114\n" qB "v[64:79],a[16:19],a[160:163],v[208:223]\n" \
qL "v115,v115\n" qL "v116,v116\n" qL "v117,v117\n" qL "v118,v118\n" \
qB "v[96:111],a[32:35],a[160:163],v[208:223]\n" qL "v119,v119\n" qJ "v240,v112,v113\n" qJ "v241,v114,v115\n" \
qJ "v242,v116,v117\n" qJ "v243,v118,v119\n" qB "v[64:79],a[20:23],a[164:167],v[64:79]\n" qL "v48,v48\n" \
qL "v49,v49\n" qL "v50,v50\n" qL "v51,v51\n" qB "a[176:191],v[8:11],v[240:243],0\n" \
qL "v52,v52\n" qL "v53,v53\n" qL "v54,v54\n" qL "v55,v55\n" \
qB "v[96:111],a[36:39],a[164:167],v[96:111]\n" qL "v56,v56\n" qL "v57,v57\n" qL "v58,v58\n" \
qL "v59,v59\n" qB "v[80:95],a[24:27],a[160:163],v[208:223]\n" qL "v60,v60\n" qF "v[148:151],v46,s[84:87],0 offen offset:512 sc1\n" \
qL "v61,v61\n" qL "v62,v62\n" qB "v[80:95],a[28:31],a[164:167],v[80:95]\n" qL "v63,v63\n" \
"v_add_f32 v120,v120,v208\n" "v_add_f32 v121,v121,v208\n" "v_add_f32 v122,v122,v208\n" "v_add_f32 v123,v123,v208\n" \
"v_add_f32 v124,v124,v208\n" "v_add_f32 v125,v125,v208\n" "v_add_f32 v126,v126,v208\n" "v_add_f32 v127,v127,v208\n" \
"v_and_b32 v248,v240,v24\n" "v_and_b32 v249,v241,v25\n" "v_and_b32 v250,v242,v26\n" "v_and_b32 v251,v243,v27\n" \
qL "v120,v120\n" qL "v121,v121\n" qO "lgkmcnt(15)\n" qB "a[208:223],a[144:147],v[248:251],0\n" \
qL "v122,v122\n" qL "v123,v123\n" qL "v124,v124\n" qL "v125,v125\n" \
qL "v126,v126\n" qL "v127,v127\n" qJ "v32,v48,v49\n" qJ "v33,v50,v51\n" \
qJ "v34,v52,v53\n" qJ "v35,v54,v55\n" qJ "v36,v56,v57\n" qJ "v37,v58,v59\n" \
qJ "v38,v60,v61\n" qJ "v39,v62,v63\n" qB "v[48:63],a[40:43],a[160:163],v[208:223]\n" qJ "v244,v120,v121\n" \
qJ "v245,v122,v123\n" qJ "v246,v124,v125\n" qJ "v247,v126,v127\n" "v_and_b32 v252,v244,v28\n" \
qB "v[48:63],a[44:47],a[164:167],v[48:63]\n" "v_and_b32 v253,v245,v29\n" "v_and_b32 v254,v246,v30\n" "v_and_b32 v255,v247,v31\n" \
"v_and_b32 v32,s55,v32\n" "v_and_b32 v33,s55,v33\n" qB "a[176:191],v[8:11],v[244:247],a[176:191]\n" "v_and_b32 v34,s55,v34\n" \
"v_and_b32 v35,s55,v35\n" "v_and_b32 v36,s55,v36\n" "v_and_b32 v37,s55,v37\n" qF "v[152:155],v45,s[84:87],0 offen offset:768 sc1\n" \
qB "a[208:223],a[148:151],v[252:255],a[208:223]\n" "v_and_b32 v38,s55,v38\n" "v_and_b32 v39,s55,v39\n" qL "v64,v64\n" \
qL "v65,v65\n" qB "a[176:191],v[8:11],v[32:35],a[176:191]\n" qL "v66,v66\n" qL "v67,v67\n" \
qL "v68,v68\n" qL "v69,v69\n" qB "a[208:223],a[88:91],v[32:35],a[208:223]\n" qL "v70,v70\n" \
qL "v71,v71\n" qL "v72,v72\n" qL "v73,v73\n" qB "a[176:191],v[8:11],v[36:39],a[176:191]\n" \
qL "v74,v74\n" qL "v75,v75\n" qL "v76,v76\n" qL "v77,v77\n" \
qB "a[208:223],a[92:95],v[36:39],a[208:223]\n" qL "v78,v78\n" qL "v79,v79\n" qL "v96,v96\n" \
qL "v97,v97\n" qL "v98,v98\n" qL "v99,v99\n" qL "v100,v100\n" \
qL "v101,v101\n" qL "v102,v102\n" qL "v103,v103\n" qL "v104,v104\n" \
qL "v105,v105\n" qL "v106,v106\n" qL "v107,v107\n" qL "v108,v108\n" \
qL "v109,v109\n" qL "v110,v110\n" qF "v[156:159],v47,s[84:87],0 offen offset:768 sc1\n" qL "v111,v111\n" \
qL "v48,v48\n" qL "v49,v49\n" qL "v50,v50\n" qL "v51,v51\n" \
qL "v52,v52\n" qL "v53,v53\n" qL "v54,v54\n" qL "v55,v55\n" \
qL "v56,v56\n" qL "v57,v57\n" qL "v58,v58\n" qL "v59,v59\n" \
qL "v60,v60\n" qL "v61,v61\n" qL "v62,v62\n" qL "v63,v63\n" \
qJ "v16,v64,v65\n" qJ "v17,v66,v67\n" qJ "v18,v68,v69\n" qJ "v19,v70,v71\n" \
qJ "v20,v72,v73\n" qJ "v21,v74,v75\n" qJ "v22,v76,v77\n" qJ "v23,v78,v79\n" \
qB "v[64:79],a[48:51],a[160:163],v[208:223]\n" qJ "v248,v96,v97\n" qJ "v249,v98,v99\n" qJ "v250,v100,v101\n" \
qJ "v251,v102,v103\n" qJ "v252,v104,v105\n" qB "v[64:79],a[52:55],a[164:167],v[64:79]\n" qJ "v253,v106,v107\n" \
qJ "v254,v108,v109\n" qJ "v255,v110,v111\n" qJ "v32,v48,v49\n" qJ "v33,v50,v51\n" \
qB "v[96:111],a[72:75],a[168:171],0\n" qJ "v34,v52,v53\n" qJ "v35,v54,v55\n" qJ "v36,v56,v57\n" \
qJ "v37,v58,v59\n" qJ "v38,v60,v61\n" qB "v[96:111],a[76:79],a[172:175],v[96:111]\n" qJ "v39,v62,v63\n" \
"v_and_b32 v16,s56,v16\n" "v_and_b32 v17,s56,v17\n" qF "v[160:163],v45,s[84:87],0 offen offset:1024 sc1\n" "v_and_b32 v18,s56,v18\n" \
qB "v[48:63],a[16:19],a[168:171],0\n" "v_and_b32 v19,s56,v19\n" "v_and_b32 v20,s56,v20\n" "v_and_b32 v21,s56,v21\n" \
"v_and_b32 v22,s56,v22\n" "v_and_b32 v23,s56,v23\n" qB "v[48:63],a[20:23],a[172:175],v[48:63]\n" qL "v80,v80\n" \
qL "v81,v81\n" qL "v82,v82\n" qL "v83,v83\n" qB "a[176:191],v[8:11],v[16:19],a[176:191]\n" \
qL "v84,v84\n" qL "v85,v85\n" qL "v86,v86\n" qL "v87,v87\n" \
qB "a[208:223],a[96:99],v[16:19],a[208:223]\n" qL "v88,v88\n" qL "v89,v89\n" qL "v90,v90\n" \
qL "v91,v91\n" qB "a[176:191],v[8:11],v[20:23],a[176:191]\n" qL "v92,v92\n" qL "v93,v93\n" \
qL "v94,v94\n" qL "v95,v95\n" qB "a[208:223],a[100:103],v[20:23],a[208:223]\n" qL "v64,v64\n" \
qL "v65,v65\n" qL "v66,v66\n" qL "v67,v67\n" qL "v68,v68\n" \
qL "v69,v69\n" qL "v70,v70\n" qL "v71,v71\n" qL "v72,v72\n" \
qL "v73,v73\n" qL "v74,v74\n" qL "v75,v75\n" qF "v[164:167],v46,s[84:87],0 offen offset:1024 sc1\n" \
qL "v76,v76\n" qL "v77,v77\n" qL "v78,v78\n" qL "v79,v79\n" \
qP "v192,v1,v96,s[40:41]\n" qP "v193,v1,v97,s[42:43]\n" qP "v194,v1,v98,s[44:45]\n" qM "v15,v48,v49,v50\n" \
qP "v195,v1,v99,s[64:65]\n" qP "v196,v1,v100,s[66:67]\n" qM "v13,v192,v193,v194\n" qM "v15,v15,v51,v52\n" \
qJ "v240,v80,v81\n" qJ "v241,v82,v83\n" qJ "v242,v84,v85\n" qJ "v243,v86,v87\n" \
qJ "v244,v88,v89\n" qJ "v245,v90,v91\n" qJ "v246,v92,v93\n" qJ "v247,v94,v95\n" \
qB "v[80:95],a[56:59],a[160:163],v[208:223]\n" qJ "v16,v64,v65\n" qJ "v17,v66,v67\n" qJ "v18,v68,v69\n" \
qJ "v19,v70,v71\n" qJ "v20,v72,v73\n" qB "v[80:95],a[60:63],a[164:167],v[80:95]\n" qJ "v21,v74,v75\n" \
qJ "v22,v76,v77\n" qJ "v23,v78,v79\n" qP "v197,v1,v101,s[70:71]\n" qP "v198,v1,v102,s[72:73]\n" \
qB "v[64:79],a[24:27],a[168:171],0\n" qM "v13,v13,v195,v196\n" qM "v15,v15,v53,v54\n" qP "v199,v1,v103,s[76:77]\n" \
qP "v200,v1,v104,s[78:79]\n" qM "v13,v13,v197,v198\n" qB "v[64:79],a[28:31],a[172:175],v[64:79]\n" qM "v15,v15,v55,v56\n" \
qP "v201,v1,v105,s[80:81]\n" qP "v202,v1,v106,s[82:83]\n" qM "v13,v13,v199,v200\n" qM "v15,v15,v57,v58\n" \
qP "v203,v1,v107,s[88:89]\n" qP "v204,v1,v108,s[90:91]\n" qM "v13,v13,v201,v202\n" qM "v15,v15,v59,v60\n" \
"v_and_b32 v240,s57,v240\n" "v_and_b32 v241,s57,v241\n" "v_and_b32 v242,s57,v242\n" "v_and_b32 v243,s57,v243\n" \
qP "v205,v1,v109,s[92:93]\n" qP "v206,v1,v110,s[94:95]\n" qB "a[176:191],v[8:11],v[240:243],a[176:191]\n" qF "v[168:171],v45,s[84:87],0 offen offset:1280 sc1\n" \
qM "v13,v13,v203,v204\n" qM "v15,v15,v61,v62\n" qP "v207,v1,v111,s[98:99]\n" qM "v13,v13,v205,v206\n" \
qB "a[208:223],a[104:107],v[240:243],a[208:223]\n" "v_max_f32 v15,v15,v63\n" "v_max_f32 v13,v13,v207\n" "v_add_f32 v15,s47,v15\n" \
"v_and_b32 v244,s57,v244\n" "v_and_b32 v245,s57,v245\n" qB "v[96:111],a[40:43],a[168:171],0\n" "v_and_b32 v246,s57,v246\n" \
"v_and_b32 v247,s57,v247\n" "v_max_f32 v13,v13,v15\n" qL "v80,v80\n" qL "v81,v81\n" \
qB "a[176:191],v[8:11],v[244:247],a[176:191]\n" qL "v82,v82\n" qL "v83,v83\n" qL "v84,v84\n" \
qL "v85,v85\n" qB "a[208:223],a[108:111],v[244:247],a[208:223]\n" qL "v86,v86\n" qL "v87,v87\n" \
qL "v88,v88\n" qL "v89,v89\n" qB "v[96:111],a[44:47],a[172:175],v[96:111]\n" qL "v90,v90\n" \
qL "v91,v91\n" qL "v92,v92\n" qL "v93,v93\n" qB "v[48:63],a[48:51],a[168:171],0\n" \
qL "v94,v94\n" qL "v95,v95\n" qM "v15,v64,v65,v66\n" qJ "v240,v80,v81\n" \
qB "v[48:63],a[52:55],a[172:175],v[48:63]\n" qM "v15,v15,v67,v68\n" qJ "v241,v82,v83\n" qJ "v242,v84,v85\n" \
qJ "v243,v86,v87\n" qJ "v244,v88,v89\n" qJ "v245,v90,v91\n" qJ "v246,v92,v93\n" \
qJ "v247,v94,v95\n" qB "v[80:95],a[32:35],a[168:171],0\n" qF "v[172:175],v47,s[84:87],0 offen offset:1280 sc1\n" qM "v15,v15,v69,v70\n" \
"v_and_b32 v248,s58,v248\n" qM "v15,v15,v71,v72\n" "v_and_b32 v249,s58,v249\n" qB "v[80:95],a[36:39],a[172:175],v[80:95]\n" \
qM "v15,v15,v73,v74\n" "v_and_b32 v250,s58,v250\n" qM "v15,v15,v75,v76\n" "v_and_b32 v251,s58,v251\n" \
qM "v15,v15,v77,v78\n" "v_and_b32 v252,s58,v252\n" qB "a[176:191],v[8:11],v[248:251],a[176:191]\n" "v_max_f32 v15,v15,v79\n" \
"v_and_b32 v253,s58,v253\n" "v_add_f32 v15,s48,v15\n" "v_and_b32 v254,s58,v254\n" "v_max_f32 v13,v13,v15\n" \
qB "v[64:79],a[56:59],a[168:171],0\n" qM "v15,v80,v81,v82\n" "v_and_b32 v255,s58,v255\n" qM "v15,v15,v83,v84\n" \
"v_and_b32 v32,s59,v32\n" qM "v15,v15,v85,v86\n" qB "v[64:79],a[60:63],a[172:175],v[64:79]\n" qM "v15,v15,v87,v88\n" \
"v_and_b32 v33,s59,v33\n" qM "v15,v15,v89,v90\n" "v_and_b32 v34,s59,v34\n" qM "v15,v15,v91,v92\n" \
qB "a[208:223],a[112:115],v[248:251],a[208:223]\n" qM "v15,v15,v93,v94\n" "v_and_b32 v35,s59,v35\n" "v_max_f32 v15,v15,v95\n" \
"v_and_b32 v36,s59,v36\n" "v_add_f32 v15,s49,v15\n" qB "v[80:95],a[64:67],a[168:171],0\n" "v_max_f32 v13,v13,v15\n" \
qM "v15,v96,v97,v98\n" "v_and_b32 v37,s59,v37\n" qM "v15,v15,v99,v100\n" "v_and_b32 v38,s59,v38\n" \
qB "a[176:191],v[8:11],v[252:255],a[176:191]\n" qM "v15,v15,v101,v102\n" "v_and_b32 v39,s59,v39\n" qM "v15,v15,v103,v104\n" \
"v_and_b32 v16,s60,v16\n" qM "v15,v15,v105,v106\n" qB "v[80:95],a[68:71],a[172:175],v[80:95]\n" qM "v15,v15,v107,v108\n" \
"v_and_b32 v17,s60,v17\n" qM "v15,v15,v109,v110\n" "v_and_b32 v18,s60,v18\n" "v_max_f32 v15,v15,v111\n" \
qB "a[208:223],a[116:119],v[252:255],a[208:223]\n" "v_add_f32 v15,s50,v15\n" qF "v[176:179],v45,s[84:87],0 offen offset:1536 sc1\n" "v_max_f32 v13,v13,v15\n" \
qM "v15,v48,v49,v50\n" "v_and_b32 v19,s60,v19\n" qB "a[176:191],v[8:11],v[32:35],a[176:191]\n" qM "v15,v15,v51,v52\n" \
"v_and_b32 v20,s60,v20\n" qM "v15,v15,v53,v54\n" "v_and_b32 v21,s60,v21\n" qM "v15,v15,v55,v56\n" \
qO "lgkmcnt(14)\n" qB "a[208:223],a[120:123],v[32:35],a[208:223]\n" qM "v15,v15,v57,v58\n" "v_and_b32 v22,s60,v22\n" \
qM "v15,v15,v59,v60\n" "v_and_b32 v23,s60,v23\n" qM "v15,v15,v61,v62\n" qB "a[176:191],v[8:11],v[36:39],a[176:191]\n" \
"v_max_f32 v15,v15,v63\n" "v_and_b32 v240,s61,v240\n" "v_add_f32 v15,s51,v15\n" "v_and_b32 v241,s61,v241\n" \
"v_max_f32 v13,v13,v15\n" qO "lgkmcnt(12)\n" qB "a[208:223],a[124:127],v[36:39],a[208:223]\n" qM "v15,v64,v65,v66\n" \
"v_and_b32 v242,s61,v242\n" qM "v15,v15,v67,v68\n" "v_and_b32 v243,s61,v243\n" qM "v15,v15,v69,v70\n" \
qB "a[176:191],v[8:11],v[16:19],a[176:191]\n" qM "v15,v15,v71,v72\n" "v_and_b32 v244,s61,v244\n" qM "v15,v15,v73,v74\n" \
"v_and_b32 v245,s61,v245\n" qM "v15,v15,v75,v76\n" qO "lgkmcnt(10)\n" qB "a[208:223],a[128:131],v[16:19],a[208:223]\n" \
qM "v15,v15,v77,v78\n" "v_and_b32 v246,s61,v246\n" "v_max_f32 v15,v15,v79\n" "v_and_b32 v247,s61,v247\n" \
"v_add_f32 v15,s52,v15\n" qB "a[176:191],v[8:11],v[20:23],a[176:191]\n" "v_max_f32 v13,v13,v15\n" qM "v15,v80,v81,v82\n" \
qM "v15,v15,v83,v84\n" qM "v15,v15,v85,v86\n" qO "lgkmcnt(8)\n" qB "a[208:223],a[132:135],v[20:23],a[208:223]\n" \
qM "v15,v15,v87,v88\n" qM "v15,v15,v89,v90\n" qM "v15,v15,v91,v92\n" qB "a[176:191],v[8:11],v[240:243],a[176:191]\n" \
qM "v15,v15,v93,v94\n" "v_max_f32 v15,v15,v95\n" qF "v[180:183],v46,s[84:87],0 offen offset:1536 sc1\n" "v_add_f32 v15,s53,v15\n" \
qO "lgkmcnt(6)\n" qB "a[208:223],a[136:139],v[240:243],a[208:223]\n" "v_max_f32 v13,v13,v15\n" "v_mov_b32 v15,v13\n" \
"s_nop 1\n" "v_permlane32_swap_b32 v13,v15\n" qB "a[176:191],v[8:11],v[244:247],a[176:191]\n" "v_max_f32 v13,v13,v15\n" \
"v_mul_f32 v224,-1.0,v13\n" "v_mul_f32 v225,-1.0,v13\n" "v_mul_f32 v226,-1.0,v13\n" "v_mul_f32 v227,-1.0,v13\n" \
qO "lgkmcnt(4)\n" qB "a[208:223],a[140:143],v[244:247],a[208:223]\n" "v_mul_f32 v228,-1.0,v13\n" "v_mul_f32 v229,-1.0,v13\n" \
"v_mul_f32 v230,-1.0,v13\n" "v_mul_f32 v231,-1.0,v13\n" "v_mul_f32 v232,-1.0,v13\n" "v_mul_f32 v233,-1.0,v13\n" \
"v_mul_f32 v234,-1.0,v13\n" "v_mul_f32 v235,-1.0,v13\n" "v_mul_f32 v236,-1.0,v13\n" "v_mul_f32 v237,-1.0,v13\n" \
"v_mul_f32 v238,-1.0,v13\n" "v_mul_f32 v239,-1.0,v13\n" qG "v2,a176\n" "v_add_f32 v192,v192,v224\n" \
qB "v[96:111],a[16:19],a[168:171],v[224:239]\n" "v_add_f32 v193,v193,v224\n" "v_add_f32 v194,v194,v224\n" "v_add_f32 v195,v195,v224\n" \
"v_add_f32 v196,v196,v224\n" "v_add_f32 v197,v197,v224\n" qB "v[96:111],a[20:23],a[172:175],v[96:111]\n" "v_add_f32 v198,v198,v224\n" \
"v_add_f32 v199,v199,v224\n" "v_rcp_f32 v2,v2\n" qG "v112,a208\n" qG "v113,a209\n" \
qB "v[48:63],a[24:27],a[168:171],v[224:239]\n" qG "v114,a210\n" qG "v115,a211\n" qG "v116,a212\n" \
qG "v117,a213\n" qG "v118,a214\n" qB "v[48:63],a[28:31],a[172:175],v[48:63]\n" qG "v119,a215\n" \
qL "v192,v192\n" qL "v193,v193\n" qL "v194,v194\n" qF "v[184:187],v45,s[84:87],0 offen offset:1792 sc1\n" \
qB "v[64:79],a[32:35],a[168:171],v[224:239]\n" qL "v195,v195\n" qL "v196,v196\n" qL "v197,v197\n" \
qL "v198,v198\n" qB "v[64:79],a[36:39],a[172:175],v[64:79]\n" qL "v199,v199\n" "v_mul_f32 v112,v112,v2\n" \
"v_mul_f32 v113,v113,v2\n" "v_mul_f32 v114,v114,v2\n" "v_mul_f32 v115,v115,v2\n" qB "v[80:95],a[40:43],a[168:171],v[224:239]\n" \
"v_mul_f32 v116,v116,v2\n" "v_mul_f32 v117,v117,v2\n" "v_mul_f32 v118,v118,v2\n" "v_mul_f32 v119,v119,v2\n" \
qJ "v16,v192,v193\n" qB "v[80:95],a[44:47],a[172:175],v[80:95]\n" qJ "v17,v194,v195\n" qJ "v18,v196,v197\n" \
qJ "v19,v198,v199\n" "v_add_f32 v200,v200,v224\n" "v_add_f32 v201,v201,v224\n" qB "a[192:207],v[8:11],v[16:19],0\n" \
"v_add_f32 v202,v202,v224\n" "v_add_f32 v203,v203,v224\n" "v_add_f32 v204,v204,v224\n" "v_add_f32 v205,v205,v224\n" \
"v_add_f32 v206,v206,v224\n" "v_add_f32 v207,v207,v224\n" qJ "v248,v112,v113\n" qJ "v249,v114,v115\n" \
qJ "v250,v116,v117\n" qJ "v251,v118,v119\n" qL "v200,v200\n" qL "v201,v201\n" \
qL "v202,v202\n" qL "v203,v203\n" qL "v204,v204\n" qL "v205,v205\n" \
qL "v206,v206\n" qL "v207,v207\n" "v_permlane32_swap_b32 v248,v250\n" "v_permlane32_swap_b32 v249,v251\n" \
"v_and_b32 v240,v16,v24\n" "v_and_b32 v241,v17,v25\n" "v_and_b32 v242,v18,v26\n" "v_and_b32 v243,v19,v27\n" \
qF "v[248:251],v43,s[84:87],0 offen offset:0 sc1\n" qJ "v20,v200,v201\n" qO "lgkmcnt(2)\n" qB "a[224:239],a[152:155],v[240:243],0\n" \
qJ "v21,v202,v203\n" qJ "v22,v204,v205\n" qJ "v23,v206,v207\n" qL "v96,v96\n" \
qL "v97,v97\n" qB "a[192:207],v[8:11],v[20:23],a[192:207]\n" qL "v98,v98\n" qL "v99,v99\n" \
qL "v100,v100\n" qL "v101,v101\n" qL "v102,v102\n" qL "v103,v103\n" \
qG "v120,a216\n" qG "v121,a217\n" qG "v122,a218\n" qF "v[188:191],v47,s[84:87],0 offen offset:1792 sc1\n" \
qG "v123,a219\n" qG "v124,a220\n" qG "v125,a221\n" qG "v126,a222\n" \
qG "v127,a223\n" "v_and_b32 v244,v20,v28\n" "v_and_b32 v245,v21,v29\n" "v_and_b32 v246,v22,v30\n" \
"v_and_b32 v247,v23,v31\n" qJ "v248,v96,v97\n" qJ "v249,v98,v99\n" qO "lgkmcnt(0)\n" \
qB "a[224:239],a[156:159],v[244:247],a[224:239]\n" qJ "v250,v100,v101\n" qJ "v251,v102,v103\n" "v_mul_f32 v120,v120,v2\n" \
"v_mul_f32 v121,v121,v2\n" "v_mul_f32 v122,v122,v2\n" "v_mul_f32 v123,v123,v2\n" "v_mul_f32 v124,v124,v2\n" \
"v_mul_f32 v125,v125,v2\n" "v_mul_f32 v126,v126,v2\n" "v_mul_f32 v127,v127,v2\n" "v_and_b32 v248,s56,v248\n" \
"v_and_b32 v249,s56,v249\n" "v_and_b32 v250,s56,v250\n" "v_and_b32 v251,s56,v251\n" qL "v104,v104\n" \
qL "v105,v105\n" qB "a[192:207],v[8:11],v[248:251],a[192:207]\n" qL "v106,v106\n" qL "v107,v107\n" \
qL "v108,v108\n" qL "v109,v109\n" qB "a[224:239],a[96:99],v[248:251],a[224:239]\n" qL "v110,v110\n" \
qL "v111,v111\n" qJ "v32,v120,v121\n" qJ "v33,v122,v123\n" qJ "v34,v124,v125\n" \
qJ "v35,v126,v127\n" qJ "v252,v104,v105\n" "v_permlane32_swap_b32 v32,v34\n" "v_permlane32_swap_b32 v33,v35\n" \
qJ "v253,v106,v107\n" qJ "v254,v108,v109\n" qJ "v255,v110,v111\n" qB "v[96:111],a[48:51],a[168:171],v[224:239]\n" \
"v_and_b32 v252,s56,v252\n" "v_and_b32 v253,s56,v253\n" "v_and_b32 v254,s56,v254\n" "v_and_b32 v255,s56,v255\n" \
qF "v[32:35],v43,s[84:87],s68 offen offset:0 sc1\n" qB "v[96:111],a[52:55],a[172:175],v[96:111]\n" qL "v48,v48\n" qL "v49,v49\n" \
qL "v50,v50\n" qL "v51,v51\n" qB "a[192:207],v[8:11],v[252:255],a[192:207]\n" qL "v52,v52\n" \
qL "v53,v53\n" qL "v54,v54\n" qL "v55,v55\n" qB "a[224:239],a[100:103],v[252:255],a[224:239]\n" \
qJ "v32,v48,v49\n" qJ "v33,v50,v51\n" qJ "v34,v52,v53\n" qJ "v35,v54,v55\n" \
"v_and_b32 v32,s57,v32\n" "v_and_b32 v33,s57,v33\n" "v_and_b32 v34,s57,v34\n" "v_and_b32 v35,s57,v35\n" \
qL "v56,v56\n" qL "v57,v57\n" qB "a[192:207],v[8:11],v[32:35],a[192:207]\n" qL "v58,v58\n" \
qL "v59,v59\n" qL "v60,v60\n" qL "v61,v61\n" qB "a[224:239],a[104:107],v[32:35],a[224:239]\n" \
qL "v62,v62\n" qL "v63,v63\n" qJ "v36,v56,v57\n" qJ "v37,v58,v59\n" \
qJ "v38,v60,v61\n" qJ "v39,v62,v63\n" qB "v[48:63],a[56:59],a[168:171],v[224:239]\n" "v_and_b32 v36,s57,v36\n" \
"v_and_b32 v37,s57,v37\n" "v_and_b32 v38,s57,v38\n" "v_and_b32 v39,s57,v39\n" qL "v64,v64\n" \
qB "v[48:63],a[60:63],a[172:175],v[48:63]\n" qL "v65,v65\n" qL "v66,v66\n" qL "v67,v67\n" \
qL "v68,v68\n" qB "a[192:207],v[8:11],v[36:39],a[192:207]\n" qL "v69,v69\n" qL "v70,v70\n" \
qL "v71,v71\n" qJ "v16,v64,v65\n" qB "a[224:239],a[108:111],v[36:39],a[224:239]\n" qJ "v17,v66,v67\n" \
qJ "v18,v68,v69\n" qJ "v19,v70,v71\n" "v_and_b32 v16,s58,v16\n" "v_and_b32 v17,s58,v17\n" \
"v_and_b32 v18,s58,v18\n" "v_and_b32 v19,s58,v19\n" qL "v72,v72\n" qL "v73,v73\n" \
qB "a[192:207],v[8:11],v[16:19],a[192:207]\n" qL "v74,v74\n" qL "v75,v75\n" qL "v76,v76\n" \
qL "v77,v77\n" qB "a[224:239],a[112:115],v[16:19],a[224:239]\n" qL "v78,v78\n" qL "v79,v79\n" \
qJ "v20,v72,v73\n" qJ "v21,v74,v75\n" qJ "v22,v76,v77\n" qJ "v23,v78,v79\n" \
qB "v[64:79],a[64:67],a[168:171],v[224:239]\n" "v_and_b32 v20,s58,v20\n" "v_and_b32 v21,s58,v21\n" "v_and_b32 v22,s58,v22\n" \
"v_and_b32 v23,s58,v23\n" qL "v80,v80\n" qB "v[64:79],a[68:71],a[172:175],v[64:79]\n" qL "v81,v81\n" \
qL "v82,v82\n" qL "v83,v83\n" qL "v84,v84\n" qB "a[192:207],v[8:11],v[20:23],a[192:207]\n" \
qL "v85,v85\n" qL "v86,v86\n" qL "v87,v87\n" qJ "v240,v80,v81\n" \
qB "a[224:239],a[116:119],v[20:23],a[224:239]\n" qJ "v241,v82,v83\n" qJ "v242,v84,v85\n" qJ "v243,v86,v87\n" \
"v_and_b32 v240,s59,v240\n" "v_and_b32 v241,s59,v241\n" "v_and_b32 v242,s59,v242\n" "v_and_b32 v243,s59,v243\n" \
qL "v88,v88\n" qL "v89,v89\n" qB "a[192:207],v[8:11],v[240:243],a[192:207]\n" qL "v90,v90\n" \
qL "v91,v91\n" qL "v92,v92\n" qL "v93,v93\n" qB "a[224:239],a[120:123],v[240:243],a[224:239]\n" \
qL "v94,v94\n" qL "v95,v95\n" qJ "v244,v88,v89\n" qJ "v245,v90,v91\n" \
qJ "v246,v92,v93\n" qJ "v247,v94,v95\n" "v_and_b32 v244,s59,v244\n" "v_and_b32 v245,s59,v245\n" \
"v_and_b32 v246,s59,v246\n" "v_and_b32 v247,s59,v247\n" qL "v96,v96\n" qL "v97,v97\n" \
qB "a[192:207],v[8:11],v[244:247],a[192:207]\n" qL "v98,v98\n" qL "v99,v99\n" qL "v100,v100\n" \
qL "v101,v101\n" qB "a[224:239],a[124:127],v[244:247],a[224:239]\n" qL "v102,v102\n" qL "v103,v103\n" \
qJ "v248,v96,v97\n" qJ "v249,v98,v99\n" qJ "v250,v100,v101\n" qJ "v251,v102,v103\n" \
"v_and_b32 v248,s60,v248\n" "v_and_b32 v249,s60,v249\n" "v_and_b32 v250,s60,v250\n" "v_and_b32 v251,s60,v251\n" \
qL "v104,v104\n" qL "v105,v105\n" qB "a[192:207],v[8:11],v[248:251],a[192:207]\n" qL "v106,v106\n" \
qL "v107,v107\n" qL "v108,v108\n" qL "v109,v109\n" qB "a[224:239],a[128:131],v[248:251],a[224:239]\n" \
qL "v110,v110\n" qL "v111,v111\n" qJ "v252,v104,v105\n" qJ "v253,v106,v107\n" \
qJ "v254,v108,v109\n" qJ "v255,v110,v111\n" "v_and_b32 v252,s60,v252\n" "v_and_b32 v253,s60,v253\n" \
"v_and_b32 v254,s60,v254\n" "v_and_b32 v255,s60,v255\n" qL "v48,v48\n" qL "v49,v49\n" \
qB "a[192:207],v[8:11],v[252:255],a[192:207]\n" qL "v50,v50\n" qL "v51,v51\n" qL "v52,v52\n" \
qL "v53,v53\n" qB "a[224:239],a[132:135],v[252:255],a[224:239]\n" qL "v54,v54\n" qL "v55,v55\n" \
qJ "v32,v48,v49\n" qJ "v33,v50,v51\n" qJ "v34,v52,v53\n" qJ "v35,v54,v55\n" \
"v_and_b32 v32,s61,v32\n" "v_and_b32 v33,s61,v33\n" "v_and_b32 v34,s61,v34\n" "v_and_b32 v35,s61,v35\n" \
qL "v56,v56\n" qL "v57,v57\n" qB "a[192:207],v[8:11],v[32:35],a[192:207]\n" qL "v58,v58\n" \
qL "v59,v59\n" qL "v60,v60\n" qL "v61,v61\n" qB "a[224:239],a[136:139],v[32:35],a[224:239]\n" \
qL "v62,v62\n" qL "v63,v63\n" qJ "v36,v56,v57\n" qJ "v37,v58,v59\n" \
qJ "v38,v60,v61\n" qJ "v39,v62,v63\n" "v_and_b32 v36,s61,v36\n" "v_and_b32 v37,s61,v37\n" \
"v_and_b32 v38,s61,v38\n" "v_and_b32 v39,s61,v39\n" qL "v64,v64\n" qL "v65,v65\n" \
qB "a[192:207],v[8:11],v[36:39],a[192:207]\n" qL "v66,v66\n" qL "v67,v67\n" qL "v68,v68\n" \
qL "v69,v69\n" qB "a[224:239],a[140:143],v[36:39],a[224:239]\n" qL "v70,v70\n" qL "v71,v71\n" \
qJ "v16,v64,v65\n" qJ "v17,v66,v67\n" qJ "v18,v68,v69\n" qJ "v19,v70,v71\n" \
"v_and_b32 v16,s62,v16\n" "v_and_b32 v17,s62,v17\n" "v_and_b32 v18,s62,v18\n" "v_and_b32 v19,s62,v19\n" \
qL "v72,v72\n" qL "v73,v73\n" qB "a[192:207],v[8:11],v[16:19],a[192:207]\n" qL "v74,v74\n" \
qL "v75,v75\n" qL "v76,v76\n" qL "v77,v77\n" qB "a[224:239],a[144:147],v[16:19],a[224:239]\n" \
qL "v78,v78\n" qL "v79,v79\n" qJ "v20,v72,v73\n" qJ "v21,v74,v75\n" \
qJ "v22,v76,v77\n" qJ "v23,v78,v79\n" "v_and_b32 v20,s62,v20\n" "v_and_b32 v21,s62,v21\n" \
"v_and_b32 v22,s62,v22\n" "v_and_b32 v23,s62,v23\n" "s_nop 1\n" qB "a[192:207],v[8:11],v[20:23],a[192:207]\n" \
qB "a[224:239],a[148:151],v[20:23],a[224:239]\n" "s_nop 7\n" "s_nop 2\n" qG "v2,a192\n" \
"v_rcp_f32 v2,v2\n" qG "v192,a224\n" qG "v193,a225\n" qG "v194,a226\n" \
qG "v195,a227\n" qG "v196,a228\n" qG "v197,a229\n" qG "v198,a230\n" \
qG "v199,a231\n" qG "v200,a232\n" qG "v201,a233\n" qG "v202,a234\n" \
qG "v203,a235\n" qG "v204,a236\n" qG "v205,a237\n" qG "v206,a238\n" \
qG "v207,a239\n" "v_mul_f32 v192,v192,v2\n" "v_mul_f32 v193,v193,v2\n" "v_mul_f32 v194,v194,v2\n" \
"v_mul_f32 v195,v195,v2\n" "v_mul_f32 v196,v196,v2\n" "v_mul_f32 v197,v197,v2\n" "v_mul_f32 v198,v198,v2\n" \
"v_mul_f32 v199,v199,v2\n" "v_mul_f32 v200,v200,v2\n" "v_mul_f32 v201,v201,v2\n" "v_mul_f32 v202,v202,v2\n" \
"v_mul_f32 v203,v203,v2\n" "v_mul_f32 v204,v204,v2\n" "v_mul_f32 v205,v205,v2\n" "v_mul_f32 v206,v206,v2\n" \
"v_mul_f32 v207,v207,v2\n" qJ "v240,v192,v193\n" qJ "v241,v194,v195\n" qJ "v242,v196,v197\n" \
qJ "v243,v198,v199\n" qJ "v248,v200,v201\n" qJ "v249,v202,v203\n" qJ "v250,v204,v205\n" \
qJ "v251,v206,v207\n" "v_permlane32_swap_b32 v240,v242\n" "v_permlane32_swap_b32 v241,v243\n" "v_permlane32_swap_b32 v248,v250\n" \
qF "v[240:243],v43,s[84:87],0 offen offset:512 sc1\n" "v_permlane32_swap_b32 v249,v251\n" qF "v[248:251],v43,s[84:87],s68 offen offset:512 sc1\n"

#define ATTN_CLOB "v1", "v2", "v8", "v9", "v10", "v11", "v12", "v13", "v14", "v15", "v16", "v17", "v18", "v19", "v20", "v21", "v22", "v23", "v24", "v25", "v26", "v27", "v28", "v29", "v30", "v31", "v32", "v33", "v34", "v35", "v36", "v37", "v38", "v39", "v48", "v49", "v50", "v51", "v52", "v53", "v54", "v55", "v56", "v57", "v58", "v59", "v60", "v61", "v62", "v63", "v64", "v65", "v66", "v67", "v68", "v69", "v70", "v71", "v72", "v73", "v74", "v75", "v76", "v77", "v78", "v79", "v80", "v81", "v82", "v83", "v84", "v85", "v86", "v87", "v88", "v89", "v90", "v91", "v92", "v93", "v94", "v95", "v96", "v97", "v98", "v99", "v100", "v101", "v102", "v103", "v104", "v105", "v106", "v107", "v108", "v109", "v110", "v111", "v112", "v113", "v114", "v115", "v116", "v117", "v118", "v119", "v120", "v121", "v122", "v123", "v124", "v125", "v126", "v127", "v192", "v193", "v194", "v195", "v196", "v197", "v198", "v199", "v200", "v201", "v202", "v203", "v204", "v205", "v206", "v207", "v208", "v209", "v210", "v211", "v212", "v213", "v214", "v215", "v216", "v217", "v218", "v219", "v220", "v221", "v222", "v223", "v224", "v225", "v226", "v227", "v228", "v229", "v230", "v231", "v232", "v233", "v234", "v235", "v236", "v237", "v238", "v239", "v240", "v241", "v242", "v243", "v244", "v245", "v246", "v247", "v248", "v249", "v250", "v251", "v252", "v253", "v254", "v255", "a0", "a1", "a2", "a3", "a4", "a5", "a6", "a7", "a8", "a9", "a10", "a11", "a12", "a13", "a14", "a15", "a16", "a17", "a18", "a19", "a20", "a21", "a22", "a23", "a24", "a25", "a26", "a27", "a28", "a29", "a30", "a31", "a32", "a33", "a34", "a35", "a36", "a37", "a38", "a39", "a40", "a41", "a42", "a43", "a44", "a45", "a46", "a47", "a48", "a49", "a50", "a51", "a52", "a53", "a54", "a55", "a56", "a57", "a58", "a59", "a60", "a61", "a62", "a63", "a64", "a65", "a66", "a67", "a68", "a69", "a70", "a71", "a72", "a73", "a74", "a75", "a76", "a77", "a78", "a79", "a80", "a81", "a82", "a83", "a84", "a85", "a86", "a87", "a88", "a89", "a90", "a91", "a92", "a93", "a94", "a95", "a96", "a97", "a98", "a99", "a100", "a101", "a102", "a103", "a104", "a105", "a106", "a107", "a108", "a109", "a110", "a111", "a112", "a113", "a114", "a115", "a116", "a117", "a118", "a119", "a120", "a121", "a122", "a123", "a124", "a125", "a126", "a127", "a128", "a129", "a130", "a131", "a132", "a133", "a134", "a135", "a136", "a137", "a138", "a139", "a140", "a141", "a142", "a143", "a144", "a145", "a146", "a147", "a148", "a149", "a150", "a151", "a152", "a153", "a154", "a155", "a156", "a157", "a158", "a159", "a160", "a161", "a162", "a163", "a164", "a165", "a166", "a167", "a168", "a169", "a170", "a171", "a172", "a173", "a174", "a175", "a176", "a177", "a178", "a179", "a180", "a181", "a182", "a183", "a184", "a185", "a186", "a187", "a188", "a189", "a190", "a191", "a192", "a193", "a194", "a195", "a196", "a197", "a198", "a199", "a200", "a201", "a202", "a203", "a204", "a205", "a206", "a207", "a208", "a209", "a210", "a211", "a212", "a213", "a214", "a215", "a216", "a217", "a218", "a219", "a220", "a221", "a222", "a223", "a224", "a225", "a226", "a227", "a228", "a229", "a230", "a231", "a232", "a233", "a234", "a235", "a236", "a237", "a238", "a239", "s40", "s41", "s42", "s43", "s44", "s45", "s64", "s65", "s66", "s67", "s70", "s71", "s72", "s73", "s76", "s77", "s78", "s79", "s80", "s81", "s82", "s83", "s88", "s89", "s90", "s91", "s92", "s93", "s94", "s95", "s98", "s99", "memory"

typedef unsigned short u16;
typedef int      i32x4  __attribute__((ext_vector_type(4)));
typedef unsigned u32x4  __attribute__((ext_vector_type(4)));
typedef unsigned u32x2  __attribute__((ext_vector_type(2)));
typedef float    f32x2  __attribute__((ext_vector_type(2)));
typedef float    f32x4  __attribute__((ext_vector_type(4)));
typedef float    f32x32 __attribute__((ext_vector_type(32)));
typedef short    s16x4  __attribute__((ext_vector_type(4)));
typedef __bf16   bf16x2 __attribute__((ext_vector_type(2)));
#define LAS __attribute__((address_space(3)))
#define GAS __attribute__((address_space(1)))
#define CAS __attribute__((address_space(4)))
#define DI __device__ __forceinline__
#define RFL(x) __builtin_amdgcn_readfirstlane(x)
#define RLX_AGENT __ATOMIC_RELAXED, __HIP_MEMORY_SCOPE_AGENT

#define HAUX 16
#define OAUX 0
constexpr size_t WS_EPOCH = 0;
constexpr size_t WS_FLAGS = 4096;
constexpr size_t WS_STATS = WS_FLAGS + 65536;
constexpr size_t WS_DONE  = WS_STATS + 32768;
constexpr size_t WS_KVF   = WS_DONE + 8192;
constexpr size_t WS_HC    = 262144;
constexpr size_t WS_KV    = WS_HC + 576ull * 2048 * 16;
constexpr size_t WS_XCH   = WS_KV + 256ull * 32768;
constexpr size_t WS_END   = WS_XCH + 128ull * 2 * 32768;
constexpr int FLAG_KV = 0, FLAG_X = 256;
constexpr int FLAG_STRIDE = 16;

DI i32x4 mk_srd(const void* p, unsigned bytes) { unsigned long long a = (unsigned long long)p; i32x4 s; s[0] = RFL((unsigned)a); s[1] = RFL((unsigned)(a >> 32)); s[2] = RFL(bytes); s[3] = 0x00020000; return s; }
DI __amdgpu_buffer_rsrc_t mk_rsrc(const void* p, unsigned bytes) {
  unsigned long long a = (unsigned long long)p; unsigned lo = RFL((unsigned)a), hi = RFL((unsigned)(a >> 32));
  return __builtin_amdgcn_make_buffer_rsrc((void*)(((unsigned long long)hi << 32) | lo), 0, bytes, 0x00020000);
}
DI int lane_now() { int l; asm volatile("v_mbcnt_lo_u32_b32 %0, -1, 0\n\tv_mbcnt_hi_u32_b32 %0, -1, %0" : "=v"(l)); return l; }
DI unsigned pk(float a, float b) { f32x2 v = {a, b}; return __builtin_bit_cast(unsigned, __builtin_convertvector(v, bf16x2)); }
DI float bflo(unsigned u) { return __uint_as_float(u << 16); }
DI float bfhi(unsigned u) { return __uint_as_float(u & 0xffff0000u); }
DI u32x4 pair16(f32x4 f0, f32x4 f1) {
  unsigned d0 = pk(f0[0], f0[1]), d1 = pk(f0[2], f0[3]), e0 = pk(f1[0], f1[1]), e1 = pk(f1[2], f1[3]);
  auto r0 = __builtin_amdgcn_permlane16_swap(d0, e0, false, false);
  auto r1 = __builtin_amdgcn_permlane16_swap(d1, e1, false, false);
  u32x4 o = {r0[0], r1[0], r0[1], r1[1]};
  return o;
}
DI unsigned long long flag_value(unsigned tag) { return ((unsigned long long)tag << 32) | (tag ^ 0x9e3779b9u); }
DI void wait_flag(GAS unsigned long long* flag, unsigned long long want) {
  for (unsigned spins = 0; spins < (1u << 22); ++spins) {
    const unsigned long long v = __hip_atomic_load(flag, RLX_AGENT);
    if ((unsigned)RFL((unsigned)v) == (unsigned)want && (unsigned)RFL((unsigned)(v >> 32)) == (unsigned)(want >> 32)) return;
    __builtin_amdgcn_s_sleep(1);
  }
}
template <int CTRL> DI float dpp_add(float v) { return v + __builtin_bit_cast(float, __builtin_amdgcn_update_dpp(0, __builtin_bit_cast(int, v), CTRL, 0xf, 0xf, true)); }
DI float wave_sum(float v) {
  v = dpp_add<0xb1>(v); v = dpp_add<0x4e>(v); v = dpp_add<0x141>(v); v = dpp_add<0x140>(v);
  auto a = __builtin_amdgcn_permlane16_swap(__float_as_uint(v), __float_as_uint(v), false, false);
  v = __uint_as_float(a[0]) + __uint_as_float(a[1]);
  auto b = __builtin_amdgcn_permlane32_swap(__float_as_uint(v), __float_as_uint(v), false, false);
  return __uint_as_float(b[0]) + __uint_as_float(b[1]);
}
DI f32x2 row_stats(u32x4 a, u32x4 b) {
  float v[16];
#pragma unroll
  for (int i = 0; i < 4; i++) { v[2 * i] = bflo(a[i]); v[2 * i + 1] = bfhi(a[i]); v[8 + 2 * i] = bflo(b[i]); v[8 + 2 * i + 1] = bfhi(b[i]); }
  float s = 0.f;
#pragma unroll
  for (int i = 0; i < 16; i++) s += v[i];
  const float m = wave_sum(s) * (1.f / 1024.f);
  float q = 0.f;
#pragma unroll
  for (int i = 0; i < 16; i++) { const float d = v[i] - m; q += d * d; }
  const float var = wave_sum(q) * (1.f / 1024.f);
  f32x2 r = {m, 1.f / sqrtf(var + 1e-5f)};
  return r;
}

struct AccG1 { f32x32 v[10]; };
constexpr unsigned L_Q = 0, L_K = 16384, L_V = 49152;

constexpr unsigned L_CS = 147456;
constexpr unsigned L_PF = 149504;
constexpr unsigned L_ST = L_PF + 4096, LDS_K1 = L_ST + 4096;
typedef unsigned u32x8 __attribute__((ext_vector_type(8)));
typedef unsigned u32x16 __attribute__((ext_vector_type(16)));
typedef unsigned u32x32 __attribute__((ext_vector_type(32)));
typedef float    f32x16 __attribute__((ext_vector_type(16)));

__global__ __launch_bounds__(256, 1) void k1_gemm_attn(const u16* x_, const u16* wqkv_, const u16* wg_, const u16* wu_, const u16* wo_, const u16* wd_, char* ws_, u16* out_) {
  extern __shared__ __attribute__((aligned(128))) char smem[];
  const int w = RFL(threadIdx.x >> 6);
  const int bid = blockIdx.x, xcd = bid & 7, idx = bid >> 3;
  const int rb = idx & 7, c = xcd * 4 + (idx >> 3);
  const int wm = w >> 1, wn = w & 1;
  const unsigned lds0 = (unsigned)(size_t)smem;
  const int R0 = rb * 256;
  unsigned tag; u32x32 H0, H1; u32x8 voffs; i32x4 srdS;
  {
    const u32x16 args = *(const CAS u32x16*)__builtin_amdgcn_kernarg_segment_ptr();
#define ARG(i) ((char*)(((unsigned long long)args[2 * (i) + 1] << 32) | args[2 * (i)]))
    const char* x = ARG(0); const char* wqkv = ARG(1); const char* wg = ARG(2); const char* wu = ARG(3);
    const char* wo = ARG(4); const char* wd = ARG(5); char* ws = ARG(6);
#undef ARG
    const int l = threadIdx.x & 63;
    i32x4 srdX = mk_srd(x + (size_t)(R0 + 64 * w) * 2048, 64 * 2048);
    const int srow = R0 + 8 * c + 2 * w;
    const i32x4 srdR = mk_srd(x + (size_t)srow * 2048, 2 * 2048);
    const unsigned vx = (l >> 3) * 2048 + (((l & 7) ^ (l >> 3)) * 16);
    const unsigned sX = RFL(lds0 + w * 8192);
    u32x16 rows; unsigned epoch;
    asm volatile(G1_FIRST
      : "={v[48:63]}"(rows), "={v100}"(epoch), "+{s[40:43]}"(srdX)
      : "{s61}"(sX), "{s65}"(16384), "{s66}"(32768), "{s67}"(49152), "{s68}"(65536), "{s69}"(81920), "{s70}"(98304), "{s71}"(114688),
        "{s[76:79]}"(srdR), "{s[80:81]}"(ws + WS_EPOCH), "{v40}"(vx), "{v47}"(l * 16),
        "{s73}"(RFL(lds0 + L_PF)), "{s74}"(RFL((idx * 4 + w) * 1024))
      : "scc", "s92", "s93", "s94", "s95", "memory");
    __builtin_amdgcn_sched_barrier(0);
    const int fullb = 224 * c + ((c & 1) ? 32 : 0), halfb = 224 * c + ((c & 1) ? 0 : 192);
    i32x4 srdA = mk_srd((const char*)(w < 2 ? wg : wu) + fullb, 1024 * 7168 - fullb);
    i32x4 srdB = mk_srd((const char*)wqkv + (w < 2 ? 0 : 2048) + 64 * c, 1024 * 6144 - (w < 2 ? 0 : 2048) - 64 * c);
    i32x4 srdC = mk_srd(w < 2 ? (const char*)(w == 0 ? wg : wu) + halfb : (const char*)wqkv + 4096 + 64 * c, w < 2 ? 1024 * 7168 - halfb : 1024 * 6144 - 4096 - 64 * c);
    const int rh = w & 1;
    const unsigned sw = ((l & 3) ^ (2 * (l >> 5))) * 16;
    const unsigned vA = (rh * 16 + (l >> 2)) * 7168 + sw, vB = (rh * 16 + (l >> 2)) * 6144 + sw;
    const int rho = l >> 1, kk = (rho & 3) | ((rho & 4) << 1) | ((rho & 8) >> 1) | (rho & 16);
    const unsigned vC = w < 2 ? kk * 7168 + (l & 1) * 16 : vB;
    const int g = l >> 4, q = (l & 15) >> 2, p = l & 3;
    const unsigned A0 = lds0 + 65536 + (8 * g + q) * 64 + (g & 1) * 32 + p * 8, A1 = A0 ^ 32, A2 = lds0 + 65536 + (q + 4 * (g & 1) + 16 * (g >> 1)) * 32 + p * 8;
    const unsigned B0 = lds0 + wm * 16384 + (l & 15) * 128 + ((g ^ (l & 7)) * 16), B1 = B0 ^ 64;
    const unsigned sW = RFL(lds0 + 65536 + (w >> 1) * 10240 + (w & 1) * 1024), incC = RFL(w < 2 ? 32 * 7168 : 32 * 6144), maxC = 31 * incC;
    unsigned so0 = 0, so1 = 0, so2 = 0;
    srdS = mk_srd(ws + WS_STATS + (size_t)R0 * 16, (unsigned)(WS_END - WS_STATS) - R0 * 16);
    const char* pfd = (const char*)wd + (size_t)bid * 28672 + w * 7168;
    const char* pfo = (const char*)wo + (size_t)bid * 8192 + w * 2048;
    u32x32 st, cs32;
#pragma unroll
    for (int i = 0; i < 16; i++) st[i] = rows[i];
    voffs = (u32x8){vx, vA, vB, vC, (unsigned)(l * 16 + 4096), (unsigned)((8 * c + 2 * w + (l & 1)) * 16), (unsigned)((64 * w + l) * 16), (unsigned)(l * 16)};
    const unsigned sbase = (unsigned)WS_STATS + R0 * 16, tile = rb * 32 + c;
    const unsigned inbox = (unsigned)WS_XCH + (((((xcd & 3) * 4 + (idx & 3)) * 8 + (idx >> 2)) * 2 + (xcd >> 2)) * 4 + w) * 4096 - sbase;
    const unsigned kvown = (unsigned)WS_KV + tile * 32768 - sbase;
    const unsigned kvf_own = (unsigned)WS_KVF + tile * 128 + w * 8 - sbase, kvf_prev = (unsigned)WS_KVF + (rb > 0 ? tile - 32 : tile) * 128 - sbase;
    const int lc0 = (c & 1) ? 2 : 0, lc6 = (c & 1) ? 0 : 12;
    const unsigned hrow0 = (unsigned)WS_HC + ((14 * c + lc0 + 8 * wn) * 2048 + R0 + wm * 128) * 16 - sbase;
    const unsigned hrow1 = (unsigned)WS_HC + ((14 * c + lc6) * 2048 + R0 + wm * 128) * 16 - sbase;
    const unsigned orow = (unsigned)WS_HC + ((448 + 4 * c) * 2048 + R0 + 64 * w) * 16 - sbase;
    asm volatile(G1_ALL
      : "+{s[40:43]}"(srdX), "+{s56}"(so0), "+{s57}"(so1), "+{s58}"(so2), "+{v[48:79]}"(st), "={v[8:39]}"(cs32), "={s75}"(tag), "+{v100}"(epoch),
        "={v[128:159]}"(H0), "={v[160:191]}"(H1), "+{v[40:47]}"(voffs)
      : "{s72}"(maxC), "{s[44:47]}"(srdA), "{s[48:51]}"(srdB), "{s[52:55]}"(srdC), "{s59}"(w | (rb == 0 ? 512 : 0)),
        "{s60}"(incC), "{s61}"(sX), "{s62}"(sW),
        "{s64}"(RFL(lds0 + L_ST + w * 1024)),
        "{s65}"(16384), "{s66}"(32768), "{s67}"(49152), "{s68}"(65536), "{s69}"(81920), "{s70}"(98304), "{s71}"(114688),
        "{s73}"(RFL(lds0 + L_PF)), "{s76}"(RFL(kvown)), "{s77}"(RFL(kvf_own)), "{s78}"(RFL(kvf_prev)), "{s79}"(RFL(hrow0)), "{s80}"(RFL(hrow1)), "{s81}"(RFL(orow)),
        "{s[82:83]}"(pfo), "{s[84:87]}"(srdS), "{s90}"(RFL(inbox)), "{s[100:101]}"(pfd),
        "{v7}"(lds0 + L_ST + (128 * wm + (l & 15)) * 16),
        "{v32}"(A0), "{v33}"(A1), "{v34}"(A2), "{v35}"(A0 + 40960), "{v36}"(A1 + 40960), "{v37}"(A2 + 40960), "{v38}"(B0), "{v39}"(B1)
      : G1_CLOB);
  }
  const CAS char* ka = (const CAS char*)__builtin_amdgcn_kernarg_segment_ptr();
  asm volatile("" : "+s"(ka));
  const u16* x = *(const u16* const CAS*)(ka + 0);
  const u16* wo = *(const u16* const CAS*)(ka + 32);
  const u16* wd = *(const u16* const CAS*)(ka + 40);
  char* ws = *(char* const CAS*)(ka + 48);
  u16* out = *(u16* const CAS*)(ka + 56);
  const unsigned long long fv = flag_value(tag);
  if (rb == 0) {
    const unsigned NI = 0xff800000u;
    asm volatile(ATTN_FIRST ::
      "{v[40:47]}"(voffs), "{s[84:87]}"(srdS), "{s68}"(65536), "{v[128:159]}"(H0), "{v[160:191]}"(H1),
      "{s46}"(RFL(2 * w + 1 < 8 ? NI : 0u)), "{s47}"(RFL(2 * w + 2 < 8 ? NI : 0u)), "{s48}"(RFL(2 * w + 3 < 8 ? NI : 0u)), "{s49}"(RFL(2 * w + 4 < 8 ? NI : 0u)),
      "{s50}"(RFL(2 * w + 5 < 8 ? NI : 0u)), "{s51}"(RFL(2 * w + 6 < 8 ? NI : 0u)), "{s52}"(RFL(2 * w + 7 < 8 ? NI : 0u)), "{s53}"(0u),
      "{s55}"(RFL(2 * w + 1 < 8 ? 0u : ~0u)), "{s56}"(RFL(2 * w + 2 < 8 ? 0u : ~0u)), "{s57}"(RFL(2 * w + 3 < 8 ? 0u : ~0u)), "{s58}"(RFL(2 * w + 4 < 8 ? 0u : ~0u)),
      "{s59}"(RFL(2 * w + 5 < 8 ? 0u : ~0u)), "{s60}"(RFL(2 * w + 6 < 8 ? 0u : ~0u)), "{s61}"(RFL(2 * w + 7 < 8 ? 0u : ~0u)), "{s62}"(~0u)
      : ATTN_CLOB);
  } else {
    asm volatile(ATTN_MAIN ::
      "{v[40:47]}"(voffs), "{s[84:87]}"(srdS), "{s68}"(65536), "{v[128:159]}"(H0), "{v[160:191]}"(H1)
      : ATTN_CLOB);
  }
  if (bid == 0 && w == 3) {
    __amdgpu_buffer_rsrc_t sr = mk_rsrc(ws + WS_STATS, 2048 * 16);
    const int le = lane_now();
    for (unsigned spins = 0; spins < (1u << 20); ++spins) {
      bool ok = true;
#pragma unroll
      for (int j = 0; j < 4; j++) ok = ok && __builtin_amdgcn_raw_buffer_load_b32(sr, (le + 64 * j) * 128, 0, 16) == tag;
      if (__all(ok)) { if (le == 0) __hip_atomic_store((GAS unsigned*)(ws + WS_EPOCH), tag, RLX_AGENT); break; }
      __builtin_amdgcn_s_sleep(1);
    }
  }
  asm volatile("s_waitcnt vmcnt(0)" ::: "memory");
  GAS unsigned long long* done = (GAS unsigned long long*)(ws + WS_DONE);
  if (lane_now() == 0) __hip_atomic_store(done + ((2 * rb + wm) * 32 + c) * 2 + wn, fv, RLX_AGENT);
  __builtin_amdgcn_s_barrier();
  asm volatile("" ::: "memory");
  {
    const int l = lane_now();
    const int kh = xcd >> 2, rt = (xcd & 3) * 4 + (idx & 3), ct = idx >> 2;
    const int T0 = rt * 128, C0 = ct * 128;
    const int t = l & 15, g = l >> 4;
    f32x32 a0, a1;
    const int ch = 2 * (g & 1) + (g >> 1);
    const size_t obase = (size_t)(T0 + 64 * wm + 32 * kh + t) * 1024 + C0 + 64 * wn + 8 * ch;
    u32x4 xres[2][2];
    {
      i32x4 srdA = mk_srd(ws + WS_HC + ((size_t)(kh * 288 + 2 * w) * 2048 + T0) * 16, (576 * 2048 - (kh * 288 + 2 * w) * 2048 - T0) * 16);
      const int cw = w & 1;
      const char* wbase = (const char*)wd + ((size_t)kh * 2304 + 32 * (w >> 1)) * 2048 + ct * 256 + cw * 128;
      i32x4 srdW = mk_srd(wbase, 0x7ffffff0);
      const char* wo0 = (const char*)wo + (size_t)(32 * (w >> 1)) * 2048 + ct * 256 + cw * 128;
      const unsigned long long delta = (unsigned long long)wo0 - ((unsigned long long)wbase + 19ull * 64 * 2048);
      const unsigned dlo = RFL((unsigned)delta), dhi = RFL((unsigned)(delta >> 32));
      const unsigned sw = kh ? 20u : 0xffffu;
      const int q = (l & 15) >> 2, p = l & 3;
      const int f = ((q >> 1) & 1) | ((g & 1) << 1);
      const unsigned wb = lds0 + 16384 + wn * 8192 + (8 * g + q) * 128 + p * 8;
      const unsigned W0 = wb + ((0 ^ f) << 5), W1 = wb + ((1 ^ f) << 5), W2 = wb + ((2 ^ f) << 5), W3 = wb + ((3 ^ f) << 5);
      const unsigned XB = lds0 + g * 2048 + (64 * wm + t) * 16;
      const int pos = l & 7, rr = l >> 3;
      const int f0 = (rr >> 1) & 1, f1 = f0 | 2;
      const unsigned vW0 = rr * 2048 + (((((pos >> 1) ^ f0) << 1) | (pos & 1)) * 16), vW1 = rr * 2048 + (((((pos >> 1) ^ f1) << 1) | (pos & 1)) * 16);
      const unsigned sA = RFL(lds0 + w * 4096), sW = RFL(lds0 + 16384 + cw * 8192 + (w >> 1) * 4096);
      unsigned so0 = 0, so1 = 32768, cnt = 0;
      asm volatile(G3_PRO
        : "+{s[44:47]}"(srdW), "+{s58}"(cnt)
        : "{s48}"(dlo), "{s49}"(dhi), "{s59}"(sw), "{s62}"(sW), "{s65}"(16384), "{s66}"(32768), "{s67}"(49152), "{v43}"(vW0), "{v44}"(vW1)
        : "s50", "s51", "scc", "memory");
      {
        GAS unsigned long long* pf = done + rt * 64 + l;
        GAS unsigned long long* pq = done + ((2 * rb + wm) * 32 + (c ^ 16)) * 2 + wn;
        for (unsigned spins = 0; spins < (1u << 22); ++spins) {
          const unsigned long long a = __hip_atomic_load(pf, RLX_AGENT), b = __hip_atomic_load(pq, RLX_AGENT);
          if (__all((a == fv) & (b == fv))) break;
          __builtin_amdgcn_s_sleep(1);
        }
      }
      asm volatile("" ::: "memory");
#pragma unroll
      for (int m = 0; m < 2; m++)
#pragma unroll
        for (int j = 0; j < 2; j++) xres[m][j] = *(const u32x4*)(x + obase + m * 16 * 1024 + j * 32);
      asm volatile(G3_ASM
        : "={a[0:31]}"(a0), "={a[32:63]}"(a1), "+{s[44:47]}"(srdW), "+{s56}"(so0), "+{s57}"(so1), "+{s58}"(cnt)
        : "{s[40:43]}"(srdA), "{s48}"(dlo), "{s49}"(dhi), "{s59}"(sw), "{s61}"(sA), "{s62}"(sW), "{s65}"(16384), "{s66}"(32768), "{s67}"(49152),
          "{v32}"(W0), "{v33}"(W1), "{v34}"(W2), "{v35}"(W3), "{v36}"(W0 + 65536), "{v37}"(W1 + 65536), "{v38}"(W2 + 65536), "{v39}"(W3 + 65536),
          "{v40}"(XB), "{v41}"(XB + 65536), "{v42}"(l * 16), "{v43}"(vW0), "{v44}"(vW1)
        : G3_CLOB);
    }
    const int le = lane_now();
    const int tile2 = rt * 8 + ct;
    __amdgpu_buffer_rsrc_t xr = mk_rsrc(ws + WS_XCH, (unsigned)(WS_END - WS_XCH));
    const f32x32 mine = kh ? a1 : a0, send = kh ? a0 : a1;
    const unsigned so = ((tile2 * 2 + (kh ^ 1)) * 4 + w) * 4096 + le * 16, ro = ((tile2 * 2 + kh) * 4 + w) * 4096 + le * 16;
#pragma unroll
    for (int i = 0; i < 4; i++) { u32x4 v = {pk(send[8 * i], send[8 * i + 1]), pk(send[8 * i + 2], send[8 * i + 3]), pk(send[8 * i + 4], send[8 * i + 5]), pk(send[8 * i + 6], send[8 * i + 7])};
      __builtin_amdgcn_raw_buffer_store_b128(v, xr, so + i * 1024, 0, 16); }
    u32x4 in[4];
    for (unsigned spins = 0; spins < (1u << 16); ++spins) {
      bool ok = true;
#pragma unroll
      for (int i = 0; i < 4; i++) in[i] = __builtin_amdgcn_raw_buffer_load_b128(xr, ro + i * 1024, 0, 16);
#pragma unroll
      for (int i = 0; i < 4; i++) ok = ok & (in[i][0] != ~0u) & (in[i][1] != ~0u) & (in[i][2] != ~0u) & (in[i][3] != ~0u);
      if (__all(ok)) break;
      __builtin_amdgcn_s_sleep(2);
    }
    float part[32];
    __amdgpu_buffer_rsrc_t outr = mk_rsrc(out, 2048 * 1024 * 2);
#pragma unroll
    for (int i = 0; i < 4; i++)
#pragma unroll
      for (int r = 0; r < 4; r++) { part[8 * i + 2 * r] = bflo(in[i][r]); part[8 * i + 2 * r + 1] = bfhi(in[i][r]); }
#pragma unroll
    for (int m = 0; m < 2; m++)
#pragma unroll
      for (int j = 0; j < 2; j++) {
        f32x4 f0, f1;
#pragma unroll
        for (int r = 0; r < 4; r++) { f0[r] = mine[16 * m + 8 * j + r] + part[16 * m + 8 * j + r]; f1[r] = mine[16 * m + 8 * j + 4 + r] + part[16 * m + 8 * j + 4 + r]; }
        float y[8];
#pragma unroll
        for (int r = 0; r < 4; r++) { auto s = __builtin_amdgcn_permlane16_swap(__float_as_uint(f0[r]), __float_as_uint(f1[r]), false, false); y[r] = __uint_as_float(s[0]); y[4 + r] = __uint_as_float(s[1]); }
        const u32x4 xv = xres[m][j];
        u32x4 o;
#pragma unroll
        for (int r = 0; r < 4; r++) o[r] = pk(y[2 * r] + bflo(xv[r]), y[2 * r + 1] + bfhi(xv[r]));
        __builtin_amdgcn_raw_buffer_store_b128(o, outr, (unsigned)(obase * 2) + m * 32768 + j * 64, 0, OAUX);
      }
  }
  asm volatile("K2_CODE_END:");
}

extern "C" void kernel_launch(void* const* d_in, const int* in_sizes, int n_in, void* d_out, int out_size, void* d_ws, size_t ws_size, hipStream_t stream) {
  const u16* x = (const u16*)d_in[0]; const u16* wqkv = (const u16*)d_in[1]; const u16* wo = (const u16*)d_in[2];
  const u16* wg = (const u16*)d_in[4]; const u16* wu = (const u16*)d_in[5]; const u16* wd = (const u16*)d_in[6];
  char* ws = (char*)d_ws;
  (void)hipFuncSetAttribute((const void*)k1_gemm_attn, hipFuncAttributeMaxDynamicSharedMemorySize, LDS_K1);
  k1_gemm_attn<<<256, 256, LDS_K1, stream>>>(x, wqkv, wg, wu, wo, wd, ws, (u16*)d_out);
}
```
